```python
import torch

def _softmax(x, axis=-1):
    return torch.softmax(x, dim=axis)
def _gelu(x):
    return torch.nn.functional.gelu(x, approximate="tanh")
def _silu(x):
    return torch.nn.functional.silu(x)
def _sigmoid(x):
    return torch.sigmoid(x)
def _layernorm(x, axis=-1, eps=1e-5):
    m = x.mean(dim=axis, keepdim=True)
    v = x.var(dim=axis, keepdim=True, unbiased=False)
    return (x - m) / torch.sqrt(v + eps)
def _scatter_add(tgt, idx, src, axis=0):
    return tgt.clone().index_add_(axis, idx, src)
def _segment_sum(src, seg, n):
    out = torch.zeros((n,) + src.shape[1:], dtype=src.dtype)
    return out.index_add_(0, seg, src)
def _pad(x, pw):
    flat = tuple(v for pair in reversed(pw) for v in pair)
    return torch.nn.functional.pad(x, flat)

kernel_name = 'concat_heads__gqa_block_cached__mha_block_cached__near_ridge'

def setup_inputs():

    _w = (lambda a: (lambda t: t.to(torch.bfloat16) if t.is_floating_point() else t)(torch.as_tensor(a)))
    def _A_setup():
        B, Sq, D = 32, 48, 512
        Hq, Hkv, Sk, Dh = 32, 8, 512, 128
        assert Hq % Hkv == 0, (Hq, Hkv)
        return {
            "x": _w((lambda *s: torch.randn(*s, dtype=torch.bfloat16) * 0.8)(B, Sq, D)),
            "k_cache": _w((lambda *s: torch.randn(*s, dtype=torch.bfloat16) * 0.8)(B, Hkv, Sk, Dh)),
            "v_cache": _w((lambda *s: torch.randn(*s, dtype=torch.bfloat16) * 0.05)(B, Hkv, Sk, Dh)),
            "wq": _w((lambda *s: torch.randn(*s, dtype=torch.bfloat16) * 0.8)(D, Hq * Dh) * (D ** -0.5)),
            "wo": _w((lambda *s: torch.randn(*s, dtype=torch.bfloat16) * 0.05)(Hq * Dh, D) * ((Hq * Dh) ** -0.5)),
        }
    _A_in = _A_setup()

    def _B_setup():
        B, Sq, D = 32, 48, 512
        H, Sk, Dh = 32, 512, 128
        return {
            "x": _w((lambda *s: torch.randn(*s, dtype=torch.bfloat16) * 0.8)(B, Sq, D)),
            "k_cache": _w((lambda *s: torch.randn(*s, dtype=torch.bfloat16) * 0.8)(B, H, Sk, Dh)),
            "v_cache": _w((lambda *s: torch.randn(*s, dtype=torch.bfloat16) * 0.05)(B, H, Sk, Dh)),
            "wq": _w((lambda *s: torch.randn(*s, dtype=torch.bfloat16) * 0.8)(D, H * Dh) * (D ** -0.5)),
            "wo": _w((lambda *s: torch.randn(*s, dtype=torch.bfloat16) * 0.05)(H * Dh, D) * ((H * Dh) ** -0.5)),
        }
    _B_in = _B_setup()

    return {
        "A_x": _A_in["x"],
        "A_k_cache": _A_in["k_cache"],
        "A_v_cache": _A_in["v_cache"],
        "A_wq": _A_in["wq"],
        "A_wo": _A_in["wo"],
        "B_x": _B_in["x"],
        "B_k_cache": _B_in["k_cache"],
        "B_v_cache": _B_in["v_cache"],
        "B_wq": _B_in["wq"],
        "B_wo": _B_in["wo"],
    }

def reference(A_x, A_k_cache, A_v_cache, A_wq, A_wo, B_x, B_k_cache, B_v_cache, B_wq, B_wo):
    def _A_ref(x, k_cache, v_cache, wq, wo):
        B, Sq, D = x.shape
        Hq, Dh = 32, 128
        Hkv = k_cache.shape[1]
        rep = Hq // Hkv
        q = (lambda x, *d: x.permute(*d))((x @ wq).reshape(B, Sq, Hq, Dh), 0, 2, 1, 3)
        k = (lambda x, n, axis: torch.repeat_interleave(x, n, dim=axis))(k_cache, rep, axis=1)
        v = (lambda x, n, axis: torch.repeat_interleave(x, n, dim=axis))(v_cache, rep, axis=1)
        scores = torch.einsum("bhqd,bhkd->bhqk", q, k) * (Dh ** -0.5)
        p = _softmax(scores, axis=-1)
        o = torch.einsum("bhqk,bhkd->bhqd", p, v)
        return (lambda x, *d: x.permute(*d))(o, 0, 2, 1, 3).reshape(B, Sq, Hq * Dh) @ wo

    def _B_ref(x, k_cache, v_cache, wq, wo):
        B, Sq, D = x.shape
        H, Dh = 32, 128
        q = (lambda x, *d: x.permute(*d))((x @ wq).reshape(B, Sq, H, Dh), 0, 2, 1, 3)
        scores = torch.einsum("bhqd,bhkd->bhqk", q, k_cache) * (Dh ** -0.5)
        p = _softmax(scores, axis=-1)
        o = torch.einsum("bhqk,bhkd->bhqd", p, v_cache)
        return (lambda x, *d: x.permute(*d))(o, 0, 2, 1, 3).reshape(B, Sq, H * Dh) @ wo

    a = _A_ref(A_x, A_k_cache, A_v_cache, A_wq, A_wo)
    b = _B_ref(B_x, B_k_cache, B_v_cache, B_wq, B_wo)
    return torch.concatenate([a, b], axis=-1)
```

```cpp
#include <hip/hip_runtime.h>
#include <hip/hip_bf16.h>
typedef unsigned short u16;
__device__ __forceinline__ float b2f(u16 x){ return __uint_as_float(((unsigned)x)<<16); }
__device__ __forceinline__ u16 f2b(float f){ unsigned u=__float_as_uint(f); u += 0x7FFF + ((u>>16)&1); return u>>16; }
__global__ __launch_bounds__(256) void gemm_naive(const u16* A, const u16* B, u16* C, int M, int N, int K, int ldc){
  int n = blockIdx.x*256+threadIdx.x; int m = blockIdx.y;
  float acc=0; for(int k=0;k<K;k++) acc += b2f(A[(size_t)m*K+k])*b2f(B[(size_t)k*N+n]);
  C[(size_t)m*ldc+n]=f2b(acc);
}
__global__ __launch_bounds__(128) void attn_naive(const u16* q, const u16* K, const u16* V, u16* o, int Hkv){
  int r = blockIdx.x, h = blockIdx.y, b = blockIdx.z; int t = threadIdx.x;
  int hk = h/(32/Hkv);
  __shared__ float qs[128], s[512], red[128];
  qs[t] = b2f(q[((size_t)(b*48+r))*4096 + h*128 + t]); __syncthreads();
  const u16* Kp = K + ((size_t)(b*Hkv+hk))*512*128; const u16* Vp = V + ((size_t)(b*Hkv+hk))*512*128;
  float mx=-1e30f;
  for(int j=t;j<512;j+=128){ float a=0; for(int d=0;d<128;d++) a+=qs[d]*b2f(Kp[j*128+d]); a*=0.08838834764831845f; s[j]=a; mx=fmaxf(mx,a);}
  red[t]=mx; __syncthreads(); for(int i=0;i<128;i++) mx=fmaxf(mx,red[i]); __syncthreads();
  float sum=0; for(int j=t;j<512;j+=128){ float e=__expf(s[j]-mx); s[j]=e; sum+=e;} red[t]=sum; __syncthreads(); sum=0; for(int i=0;i<128;i++) sum+=red[i];
  float a=0; for(int j=0;j<512;j++) a+= s[j]*b2f(Vp[j*128+t]);
  o[((size_t)(b*48+r))*4096 + h*128 + t] = f2b(a/sum);
}
extern "C" void kernel_launch(void* const* d_in, const int* in_sizes, int n_in, void* d_out, int out_size, void* d_ws, size_t ws_size, hipStream_t stream){
  u16* q = (u16*)d_ws; u16* o = q + 1536*4096;
  for(int br=0;br<2;br++){
    const u16* x=(const u16*)d_in[br*5+0]; const u16* K=(const u16*)d_in[br*5+1]; const u16* V=(const u16*)d_in[br*5+2];
    const u16* wq=(const u16*)d_in[br*5+3]; const u16* wo=(const u16*)d_in[br*5+4];
    gemm_naive<<<dim3(16,1536),256,0,stream>>>(x,wq,q,1536,4096,512,4096);
    attn_naive<<<dim3(48,32,32),128,0,stream>>>(q,K,V,o,br==0?8:32);
    gemm_naive<<<dim3(2,1536),256,0,stream>>>(o,wo,(u16*)d_out+br*512,1536,512,4096,1024);
  }
}
```

```cpp
#include <hip/hip_runtime.h>
#include <hip/hip_bf16.h>
constexpr int LDS_TOTAL = 159808;

__global__ __launch_bounds__(512, 2) void fused_attn(const char* xA, const char* kA, const char* vA, const char* wqA, const char* woA,
    const char* xB, const char* kB, const char* vB, const char* wqB, const char* woB, char* part, char* out) {
  __shared__ __attribute__((aligned(16))) char smem[LDS_TOTAL];
  asm volatile("" :: "s"((unsigned)(size_t)(__attribute__((address_space(3))) char*)smem));
  auto ka = __builtin_amdgcn_kernarg_segment_ptr();
  unsigned wg = blockIdx.x, tid = threadIdx.x;
  asm volatile(
    ".Lstart:\n"
    "s_memrealtime s[86:87]\n"
    "v_mov_b32 v253, 147456\n"
    "v_mov_b64 v[4:5], 0\n"
    "v_mov_b64 v[6:7], 0\n"
    "ds_write_b128 v253, v[4:7]\n"
    "ds_write_b128 v253, v[4:7] offset:16\n"
    "s_load_dwordx16 s[4:19], %0, 0\n"
    "s_load_dwordx8 s[20:27], %0, 64\n"
    "s_mov_b32 s73, %1\n"
    "v_lshrrev_b32 v246, 6, %2\n"
    "v_and_b32 v255, 63, %2\n"
    "v_readfirstlane_b32 s44, v246\n"
    "s_and_b32 s46, s73, 7\n"
    "s_lshr_b32 s47, s73, 3\n"
    "s_and_b32 s45, s44, 3\n"
    "s_lshr_b32 s61, s44, 2\n"
    "s_cmp_lg_u32 s44, 2\n"
    "s_cbranch_scc1 .Lnopf\n"
    "s_getpc_b64 s[66:67]\n"
    ".Lpc:\n"
    "v_and_b32 v246, 15, v255\n"
    "v_lshlrev_b32 v246, 5, v246\n"
    "v_add_u32 v246, s47, v246\n"
    "v_lshlrev_b32 v246, 7, v246\n"
    "v_min_u32 v246, .Lend-.Lpc-4, v246\n"
    "global_load_dword v2, v246, s[66:67]\n"
    ".Lnopf:\n"
    "v_lshrrev_b32 v248, 3, v255\n"
    "v_and_b32 v247, 6, v248\n"
    "v_and_b32 v245, 7, v255\n"
    "v_xor_b32 v247, v247, v245\n"
    "v_lshlrev_b32 v254, 4, v247\n"
    "v_lshl_add_u32 v249, v248, 10, v254\n"
    "v_mov_b32 v252, 1\n"
    "s_and_b32 s75, s47, 1\n"
    "s_waitcnt lgkmcnt(0)\n"
    "s_barrier\n"
    "s_cmp_eq_u32 s75, 0\n"
    "s_cbranch_scc1 .Lnot0\n"
    "s_cmp_eq_u32 s44, 7\n"
    "s_cbranch_scc1 .Lnot0\n"
    "s_sub_u32 s66, s44, 1\n"
    "s_cmp_gt_u32 s66, 2\n"
    "s_cbranch_scc1 .Lqt\n"
    "s_lshl_b32 s66, s66, 14\n"
    "s_mul_i32 s67, s47, 49152\n"
    "s_add_u32 s66, s66, s67\n"
    "s_sub_u32 s66, s66, 49024\n"
    "v_lshlrev_b32 v246, 8, v255\n"
    "v_add_u32 v246, s66, v246\n"
    "global_load_dword v3, v246, s[14:15]\n"
    "s_branch .Lnot0\n"
    ".Lqt:\n"
    "s_add_u32 s66, s45, s61\n"
    "s_lshl_b32 s66, s66, 4\n"
    "s_lshr_b32 s67, s47, 1\n"
    "s_add_u32 s66, s66, s67\n"
    "s_lshl_b32 s64, s66, 16\n"
    "s_lshl_b32 s65, s46, 10\n"
    "s_add_u32 s64, s64, s65\n"
    "v_lshrrev_b32 v246, 3, v255\n"
    "v_lshlrev_b32 v246, 13, v246\n"
    "v_and_b32 v247, 7, v255\n"
    "v_lshl_add_u32 v246, v247, 7, v246\n"
    "v_add_u32 v246, s64, v246\n"
    "global_load_dword v3, v246, s[20:21]\n"
    ".Lnot0:\n"
    "s_mul_i32 s64, s47, 49152\n"
    "s_cmp_eq_u32 s61, 0\n"
    "s_cselect_b64 s[40:41], s[14:15], s[4:5]\n"
    "s_add_u32 s40, s40, s64\n"
    "s_addc_u32 s41, s41, 0\n"
    "s_and_b32 s41, s41, 0xffff\n"
    "s_mov_b32 s42, 49152\n"
    "s_mov_b32 s43, 0x20000\n"
    "s_cmp_eq_u32 s61, 0\n"
    "s_cbranch_scc1 .Lxm\n"
    "s_setprio 2\n"
    "s_branch .Lxg\n"
    ".Lxm:\n"
    "s_mul_i32 s65, s45, 12288\n"
    "s_lshl_b32 s66, s45, 8\n"
    "s_add_u32 m0, s65, 98304\n"
    "s_add_u32 s67, s66, 0\n"
    "buffer_load_dwordx4 v249, s[40:43], s67 offen lds\n"
    "s_add_u32 m0, s65, 99328\n"
    "s_add_u32 s67, s66, 8192\n"
    "buffer_load_dwordx4 v249, s[40:43], s67 offen lds\n"
    "s_add_u32 m0, s65, 100352\n"
    "s_add_u32 s67, s66, 16384\n"
    "buffer_load_dwordx4 v249, s[40:43], s67 offen lds\n"
    "s_add_u32 m0, s65, 101376\n"
    "s_add_u32 s67, s66, 24576\n"
    "buffer_load_dwordx4 v249, s[40:43], s67 offen lds\n"
    "s_add_u32 m0, s65, 102400\n"
    "s_add_u32 s67, s66, 32768\n"
    "buffer_load_dwordx4 v249, s[40:43], s67 offen lds\n"
    "s_add_u32 m0, s65, 103424\n"
    "s_add_u32 s67, s66, 40960\n"
    "buffer_load_dwordx4 v249, s[40:43], s67 offen lds\n"
    "s_add_u32 m0, s65, 104448\n"
    "s_add_u32 s67, s66, 128\n"
    "buffer_load_dwordx4 v249, s[40:43], s67 offen lds\n"
    "s_add_u32 m0, s65, 105472\n"
    "s_add_u32 s67, s66, 8320\n"
    "buffer_load_dwordx4 v249, s[40:43], s67 offen lds\n"
    "s_add_u32 m0, s65, 106496\n"
    "s_add_u32 s67, s66, 16512\n"
    "buffer_load_dwordx4 v249, s[40:43], s67 offen lds\n"
    "s_add_u32 m0, s65, 107520\n"
    "s_add_u32 s67, s66, 24704\n"
    "buffer_load_dwordx4 v249, s[40:43], s67 offen lds\n"
    "s_add_u32 m0, s65, 108544\n"
    "s_add_u32 s67, s66, 32896\n"
    "buffer_load_dwordx4 v249, s[40:43], s67 offen lds\n"
    "s_add_u32 m0, s65, 109568\n"
    "s_add_u32 s67, s66, 41088\n"
    "buffer_load_dwordx4 v249, s[40:43], s67 offen lds\n"
    ".Lxg:\n"
    "s_lshl_b32 s64, s47, 10\n"
    "s_add_u32 s64, s64, 33554432\n"
    "s_add_u32 s84, s24, s64\n"
    "s_addc_u32 s85, s25, 0\n"
    "s_xor_b32 s79, s46, 4\n"
    "s_mul_i32 s48, s45, 24576\n"
    "s_lshl_b32 s51, s45, 11\n"
    "s_mov_b32 s55, 0\n"
    "s_mov_b32 s56, 0\n"
    "s_mov_b32 s59, 0x05040100\n"
    "s_mov_b32 s60, 0x07060302\n"
    "v_lshrrev_b32 v247, 4, v255\n"
    "v_xor_b32 v245, v247, v245\n"
    "v_lshlrev_b32 v245, 4, v245\n"
    "v_lshrrev_b32 v246, 5, v255\n"
    "v_mul_u32_u24 v246, 32768, v246\n"
    "v_and_b32 v8, 1, v248\n"
    "v_mul_u32_u24 v8, 256, v8\n"
    "v_bfe_u32 v9, v248, 1, 1\n"
    "v_mul_u32_u24 v9, 8192, v9\n"
    "v_add3_u32 v246, v246, v8, v9\n"
    "v_add_u32 v245, v245, v246\n"
    "v_add_u32 v254, v254, v246\n"
    "s_mul_i32 s64, s47, 13\n"
    "s_mul_i32 s65, s46, 4\n"
    "s_add_u32 s64, s64, s65\n"
    "s_mul_i32 s65, s45, 0\n"
    "s_add_u32 s64, s64, s65\n"
    "s_add_u32 s64, s64, 1\n"
    "s_mul_i32 s66, s47, 1\n"
    "s_mul_i32 s65, s79, 0\n"
    "s_add_u32 s66, s66, s65\n"
    "s_add_u32 s66, s66, 3\n"
    "s_and_b32 s67, s45, 1\n"
    "s_mul_i32 s67, s67, 65536\n"
    "s_lshr_b32 s65, s45, 1\n"
    "s_mul_i32 s65, s65, 4096\n"
    "s_add_u32 s67, s67, s65\n"
    "s_cmp_eq_u32 s61, 0\n"
    "s_cselect_b32 s64, s64, s66\n"
    "s_cselect_b32 s67, 0, s67\n"
    "v_add_u32 v246, s64, v255\n"
    "v_mov_b32 v244, s67\n"
    "v_bfe_u32 v8, v246, 0, 1\n"
    "v_mul_u32_u24 v8, 512, v8\n"
    "v_add_u32 v244, v244, v8\n"
    "v_bfe_u32 v8, v246, 1, 1\n"
    "v_mul_u32_u24 v8, 16384, v8\n"
    "v_add_u32 v244, v244, v8\n"
    "v_bfe_u32 v8, v246, 2, 1\n"
    "v_mul_u32_u24 v8, 2048, v8\n"
    "v_add_u32 v244, v244, v8\n"
    "v_bfe_u32 v8, v246, 3, 1\n"
    "v_mul_u32_u24 v8, 1024, v8\n"
    "v_add_u32 v244, v244, v8\n"
    "s_lshl_b32 s66, s47, 3\n"
    "s_add_u32 s66, s66, s79\n"
    "s_lshl_b32 s67, s46, 2\n"
    "s_add_u32 s67, s67, s45\n"
    "s_lshl_b32 s78, s47, 5\n"
    "s_add_u32 s78, s78, s67\n"
    "s_lshl_b32 s65, s79, 2\n"
    "s_add_u32 s65, s65, s45\n"
    "s_cmp_eq_u32 s61, 0\n"
    "s_cselect_b32 s65, s67, s65\n"
    "s_cselect_b32 s64, s78, s66\n"
    "s_cselect_b64 s[32:33], s[16:17], s[6:7]\n"
    "s_cselect_b64 s[36:37], s[18:19], s[8:9]\n"
    "s_cselect_b64 s[28:29], s[20:21], s[10:11]\n"
    "s_lshl_b32 s64, s64, 17\n"
    "s_lshl_b32 s78, s78, 17\n"
    "s_lshl_b32 s65, s65, 8\n"
    "s_add_u32 s32, s32, s64\n"
    "s_addc_u32 s33, s33, 0\n"
    "s_sub_u32 s32, s32, 3072\n"
    "s_subb_u32 s33, s33, 0\n"
    "s_and_b32 s33, s33, 0xffff\n"
    "s_mov_b32 s34, 134144\n"
    "s_mov_b32 s35, 0x20000\n"
    "s_add_u32 s36, s36, s64\n"
    "s_addc_u32 s37, s37, 0\n"
    "s_sub_u32 s36, s36, 3072\n"
    "s_subb_u32 s37, s37, 0\n"
    "s_and_b32 s37, s37, 0xffff\n"
    "s_mov_b32 s38, 134144\n"
    "s_mov_b32 s39, 0x20000\n"
    "s_add_u32 s28, s28, s65\n"
    "s_addc_u32 s29, s29, 0\n"
    "s_and_b32 s29, s29, 0xffff\n"
    "s_mov_b32 s30, 4186368\n"
    "s_mov_b32 s31, 0x20000\n"
    "s_cmp_gt_u32 s75, s61\n"
    "s_cbranch_scc0 .Lnoprime\n"
    "v_readlane_b32 s67, v244, 15\n"
    "s_add_u32 m0, s48, 0\n"
    "s_add_u32 s68, s67, 3072\n"
    "buffer_load_dwordx4 v245, s[32:35], s68 offen offset:0 nt lds\n"
    "s_add_u32 s69, s67, 2176\n"
    "buffer_load_dwordx4 v245, s[32:35], s69 offen offset:1024 nt lds\n"
    "s_add_u32 s68, s67, 66560\n"
    "buffer_load_dwordx4 v245, s[32:35], s68 offen offset:2048 nt lds\n"
    "s_add_u32 s69, s67, 65664\n"
    "buffer_load_dwordx4 v245, s[32:35], s69 offen offset:3072 nt lds\n"
    "s_add_u32 m0, s48, 4096\n"
    "s_add_u32 s68, s67, 7168\n"
    "buffer_load_dwordx4 v245, s[32:35], s68 offen offset:0 nt lds\n"
    "s_add_u32 s69, s67, 6272\n"
    "buffer_load_dwordx4 v245, s[32:35], s69 offen offset:1024 nt lds\n"
    "s_add_u32 s68, s67, 70656\n"
    "buffer_load_dwordx4 v245, s[32:35], s68 offen offset:2048 nt lds\n"
    "s_add_u32 s69, s67, 69760\n"
    "buffer_load_dwordx4 v245, s[32:35], s69 offen offset:3072 nt lds\n"
    "v_readlane_b32 s67, v244, 15\n"
    "s_add_u32 m0, s48, 8192\n"
    "s_add_u32 s68, s67, 3072\n"
    "buffer_load_dwordx4 v254, s[36:39], s68 offen offset:0 nt lds\n"
    "s_add_u32 s69, s67, 2176\n"
    "buffer_load_dwordx4 v254, s[36:39], s69 offen offset:1024 nt lds\n"
    "s_add_u32 s68, s67, 66560\n"
    "buffer_load_dwordx4 v254, s[36:39], s68 offen offset:2048 nt lds\n"
    "s_add_u32 s69, s67, 65664\n"
    "buffer_load_dwordx4 v254, s[36:39], s69 offen offset:3072 nt lds\n"
    "s_add_u32 m0, s48, 12288\n"
    "s_add_u32 s68, s67, 7168\n"
    "buffer_load_dwordx4 v254, s[36:39], s68 offen offset:0 nt lds\n"
    "s_add_u32 s69, s67, 6272\n"
    "buffer_load_dwordx4 v254, s[36:39], s69 offen offset:1024 nt lds\n"
    "s_add_u32 s68, s67, 70656\n"
    "buffer_load_dwordx4 v254, s[36:39], s68 offen offset:2048 nt lds\n"
    "s_add_u32 s69, s67, 69760\n"
    "buffer_load_dwordx4 v254, s[36:39], s69 offen offset:3072 nt lds\n"
    "v_readlane_b32 s67, v244, 16\n"
    "s_add_u32 m0, s48, 16384\n"
    "s_add_u32 s68, s67, 3072\n"
    "buffer_load_dwordx4 v245, s[32:35], s68 offen offset:0 nt lds\n"
    "s_add_u32 s69, s67, 2176\n"
    "buffer_load_dwordx4 v245, s[32:35], s69 offen offset:1024 nt lds\n"
    "s_add_u32 s68, s67, 66560\n"
    "buffer_load_dwordx4 v245, s[32:35], s68 offen offset:2048 nt lds\n"
    "s_add_u32 s69, s67, 65664\n"
    "buffer_load_dwordx4 v245, s[32:35], s69 offen offset:3072 nt lds\n"
    "s_add_u32 m0, s48, 20480\n"
    "s_add_u32 s68, s67, 7168\n"
    "buffer_load_dwordx4 v245, s[32:35], s68 offen offset:0 nt lds\n"
    "s_add_u32 s69, s67, 6272\n"
    "buffer_load_dwordx4 v245, s[32:35], s69 offen offset:1024 nt lds\n"
    "s_add_u32 s68, s67, 70656\n"
    "buffer_load_dwordx4 v245, s[32:35], s68 offen offset:2048 nt lds\n"
    "s_add_u32 s69, s67, 69760\n"
    "buffer_load_dwordx4 v245, s[32:35], s69 offen offset:3072 nt lds\n"
    ".Lnoprime:\n"
    "v_and_b32 v246, 15, v255\n"
    "v_lshlrev_b32 v246, 4, v246\n"
    "v_lshl_add_u32 v232, v247, 16, v246\n"
    "v_add_u32 v233, 8192, v232\n"
    "v_add_u32 v234, 16384, v232\n"
    "v_add_u32 v235, 24576, v232\n"
    "v_add_u32 v236, 32768, v232\n"
    "v_add_u32 v237, 40960, v232\n"
    "v_add_u32 v238, 49152, v232\n"
    "v_add_u32 v239, 57344, v232\n"
    "v_and_b32 v246, 6, v255\n"
    "v_xor_b32 v246, v246, v247\n"
    "v_lshlrev_b32 v246, 4, v246\n"
    "v_and_b32 v247, 15, v255\n"
    "v_lshl_add_u32 v246, v247, 7, v246\n"
    "v_add_u32 v240, 98304, v246\n"
    "v_xor_b32 v241, 64, v240\n"
    "s_mov_b32 s53, 262144\n"
    "s_mov_b32 s54, 12288\n"
    "s_mov_b32 s74, s61\n"
    "s_cmp_eq_u32 s61, 0\n"
    "s_cbranch_scc1 .Lprol\n"
    "s_cmp_eq_u32 s44, 7\n"
    "s_cbranch_scc0 .Lnoep\n"
    "s_mov_b64 exec, 1\n"
    "global_atomic_add_x2 v[8:9], v4, v[4:5], s[84:85] offset:512 sc0 sc1\n"
    "s_mov_b64 exec, -1\n"
    ".Lnoep:\n"
    "v_mov_b32 v10, s78\n"
    "v_mov_b32 v11, 3072\n"
    "s_mov_b64 exec, 1\n"
    "global_load_dword v12, v10, s[16:17] nt\n"
    "global_load_dword v13, v10, s[18:19] nt\n"
    "buffer_load_dword v14, v11, s[32:35], 0 offen nt\n"
    "buffer_load_dword v15, v11, s[36:39], 0 offen nt\n"
    "buffer_load_dword v16, v4, s[40:43], 0 offen\n"
    "s_mov_b64 exec, -1\n"
    "s_cmp_eq_u32 s75, 0\n"
    "s_cbranch_scc1 .Lhb\n"
    "s_mul_i32 s65, s45, 12288\n"
    "s_lshl_b32 s66, s45, 8\n"
    "s_add_u32 s67, s66, 0\n"
    "buffer_load_dwordx4 v[96:99], v249, s[40:43], s67 offen\n"
    "s_add_u32 s67, s66, 8192\n"
    "buffer_load_dwordx4 v[100:103], v249, s[40:43], s67 offen\n"
    "s_add_u32 s67, s66, 16384\n"
    "buffer_load_dwordx4 v[104:107], v249, s[40:43], s67 offen\n"
    "s_add_u32 s67, s66, 24576\n"
    "buffer_load_dwordx4 v[108:111], v249, s[40:43], s67 offen\n"
    "s_add_u32 s67, s66, 32768\n"
    "buffer_load_dwordx4 v[112:115], v249, s[40:43], s67 offen\n"
    "s_add_u32 s67, s66, 40960\n"
    "buffer_load_dwordx4 v[116:119], v249, s[40:43], s67 offen\n"
    "s_add_u32 s67, s66, 128\n"
    "buffer_load_dwordx4 v[120:123], v249, s[40:43], s67 offen\n"
    "s_add_u32 s67, s66, 8320\n"
    "buffer_load_dwordx4 v[124:127], v249, s[40:43], s67 offen\n"
    "s_add_u32 s67, s66, 16512\n"
    "buffer_load_dwordx4 v[128:131], v249, s[40:43], s67 offen\n"
    "s_add_u32 s67, s66, 24704\n"
    "buffer_load_dwordx4 v[132:135], v249, s[40:43], s67 offen\n"
    "s_add_u32 s67, s66, 32896\n"
    "buffer_load_dwordx4 v[136:139], v249, s[40:43], s67 offen\n"
    "s_add_u32 s67, s66, 41088\n"
    "buffer_load_dwordx4 v[140:143], v249, s[40:43], s67 offen\n"
    "s_mov_b32 s64, 521\n"
    ".Ldl_1:\n"
    "s_memrealtime s[70:71]\n"
    "s_waitcnt lgkmcnt(0)\n"
    "s_sub_u32 s69, s70, s86\n"
    "s_cmp_ge_u32 s69, s64\n"
    "s_cbranch_scc1 .Ldn_2\n"
    "s_sleep 4\n"
    "s_branch .Ldl_1\n"
    ".Ldn_2:\n"
    "s_lshl_b32 s66, s45, 4\n"
    "s_lshr_b32 s67, s47, 1\n"
    "s_add_u32 s66, s66, s67\n"
    "s_lshl_b32 s64, s66, 16\n"
    "s_lshl_b32 s65, s79, 10\n"
    "s_add_u32 s64, s64, s65\n"
    "v_lshrrev_b32 v246, 3, v255\n"
    "v_lshlrev_b32 v246, 13, v246\n"
    "v_and_b32 v247, 7, v255\n"
    "v_lshl_add_u32 v246, v247, 7, v246\n"
    "v_add_u32 v246, s64, v246\n"
    "global_load_dword v248, v246, s[10:11]\n"
    "s_mul_i32 s65, s45, 12288\n"
    "s_add_u32 s65, s65, 98304\n"
    "v_lshlrev_b32 v249, 4, v255\n"
    "v_add_u32 v249, s65, v249\n"
    "s_waitcnt vmcnt(0)\n"
    "s_mov_b64 exec, 1\n"
    ".Lsp_3:\n"
    "ds_read_b32 v251, v253 offset:16\n"
    "s_waitcnt lgkmcnt(0)\n"
    "v_readfirstlane_b32 s64, v251\n"
    "s_nop 0\n"
    "s_cmp_ge_u32 s64, 8\n"
    "s_cbranch_scc1 .Lok_4\n"
    "s_sleep 4\n"
    "s_branch .Lsp_3\n"
    ".Lok_4:\n"
    "s_mov_b64 exec, -1\n"
    "ds_write_b128 v249, v[96:99] offset:0\n"
    "ds_write_b128 v249, v[100:103] offset:1024\n"
    "ds_write_b128 v249, v[104:107] offset:2048\n"
    "ds_write_b128 v249, v[108:111] offset:3072\n"
    "ds_write_b128 v249, v[112:115] offset:4096\n"
    "ds_write_b128 v249, v[116:119] offset:5120\n"
    "ds_write_b128 v249, v[120:123] offset:6144\n"
    "ds_write_b128 v249, v[124:127] offset:7168\n"
    "ds_write_b128 v249, v[128:131] offset:8192\n"
    "ds_write_b128 v249, v[132:135] offset:9216\n"
    "ds_write_b128 v249, v[136:139] offset:10240\n"
    "ds_write_b128 v249, v[140:143] offset:11264\n"
    "s_setprio 1\n"
    "s_branch .Lhe\n"
    ".Lhb:\n"
    "s_mov_b64 exec, 1\n"
    ".Lsp_5:\n"
    "ds_read_b32 v251, v253 offset:16\n"
    "s_waitcnt lgkmcnt(0)\n"
    "v_readfirstlane_b32 s64, v251\n"
    "s_nop 0\n"
    "s_cmp_ge_u32 s64, 8\n"
    "s_cbranch_scc1 .Lok_6\n"
    "s_sleep 4\n"
    "s_branch .Lsp_5\n"
    ".Lok_6:\n"
    "s_mov_b64 exec, -1\n"
    "s_memrealtime s[70:71]\n"
    "s_waitcnt lgkmcnt(0)\n"
    "s_sub_u32 s64, s70, s86\n"
    "s_add_u32 s64, s64, 74\n"
    ".Ldl_7:\n"
    "s_memrealtime s[70:71]\n"
    "s_waitcnt lgkmcnt(0)\n"
    "s_sub_u32 s69, s70, s86\n"
    "s_cmp_ge_u32 s69, s64\n"
    "s_cbranch_scc1 .Ldn_8\n"
    "s_sleep 4\n"
    "s_branch .Ldl_7\n"
    ".Ldn_8:\n"
    "s_setprio 1\n"
    "s_mul_i32 s65, s45, 12288\n"
    "s_lshl_b32 s66, s45, 8\n"
    "s_add_u32 m0, s65, 98304\n"
    "s_add_u32 s67, s66, 0\n"
    "buffer_load_dwordx4 v249, s[40:43], s67 offen lds\n"
    "s_add_u32 m0, s65, 99328\n"
    "s_add_u32 s67, s66, 8192\n"
    "buffer_load_dwordx4 v249, s[40:43], s67 offen lds\n"
    "s_add_u32 m0, s65, 100352\n"
    "s_add_u32 s67, s66, 16384\n"
    "buffer_load_dwordx4 v249, s[40:43], s67 offen lds\n"
    "s_add_u32 m0, s65, 101376\n"
    "s_add_u32 s67, s66, 24576\n"
    "buffer_load_dwordx4 v249, s[40:43], s67 offen lds\n"
    "s_add_u32 m0, s65, 102400\n"
    "s_add_u32 s67, s66, 32768\n"
    "buffer_load_dwordx4 v249, s[40:43], s67 offen lds\n"
    "s_add_u32 m0, s65, 103424\n"
    "s_add_u32 s67, s66, 40960\n"
    "buffer_load_dwordx4 v249, s[40:43], s67 offen lds\n"
    "s_add_u32 m0, s65, 104448\n"
    "s_add_u32 s67, s66, 128\n"
    "buffer_load_dwordx4 v249, s[40:43], s67 offen lds\n"
    "s_add_u32 m0, s65, 105472\n"
    "s_add_u32 s67, s66, 8320\n"
    "buffer_load_dwordx4 v249, s[40:43], s67 offen lds\n"
    "s_add_u32 m0, s65, 106496\n"
    "s_add_u32 s67, s66, 16512\n"
    "buffer_load_dwordx4 v249, s[40:43], s67 offen lds\n"
    "s_add_u32 m0, s65, 107520\n"
    "s_add_u32 s67, s66, 24704\n"
    "buffer_load_dwordx4 v249, s[40:43], s67 offen lds\n"
    "s_add_u32 m0, s65, 108544\n"
    "s_add_u32 s67, s66, 32896\n"
    "buffer_load_dwordx4 v249, s[40:43], s67 offen lds\n"
    "s_add_u32 m0, s65, 109568\n"
    "s_add_u32 s67, s66, 41088\n"
    "buffer_load_dwordx4 v249, s[40:43], s67 offen lds\n"
    ".Lhe:\n"
    "s_waitcnt vmcnt(0) lgkmcnt(0)\n"
    "s_cmp_eq_u32 s44, 7\n"
    "s_cbranch_scc0 .Lnoep2\n"
    "s_mov_b64 exec, 1\n"
    "ds_write_b64 v253, v[8:9] offset:24\n"
    "s_mov_b64 exec, -1\n"
    ".Lnoep2:\n"
    "s_mov_b64 exec, 1\n"
    "ds_add_u32 v253, v252 offset:0\n"
    "s_mov_b64 exec, -1\n"
    "s_add_u32 s55, s55, 4\n"
    "s_mov_b64 exec, 1\n"
    ".Lsp_9:\n"
    "ds_read_b32 v251, v253 offset:0\n"
    "s_waitcnt lgkmcnt(0)\n"
    "v_readfirstlane_b32 s64, v251\n"
    "s_nop 0\n"
    "s_cmp_ge_u32 s64, s55\n"
    "s_cbranch_scc1 .Lok_10\n"
    "s_sleep 1\n"
    "s_branch .Lsp_9\n"
    ".Lok_10:\n"
    "s_mov_b64 exec, -1\n"
    "ds_read_b64 v[246:247], v253 offset:24\n"
    "s_waitcnt lgkmcnt(0)\n"
    "v_readfirstlane_b32 s82, v246\n"
    "v_readfirstlane_b32 s83, v247\n"
    "s_add_u32 s82, s82, 0x7f4a7c15\n"
    "s_addc_u32 s83, s83, 0x9e3779b9\n"
    "s_lshr_b32 s64, s47, 2\n"
    "s_mov_b32 s77, 67174912\n"
    "s_cmp_eq_u32 s64, 1\n"
    "s_cselect_b32 s77, 117835011, s77\n"
    "s_cmp_eq_u32 s64, 2\n"
    "s_cselect_b32 s77, 185207048, s77\n"
    "s_cmp_eq_u32 s64, 3\n"
    "s_cselect_b32 s77, 235867404, s77\n"
    "s_cmp_eq_u32 s64, 4\n"
    "s_cselect_b32 s77, 303239185, s77\n"
    "s_cmp_eq_u32 s64, 5\n"
    "s_cselect_b32 s77, 370611221, s77\n"
    "s_cmp_eq_u32 s64, 6\n"
    "s_cselect_b32 s77, 421271578, s77\n"
    "s_cmp_eq_u32 s64, 7\n"
    "s_cselect_b32 s77, 488577822, s77\n"
    "s_and_b32 s64, s47, 3\n"
    "s_lshl_b32 s64, s64, 3\n"
    "s_lshr_b32 s77, s77, s64\n"
    "s_and_b32 s77, s77, 31\n"
    "s_mul_i32 s76, s77, 13\n"
    "s_ashr_i32 s76, s76, 5\n"
    "s_add_u32 s76, s76, 86\n"
    "s_min_u32 s64, s77, 11\n"
    "s_mul_i32 s64, s64, 30\n"
    "s_mul_i32 s78, s77, 53\n"
    "s_add_u32 s78, s78, s64\n"
    "s_add_u32 s78, s78, 3112\n"
    "s_min_u32 s65, s77, 15\n"
    "s_mul_i32 s65, s65, 40\n"
    "s_mul_i32 s64, s77, 69\n"
    "s_sub_u32 s64, s64, s65\n"
    "s_add_u32 s64, s64, 1166\n"
    ".Ldl_11:\n"
    "s_memrealtime s[70:71]\n"
    "s_waitcnt lgkmcnt(0)\n"
    "s_sub_u32 s69, s70, s86\n"
    "s_cmp_ge_u32 s69, s64\n"
    "s_cbranch_scc1 .Ldn_12\n"
    "s_sleep 4\n"
    "s_branch .Ldl_11\n"
    ".Ldn_12:\n"
    "s_mov_b64 exec, 1\n"
    "ds_add_u32 v253, v252 offset:8\n"
    "s_mov_b64 exec, -1\n"
    ".Lprol:\n"
    "s_mov_b32 s52, 0\n"
    "buffer_load_dwordx4 v[96:99], v232, s[28:31], s52 offen\n"
    "buffer_load_dwordx4 v[100:103], v233, s[28:31], s52 offen\n"
    "buffer_load_dwordx4 v[104:107], v234, s[28:31], s52 offen\n"
    "buffer_load_dwordx4 v[108:111], v235, s[28:31], s52 offen\n"
    "buffer_load_dwordx4 v[112:115], v236, s[28:31], s52 offen\n"
    "buffer_load_dwordx4 v[116:119], v237, s[28:31], s52 offen\n"
    "buffer_load_dwordx4 v[120:123], v238, s[28:31], s52 offen\n"
    "buffer_load_dwordx4 v[124:127], v239, s[28:31], s52 offen\n"
    "s_add_u32 s52, s52, s53\n"
    "buffer_load_dwordx4 v[128:131], v232, s[28:31], s52 offen\n"
    "buffer_load_dwordx4 v[132:135], v233, s[28:31], s52 offen\n"
    "buffer_load_dwordx4 v[136:139], v234, s[28:31], s52 offen\n"
    "buffer_load_dwordx4 v[140:143], v235, s[28:31], s52 offen\n"
    "buffer_load_dwordx4 v[144:147], v236, s[28:31], s52 offen\n"
    "buffer_load_dwordx4 v[148:151], v237, s[28:31], s52 offen\n"
    "buffer_load_dwordx4 v[152:155], v238, s[28:31], s52 offen\n"
    "buffer_load_dwordx4 v[156:159], v239, s[28:31], s52 offen\n"
    "s_add_u32 s52, s52, s53\n"
    "buffer_load_dwordx4 v[160:163], v232, s[28:31], s52 offen\n"
    "buffer_load_dwordx4 v[164:167], v233, s[28:31], s52 offen\n"
    "buffer_load_dwordx4 v[168:171], v234, s[28:31], s52 offen\n"
    "buffer_load_dwordx4 v[172:175], v235, s[28:31], s52 offen\n"
    "buffer_load_dwordx4 v[176:179], v236, s[28:31], s52 offen\n"
    "buffer_load_dwordx4 v[180:183], v237, s[28:31], s52 offen\n"
    "buffer_load_dwordx4 v[184:187], v238, s[28:31], s52 offen\n"
    "buffer_load_dwordx4 v[188:191], v239, s[28:31], s52 offen\n"
    "s_cmp_eq_u32 s74, 0\n"
    "s_cbranch_scc0 .Lsyncg\n"
    "s_waitcnt vmcnt(24)\n"
    "s_mov_b64 exec, 1\n"
    "ds_add_u32 v253, v252 offset:16\n"
    "s_mov_b64 exec, -1\n"
    "s_mov_b64 exec, 1\n"
    ".Lsp_13:\n"
    "ds_read_b32 v251, v253 offset:16\n"
    "s_waitcnt lgkmcnt(0)\n"
    "v_readfirstlane_b32 s64, v251\n"
    "s_nop 0\n"
    "s_cmp_ge_u32 s64, 4\n"
    "s_cbranch_scc1 .Lok_14\n"
    "s_sleep 1\n"
    "s_branch .Lsp_13\n"
    ".Lok_14:\n"
    "s_mov_b64 exec, -1\n"
    "s_branch .Lproj\n"
    ".Lsyncg:\n"
    "s_mov_b64 exec, 1\n"
    ".Lsp_15:\n"
    "ds_read_b32 v251, v253 offset:0\n"
    "s_waitcnt lgkmcnt(0)\n"
    "v_readfirstlane_b32 s64, v251\n"
    "s_nop 0\n"
    "s_cmp_ge_u32 s64, s55\n"
    "s_cbranch_scc1 .Lok_16\n"
    "s_sleep 1\n"
    "s_branch .Lsp_15\n"
    ".Lok_16:\n"
    "s_mov_b64 exec, -1\n"
    ".Lproj:\n"
    "s_cmp_eq_u32 s56, 0\n"
    "s_cbranch_scc0 .Lprojo\n"
    "ds_read_b128 v[192:195], v240 offset:0\n"
    "ds_read_b128 v[196:199], v240 offset:2048\n"
    "ds_read_b128 v[200:203], v240 offset:4096\n"
    "s_waitcnt vmcnt(16)\n"
    "v_perm_b32 v219, v124, v120, s59\n"
    "v_perm_b32 v218, v116, v112, s59\n"
    "v_perm_b32 v217, v108, v104, s59\n"
    "v_perm_b32 v216, v100, v96, s59\n"
    "v_perm_b32 v220, v100, v96, s60\n"
    "v_perm_b32 v221, v108, v104, s60\n"
    "v_perm_b32 v222, v116, v112, s60\n"
    "v_perm_b32 v223, v124, v120, s60\n"
    "s_waitcnt lgkmcnt(2)\n"
    "v_mfma_f32_16x16x32_bf16 v[0:3], v[216:219], v[192:195], 0\n"
    "v_perm_b32 v224, v101, v97, s59\n"
    "v_perm_b32 v225, v109, v105, s59\n"
    "v_mfma_f32_16x16x32_bf16 v[12:15], v[220:223], v[192:195], 0\n"
    "v_perm_b32 v226, v117, v113, s59\n"
    "v_perm_b32 v227, v125, v121, s59\n"
    "s_waitcnt lgkmcnt(1)\n"
    "v_mfma_f32_16x16x32_bf16 v[16:19], v[220:223], v[196:199], 0\n"
    "v_mfma_f32_16x16x32_bf16 v[4:7], v[216:219], v[196:199], 0\n"
    "v_perm_b32 v228, v101, v97, s60\n"
    "v_perm_b32 v229, v109, v105, s60\n"
    "s_waitcnt lgkmcnt(0)\n"
    "v_mfma_f32_16x16x32_bf16 v[8:11], v[216:219], v[200:203], 0\n"
    "v_perm_b32 v230, v117, v113, s60\n"
    "v_perm_b32 v231, v125, v121, s60\n"
    "v_mfma_f32_16x16x32_bf16 v[20:23], v[220:223], v[200:203], 0\n"
    "v_mfma_f32_16x16x32_bf16 v[32:35], v[224:227], v[200:203], 0\n"
    "v_perm_b32 v216, v102, v98, s59\n"
    "v_perm_b32 v217, v110, v106, s59\n"
    "v_mfma_f32_16x16x32_bf16 v[44:47], v[228:231], v[200:203], 0\n"
    "v_perm_b32 v218, v118, v114, s59\n"
    "v_perm_b32 v219, v126, v122, s59\n"
    "v_mfma_f32_16x16x32_bf16 v[40:43], v[228:231], v[196:199], 0\n"
    "v_mfma_f32_16x16x32_bf16 v[28:31], v[224:227], v[196:199], 0\n"
    "v_perm_b32 v220, v102, v98, s60\n"
    "v_perm_b32 v221, v110, v106, s60\n"
    "v_mfma_f32_16x16x32_bf16 v[24:27], v[224:227], v[192:195], 0\n"
    "v_perm_b32 v222, v118, v114, s60\n"
    "v_perm_b32 v223, v126, v122, s60\n"
    "v_mfma_f32_16x16x32_bf16 v[36:39], v[228:231], v[192:195], 0\n"
    "ds_read_b128 v[204:207], v241 offset:0\n"
    "v_mfma_f32_16x16x32_bf16 v[48:51], v[216:219], v[192:195], 0\n"
    "v_perm_b32 v224, v103, v99, s59\n"
    "v_perm_b32 v225, v111, v107, s59\n"
    "v_mfma_f32_16x16x32_bf16 v[60:63], v[220:223], v[192:195], 0\n"
    "v_perm_b32 v226, v119, v115, s59\n"
    "v_perm_b32 v227, v127, v123, s59\n"
    "v_mfma_f32_16x16x32_bf16 v[64:67], v[220:223], v[196:199], 0\n"
    "ds_read_b128 v[208:211], v241 offset:2048\n"
    "v_mfma_f32_16x16x32_bf16 v[52:55], v[216:219], v[196:199], 0\n"
    "v_perm_b32 v228, v103, v99, s60\n"
    "v_perm_b32 v229, v111, v107, s60\n"
    "v_mfma_f32_16x16x32_bf16 v[56:59], v[216:219], v[200:203], 0\n"
    "v_perm_b32 v230, v119, v115, s60\n"
    "v_perm_b32 v231, v127, v123, s60\n"
    "v_mfma_f32_16x16x32_bf16 v[68:71], v[220:223], v[200:203], 0\n"
    "ds_read_b128 v[212:215], v241 offset:4096\n"
    "v_mfma_f32_16x16x32_bf16 v[80:83], v[224:227], v[200:203], 0\n"
    "s_waitcnt vmcnt(8)\n"
    "v_perm_b32 v219, v156, v152, s59\n"
    "v_perm_b32 v218, v148, v144, s59\n"
    "v_mfma_f32_16x16x32_bf16 v[92:95], v[228:231], v[200:203], 0\n"
    "v_perm_b32 v217, v140, v136, s59\n"
    "v_perm_b32 v216, v132, v128, s59\n"
    "s_add_u32 s52, s52, s53\n"
    "buffer_load_dwordx4 v[96:99], v232, s[28:31], s52 offen\n"
    "v_mfma_f32_16x16x32_bf16 v[88:91], v[228:231], v[196:199], 0\n"
    "buffer_load_dwordx4 v[100:103], v233, s[28:31], s52 offen\n"
    "v_mfma_f32_16x16x32_bf16 v[76:79], v[224:227], v[196:199], 0\n"
    "v_perm_b32 v220, v132, v128, s60\n"
    "v_perm_b32 v221, v140, v136, s60\n"
    "v_mfma_f32_16x16x32_bf16 v[72:75], v[224:227], v[192:195], 0\n"
    "v_perm_b32 v222, v148, v144, s60\n"
    "v_perm_b32 v223, v156, v152, s60\n"
    "buffer_load_dwordx4 v[104:107], v234, s[28:31], s52 offen\n"
    "v_mfma_f32_16x16x32_bf16 v[84:87], v[228:231], v[192:195], 0\n"
    "buffer_load_dwordx4 v[108:111], v235, s[28:31], s52 offen\n"
    "s_waitcnt lgkmcnt(0)\n"
    "v_mfma_f32_16x16x32_bf16 v[0:3], v[216:219], v[204:207], v[0:3]\n"
    "v_perm_b32 v224, v133, v129, s59\n"
    "v_perm_b32 v225, v141, v137, s59\n"
    "v_mfma_f32_16x16x32_bf16 v[12:15], v[220:223], v[204:207], v[12:15]\n"
    "v_perm_b32 v226, v149, v145, s59\n"
    "v_perm_b32 v227, v157, v153, s59\n"
    "buffer_load_dwordx4 v[112:115], v236, s[28:31], s52 offen\n"
    "v_mfma_f32_16x16x32_bf16 v[16:19], v[220:223], v[208:211], v[16:19]\n"
    "buffer_load_dwordx4 v[116:119], v237, s[28:31], s52 offen\n"
    "v_mfma_f32_16x16x32_bf16 v[4:7], v[216:219], v[208:211], v[4:7]\n"
    "v_perm_b32 v228, v133, v129, s60\n"
    "v_perm_b32 v229, v141, v137, s60\n"
    "v_mfma_f32_16x16x32_bf16 v[8:11], v[216:219], v[212:215], v[8:11]\n"
    "v_perm_b32 v230, v149, v145, s60\n"
    "v_perm_b32 v231, v157, v153, s60\n"
    "buffer_load_dwordx4 v[120:123], v238, s[28:31], s52 offen\n"
    "v_mfma_f32_16x16x32_bf16 v[20:23], v[220:223], v[212:215], v[20:23]\n"
    "buffer_load_dwordx4 v[124:127], v239, s[28:31], s52 offen\n"
    "v_mfma_f32_16x16x32_bf16 v[32:35], v[224:227], v[212:215], v[32:35]\n"
    "v_perm_b32 v216, v134, v130, s59\n"
    "v_perm_b32 v217, v142, v138, s59\n"
    "v_mfma_f32_16x16x32_bf16 v[44:47], v[228:231], v[212:215], v[44:47]\n"
    "v_perm_b32 v218, v150, v146, s59\n"
    "v_perm_b32 v219, v158, v154, s59\n"
    "v_mfma_f32_16x16x32_bf16 v[40:43], v[228:231], v[208:211], v[40:43]\n"
    "v_mfma_f32_16x16x32_bf16 v[28:31], v[224:227], v[208:211], v[28:31]\n"
    "v_perm_b32 v220, v134, v130, s60\n"
    "v_perm_b32 v221, v142, v138, s60\n"
    "v_mfma_f32_16x16x32_bf16 v[24:27], v[224:227], v[204:207], v[24:27]\n"
    "v_perm_b32 v222, v150, v146, s60\n"
    "v_perm_b32 v223, v158, v154, s60\n"
    "v_mfma_f32_16x16x32_bf16 v[36:39], v[228:231], v[204:207], v[36:39]\n"
    "ds_read_b128 v[192:195], v240 offset:6144\n"
    "v_mfma_f32_16x16x32_bf16 v[48:51], v[216:219], v[204:207], v[48:51]\n"
    "v_perm_b32 v224, v135, v131, s59\n"
    "v_perm_b32 v225, v143, v139, s59\n"
    "v_mfma_f32_16x16x32_bf16 v[60:63], v[220:223], v[204:207], v[60:63]\n"
    "v_perm_b32 v226, v151, v147, s59\n"
    "v_perm_b32 v227, v159, v155, s59\n"
    "v_mfma_f32_16x16x32_bf16 v[64:67], v[220:223], v[208:211], v[64:67]\n"
    "ds_read_b128 v[196:199], v240 offset:8192\n"
    "v_mfma_f32_16x16x32_bf16 v[52:55], v[216:219], v[208:211], v[52:55]\n"
    "v_perm_b32 v228, v135, v131, s60\n"
    "v_perm_b32 v229, v143, v139, s60\n"
    "v_mfma_f32_16x16x32_bf16 v[56:59], v[216:219], v[212:215], v[56:59]\n"
    "v_perm_b32 v230, v151, v147, s60\n"
    "v_perm_b32 v231, v159, v155, s60\n"
    "v_mfma_f32_16x16x32_bf16 v[68:71], v[220:223], v[212:215], v[68:71]\n"
    "ds_read_b128 v[200:203], v240 offset:10240\n"
    "v_mfma_f32_16x16x32_bf16 v[80:83], v[224:227], v[212:215], v[80:83]\n"
    "s_waitcnt vmcnt(8)\n"
    "v_perm_b32 v219, v188, v184, s59\n"
    "v_perm_b32 v218, v180, v176, s59\n"
    "v_mfma_f32_16x16x32_bf16 v[92:95], v[228:231], v[212:215], v[92:95]\n"
    "v_perm_b32 v217, v172, v168, s59\n"
    "v_perm_b32 v216, v164, v160, s59\n"
    "s_add_u32 s52, s52, s53\n"
    "buffer_load_dwordx4 v[128:131], v232, s[28:31], s52 offen\n"
    "v_mfma_f32_16x16x32_bf16 v[88:91], v[228:231], v[208:211], v[88:91]\n"
    "buffer_load_dwordx4 v[132:135], v233, s[28:31], s52 offen\n"
    "v_mfma_f32_16x16x32_bf16 v[76:79], v[224:227], v[208:211], v[76:79]\n"
    "v_perm_b32 v220, v164, v160, s60\n"
    "v_perm_b32 v221, v172, v168, s60\n"
    "v_mfma_f32_16x16x32_bf16 v[72:75], v[224:227], v[204:207], v[72:75]\n"
    "v_perm_b32 v222, v180, v176, s60\n"
    "v_perm_b32 v223, v188, v184, s60\n"
    "buffer_load_dwordx4 v[136:139], v234, s[28:31], s52 offen\n"
    "v_mfma_f32_16x16x32_bf16 v[84:87], v[228:231], v[204:207], v[84:87]\n"
    "buffer_load_dwordx4 v[140:143], v235, s[28:31], s52 offen\n"
    "s_waitcnt lgkmcnt(0)\n"
    "v_mfma_f32_16x16x32_bf16 v[0:3], v[216:219], v[192:195], v[0:3]\n"
    "v_perm_b32 v224, v165, v161, s59\n"
    "v_perm_b32 v225, v173, v169, s59\n"
    "v_mfma_f32_16x16x32_bf16 v[12:15], v[220:223], v[192:195], v[12:15]\n"
    "v_perm_b32 v226, v181, v177, s59\n"
    "v_perm_b32 v227, v189, v185, s59\n"
    "buffer_load_dwordx4 v[144:147], v236, s[28:31], s52 offen\n"
    "v_mfma_f32_16x16x32_bf16 v[16:19], v[220:223], v[196:199], v[16:19]\n"
    "buffer_load_dwordx4 v[148:151], v237, s[28:31], s52 offen\n"
    "v_mfma_f32_16x16x32_bf16 v[4:7], v[216:219], v[196:199], v[4:7]\n"
    "v_perm_b32 v228, v165, v161, s60\n"
    "v_perm_b32 v229, v173, v169, s60\n"
    "v_mfma_f32_16x16x32_bf16 v[8:11], v[216:219], v[200:203], v[8:11]\n"
    "v_perm_b32 v230, v181, v177, s60\n"
    "v_perm_b32 v231, v189, v185, s60\n"
    "buffer_load_dwordx4 v[152:155], v238, s[28:31], s52 offen\n"
    "v_mfma_f32_16x16x32_bf16 v[20:23], v[220:223], v[200:203], v[20:23]\n"
    "buffer_load_dwordx4 v[156:159], v239, s[28:31], s52 offen\n"
    "v_mfma_f32_16x16x32_bf16 v[32:35], v[224:227], v[200:203], v[32:35]\n"
    "v_perm_b32 v216, v166, v162, s59\n"
    "v_perm_b32 v217, v174, v170, s59\n"
    "v_mfma_f32_16x16x32_bf16 v[44:47], v[228:231], v[200:203], v[44:47]\n"
    "v_perm_b32 v218, v182, v178, s59\n"
    "v_perm_b32 v219, v190, v186, s59\n"
    "v_mfma_f32_16x16x32_bf16 v[40:43], v[228:231], v[196:199], v[40:43]\n"
    "v_mfma_f32_16x16x32_bf16 v[28:31], v[224:227], v[196:199], v[28:31]\n"
    "v_perm_b32 v220, v166, v162, s60\n"
    "v_perm_b32 v221, v174, v170, s60\n"
    "v_mfma_f32_16x16x32_bf16 v[24:27], v[224:227], v[192:195], v[24:27]\n"
    "v_perm_b32 v222, v182, v178, s60\n"
    "v_perm_b32 v223, v190, v186, s60\n"
    "v_mfma_f32_16x16x32_bf16 v[36:39], v[228:231], v[192:195], v[36:39]\n"
    "ds_read_b128 v[204:207], v241 offset:6144\n"
    "v_mfma_f32_16x16x32_bf16 v[48:51], v[216:219], v[192:195], v[48:51]\n"
    "v_perm_b32 v224, v167, v163, s59\n"
    "v_perm_b32 v225, v175, v171, s59\n"
    "v_mfma_f32_16x16x32_bf16 v[60:63], v[220:223], v[192:195], v[60:63]\n"
    "v_perm_b32 v226, v183, v179, s59\n"
    "v_perm_b32 v227, v191, v187, s59\n"
    "v_mfma_f32_16x16x32_bf16 v[64:67], v[220:223], v[196:199], v[64:67]\n"
    "ds_read_b128 v[208:211], v241 offset:8192\n"
    "v_mfma_f32_16x16x32_bf16 v[52:55], v[216:219], v[196:199], v[52:55]\n"
    "v_perm_b32 v228, v167, v163, s60\n"
    "v_perm_b32 v229, v175, v171, s60\n"
    "v_mfma_f32_16x16x32_bf16 v[56:59], v[216:219], v[200:203], v[56:59]\n"
    "v_perm_b32 v230, v183, v179, s60\n"
    "v_perm_b32 v231, v191, v187, s60\n"
    "v_mfma_f32_16x16x32_bf16 v[68:71], v[220:223], v[200:203], v[68:71]\n"
    "ds_read_b128 v[212:215], v241 offset:10240\n"
    "v_mfma_f32_16x16x32_bf16 v[80:83], v[224:227], v[200:203], v[80:83]\n"
    "s_waitcnt vmcnt(8)\n"
    "v_perm_b32 v219, v124, v120, s59\n"
    "v_perm_b32 v218, v116, v112, s59\n"
    "v_mfma_f32_16x16x32_bf16 v[92:95], v[228:231], v[200:203], v[92:95]\n"
    "v_perm_b32 v217, v108, v104, s59\n"
    "v_perm_b32 v216, v100, v96, s59\n"
    "s_add_u32 s52, s52, s53\n"
    "buffer_load_dwordx4 v[160:163], v232, s[28:31], s52 offen\n"
    "v_mfma_f32_16x16x32_bf16 v[88:91], v[228:231], v[196:199], v[88:91]\n"
    "buffer_load_dwordx4 v[164:167], v233, s[28:31], s52 offen\n"
    "v_mfma_f32_16x16x32_bf16 v[76:79], v[224:227], v[196:199], v[76:79]\n"
    "v_perm_b32 v220, v100, v96, s60\n"
    "v_perm_b32 v221, v108, v104, s60\n"
    "v_mfma_f32_16x16x32_bf16 v[72:75], v[224:227], v[192:195], v[72:75]\n"
    "v_perm_b32 v222, v116, v112, s60\n"
    "v_perm_b32 v223, v124, v120, s60\n"
    "buffer_load_dwordx4 v[168:171], v234, s[28:31], s52 offen\n"
    "v_mfma_f32_16x16x32_bf16 v[84:87], v[228:231], v[192:195], v[84:87]\n"
    "buffer_load_dwordx4 v[172:175], v235, s[28:31], s52 offen\n"
    "s_waitcnt lgkmcnt(0)\n"
    "v_mfma_f32_16x16x32_bf16 v[0:3], v[216:219], v[204:207], v[0:3]\n"
    "v_perm_b32 v224, v101, v97, s59\n"
    "v_perm_b32 v225, v109, v105, s59\n"
    "v_mfma_f32_16x16x32_bf16 v[12:15], v[220:223], v[204:207], v[12:15]\n"
    "v_perm_b32 v226, v117, v113, s59\n"
    "v_perm_b32 v227, v125, v121, s59\n"
    "buffer_load_dwordx4 v[176:179], v236, s[28:31], s52 offen\n"
    "v_mfma_f32_16x16x32_bf16 v[16:19], v[220:223], v[208:211], v[16:19]\n"
    "buffer_load_dwordx4 v[180:183], v237, s[28:31], s52 offen\n"
    "v_mfma_f32_16x16x32_bf16 v[4:7], v[216:219], v[208:211], v[4:7]\n"
    "v_perm_b32 v228, v101, v97, s60\n"
    "v_perm_b32 v229, v109, v105, s60\n"
    "v_mfma_f32_16x16x32_bf16 v[8:11], v[216:219], v[212:215], v[8:11]\n"
    "v_perm_b32 v230, v117, v113, s60\n"
    "v_perm_b32 v231, v125, v121, s60\n"
    "buffer_load_dwordx4 v[184:187], v238, s[28:31], s52 offen\n"
    "v_mfma_f32_16x16x32_bf16 v[20:23], v[220:223], v[212:215], v[20:23]\n"
    "buffer_load_dwordx4 v[188:191], v239, s[28:31], s52 offen\n"
    "v_mfma_f32_16x16x32_bf16 v[32:35], v[224:227], v[212:215], v[32:35]\n"
    "v_perm_b32 v216, v102, v98, s59\n"
    "v_perm_b32 v217, v110, v106, s59\n"
    "v_mfma_f32_16x16x32_bf16 v[44:47], v[228:231], v[212:215], v[44:47]\n"
    "v_perm_b32 v218, v118, v114, s59\n"
    "v_perm_b32 v219, v126, v122, s59\n"
    "v_mfma_f32_16x16x32_bf16 v[40:43], v[228:231], v[208:211], v[40:43]\n"
    "v_add_u32 v240, s54, v240\n"
    "v_mfma_f32_16x16x32_bf16 v[28:31], v[224:227], v[208:211], v[28:31]\n"
    "v_perm_b32 v220, v102, v98, s60\n"
    "v_perm_b32 v221, v110, v106, s60\n"
    "v_mfma_f32_16x16x32_bf16 v[24:27], v[224:227], v[204:207], v[24:27]\n"
    "v_perm_b32 v222, v118, v114, s60\n"
    "v_perm_b32 v223, v126, v122, s60\n"
    "v_mfma_f32_16x16x32_bf16 v[36:39], v[228:231], v[204:207], v[36:39]\n"
    "ds_read_b128 v[192:195], v240 offset:0\n"
    "v_mfma_f32_16x16x32_bf16 v[48:51], v[216:219], v[204:207], v[48:51]\n"
    "v_perm_b32 v224, v103, v99, s59\n"
    "v_perm_b32 v225, v111, v107, s59\n"
    "v_mfma_f32_16x16x32_bf16 v[60:63], v[220:223], v[204:207], v[60:63]\n"
    "v_perm_b32 v226, v119, v115, s59\n"
    "v_perm_b32 v227, v127, v123, s59\n"
    "v_mfma_f32_16x16x32_bf16 v[64:67], v[220:223], v[208:211], v[64:67]\n"
    "ds_read_b128 v[196:199], v240 offset:2048\n"
    "v_mfma_f32_16x16x32_bf16 v[52:55], v[216:219], v[208:211], v[52:55]\n"
    "v_perm_b32 v228, v103, v99, s60\n"
    "v_perm_b32 v229, v111, v107, s60\n"
    "v_mfma_f32_16x16x32_bf16 v[56:59], v[216:219], v[212:215], v[56:59]\n"
    "v_perm_b32 v230, v119, v115, s60\n"
    "v_perm_b32 v231, v127, v123, s60\n"
    "v_mfma_f32_16x16x32_bf16 v[68:71], v[220:223], v[212:215], v[68:71]\n"
    "ds_read_b128 v[200:203], v240 offset:4096\n"
    "v_mfma_f32_16x16x32_bf16 v[80:83], v[224:227], v[212:215], v[80:83]\n"
    "s_waitcnt vmcnt(8)\n"
    "v_perm_b32 v219, v156, v152, s59\n"
    "v_perm_b32 v218, v148, v144, s59\n"
    "v_mfma_f32_16x16x32_bf16 v[92:95], v[228:231], v[212:215], v[92:95]\n"
    "v_perm_b32 v217, v140, v136, s59\n"
    "v_perm_b32 v216, v132, v128, s59\n"
    "s_add_u32 s52, s52, s53\n"
    "buffer_load_dwordx4 v[96:99], v232, s[28:31], s52 offen\n"
    "v_mfma_f32_16x16x32_bf16 v[88:91], v[228:231], v[208:211], v[88:91]\n"
    "buffer_load_dwordx4 v[100:103], v233, s[28:31], s52 offen\n"
    "v_mfma_f32_16x16x32_bf16 v[76:79], v[224:227], v[208:211], v[76:79]\n"
    "v_perm_b32 v220, v132, v128, s60\n"
    "v_perm_b32 v221, v140, v136, s60\n"
    "v_mfma_f32_16x16x32_bf16 v[72:75], v[224:227], v[204:207], v[72:75]\n"
    "v_perm_b32 v222, v148, v144, s60\n"
    "v_perm_b32 v223, v156, v152, s60\n"
    "buffer_load_dwordx4 v[104:107], v234, s[28:31], s52 offen\n"
    "v_mfma_f32_16x16x32_bf16 v[84:87], v[228:231], v[204:207], v[84:87]\n"
    "buffer_load_dwordx4 v[108:111], v235, s[28:31], s52 offen\n"
    "s_waitcnt lgkmcnt(0)\n"
    "v_mfma_f32_16x16x32_bf16 v[0:3], v[216:219], v[192:195], v[0:3]\n"
    "v_perm_b32 v224, v133, v129, s59\n"
    "v_perm_b32 v225, v141, v137, s59\n"
    "v_mfma_f32_16x16x32_bf16 v[12:15], v[220:223], v[192:195], v[12:15]\n"
    "v_perm_b32 v226, v149, v145, s59\n"
    "v_perm_b32 v227, v157, v153, s59\n"
    "buffer_load_dwordx4 v[112:115], v236, s[28:31], s52 offen\n"
    "v_mfma_f32_16x16x32_bf16 v[16:19], v[220:223], v[196:199], v[16:19]\n"
    "buffer_load_dwordx4 v[116:119], v237, s[28:31], s52 offen\n"
    "v_mfma_f32_16x16x32_bf16 v[4:7], v[216:219], v[196:199], v[4:7]\n"
    "v_perm_b32 v228, v133, v129, s60\n"
    "v_perm_b32 v229, v141, v137, s60\n"
    "v_mfma_f32_16x16x32_bf16 v[8:11], v[216:219], v[200:203], v[8:11]\n"
    "v_perm_b32 v230, v149, v145, s60\n"
    "v_perm_b32 v231, v157, v153, s60\n"
    "buffer_load_dwordx4 v[120:123], v238, s[28:31], s52 offen\n"
    "v_mfma_f32_16x16x32_bf16 v[20:23], v[220:223], v[200:203], v[20:23]\n"
    "buffer_load_dwordx4 v[124:127], v239, s[28:31], s52 offen\n"
    "v_mfma_f32_16x16x32_bf16 v[32:35], v[224:227], v[200:203], v[32:35]\n"
    "v_perm_b32 v216, v134, v130, s59\n"
    "v_perm_b32 v217, v142, v138, s59\n"
    "v_mfma_f32_16x16x32_bf16 v[44:47], v[228:231], v[200:203], v[44:47]\n"
    "v_perm_b32 v218, v150, v146, s59\n"
    "v_perm_b32 v219, v158, v154, s59\n"
    "v_mfma_f32_16x16x32_bf16 v[40:43], v[228:231], v[196:199], v[40:43]\n"
    "v_add_u32 v241, s54, v241\n"
    "v_mfma_f32_16x16x32_bf16 v[28:31], v[224:227], v[196:199], v[28:31]\n"
    "v_perm_b32 v220, v134, v130, s60\n"
    "v_perm_b32 v221, v142, v138, s60\n"
    "v_mfma_f32_16x16x32_bf16 v[24:27], v[224:227], v[192:195], v[24:27]\n"
    "v_perm_b32 v222, v150, v146, s60\n"
    "v_perm_b32 v223, v158, v154, s60\n"
    "v_mfma_f32_16x16x32_bf16 v[36:39], v[228:231], v[192:195], v[36:39]\n"
    "ds_read_b128 v[204:207], v241 offset:0\n"
    "v_mfma_f32_16x16x32_bf16 v[48:51], v[216:219], v[192:195], v[48:51]\n"
    "v_perm_b32 v224, v135, v131, s59\n"
    "v_perm_b32 v225, v143, v139, s59\n"
    "v_mfma_f32_16x16x32_bf16 v[60:63], v[220:223], v[192:195], v[60:63]\n"
    "v_perm_b32 v226, v151, v147, s59\n"
    "v_perm_b32 v227, v159, v155, s59\n"
    "v_mfma_f32_16x16x32_bf16 v[64:67], v[220:223], v[196:199], v[64:67]\n"
    "ds_read_b128 v[208:211], v241 offset:2048\n"
    "v_mfma_f32_16x16x32_bf16 v[52:55], v[216:219], v[196:199], v[52:55]\n"
    "v_perm_b32 v228, v135, v131, s60\n"
    "v_perm_b32 v229, v143, v139, s60\n"
    "v_mfma_f32_16x16x32_bf16 v[56:59], v[216:219], v[200:203], v[56:59]\n"
    "v_perm_b32 v230, v151, v147, s60\n"
    "v_perm_b32 v231, v159, v155, s60\n"
    "v_mfma_f32_16x16x32_bf16 v[68:71], v[220:223], v[200:203], v[68:71]\n"
    "ds_read_b128 v[212:215], v241 offset:4096\n"
    "v_mfma_f32_16x16x32_bf16 v[80:83], v[224:227], v[200:203], v[80:83]\n"
    "s_waitcnt vmcnt(8)\n"
    "v_perm_b32 v219, v188, v184, s59\n"
    "v_perm_b32 v218, v180, v176, s59\n"
    "v_mfma_f32_16x16x32_bf16 v[92:95], v[228:231], v[200:203], v[92:95]\n"
    "v_perm_b32 v217, v172, v168, s59\n"
    "v_perm_b32 v216, v164, v160, s59\n"
    "s_add_u32 s52, s52, s53\n"
    "buffer_load_dwordx4 v[128:131], v232, s[28:31], s52 offen\n"
    "v_mfma_f32_16x16x32_bf16 v[88:91], v[228:231], v[196:199], v[88:91]\n"
    "buffer_load_dwordx4 v[132:135], v233, s[28:31], s52 offen\n"
    "v_mfma_f32_16x16x32_bf16 v[76:79], v[224:227], v[196:199], v[76:79]\n"
    "v_perm_b32 v220, v164, v160, s60\n"
    "v_perm_b32 v221, v172, v168, s60\n"
    "v_mfma_f32_16x16x32_bf16 v[72:75], v[224:227], v[192:195], v[72:75]\n"
    "v_perm_b32 v222, v180, v176, s60\n"
    "v_perm_b32 v223, v188, v184, s60\n"
    "buffer_load_dwordx4 v[136:139], v234, s[28:31], s52 offen\n"
    "v_mfma_f32_16x16x32_bf16 v[84:87], v[228:231], v[192:195], v[84:87]\n"
    "buffer_load_dwordx4 v[140:143], v235, s[28:31], s52 offen\n"
    "s_waitcnt lgkmcnt(0)\n"
    "v_mfma_f32_16x16x32_bf16 v[0:3], v[216:219], v[204:207], v[0:3]\n"
    "v_perm_b32 v224, v165, v161, s59\n"
    "v_perm_b32 v225, v173, v169, s59\n"
    "v_mfma_f32_16x16x32_bf16 v[12:15], v[220:223], v[204:207], v[12:15]\n"
    "v_perm_b32 v226, v181, v177, s59\n"
    "v_perm_b32 v227, v189, v185, s59\n"
    "buffer_load_dwordx4 v[144:147], v236, s[28:31], s52 offen\n"
    "v_mfma_f32_16x16x32_bf16 v[16:19], v[220:223], v[208:211], v[16:19]\n"
    "buffer_load_dwordx4 v[148:151], v237, s[28:31], s52 offen\n"
    "v_mfma_f32_16x16x32_bf16 v[4:7], v[216:219], v[208:211], v[4:7]\n"
    "v_perm_b32 v228, v165, v161, s60\n"
    "v_perm_b32 v229, v173, v169, s60\n"
    "v_mfma_f32_16x16x32_bf16 v[8:11], v[216:219], v[212:215], v[8:11]\n"
    "v_perm_b32 v230, v181, v177, s60\n"
    "v_perm_b32 v231, v189, v185, s60\n"
    "buffer_load_dwordx4 v[152:155], v238, s[28:31], s52 offen\n"
    "v_mfma_f32_16x16x32_bf16 v[20:23], v[220:223], v[212:215], v[20:23]\n"
    "buffer_load_dwordx4 v[156:159], v239, s[28:31], s52 offen\n"
    "v_mfma_f32_16x16x32_bf16 v[32:35], v[224:227], v[212:215], v[32:35]\n"
    "v_perm_b32 v216, v166, v162, s59\n"
    "v_perm_b32 v217, v174, v170, s59\n"
    "v_mfma_f32_16x16x32_bf16 v[44:47], v[228:231], v[212:215], v[44:47]\n"
    "v_perm_b32 v218, v182, v178, s59\n"
    "v_perm_b32 v219, v190, v186, s59\n"
    "v_mfma_f32_16x16x32_bf16 v[40:43], v[228:231], v[208:211], v[40:43]\n"
    "v_mfma_f32_16x16x32_bf16 v[28:31], v[224:227], v[208:211], v[28:31]\n"
    "v_perm_b32 v220, v166, v162, s60\n"
    "v_perm_b32 v221, v174, v170, s60\n"
    "v_mfma_f32_16x16x32_bf16 v[24:27], v[224:227], v[204:207], v[24:27]\n"
    "v_perm_b32 v222, v182, v178, s60\n"
    "v_perm_b32 v223, v190, v186, s60\n"
    "v_mfma_f32_16x16x32_bf16 v[36:39], v[228:231], v[204:207], v[36:39]\n"
    "ds_read_b128 v[192:195], v240 offset:6144\n"
    "v_mfma_f32_16x16x32_bf16 v[48:51], v[216:219], v[204:207], v[48:51]\n"
    "v_perm_b32 v224, v167, v163, s59\n"
    "v_perm_b32 v225, v175, v171, s59\n"
    "v_mfma_f32_16x16x32_bf16 v[60:63], v[220:223], v[204:207], v[60:63]\n"
    "v_perm_b32 v226, v183, v179, s59\n"
    "v_perm_b32 v227, v191, v187, s59\n"
    "v_mfma_f32_16x16x32_bf16 v[64:67], v[220:223], v[208:211], v[64:67]\n"
    "ds_read_b128 v[196:199], v240 offset:8192\n"
    "v_mfma_f32_16x16x32_bf16 v[52:55], v[216:219], v[208:211], v[52:55]\n"
    "v_perm_b32 v228, v167, v163, s60\n"
    "v_perm_b32 v229, v175, v171, s60\n"
    "v_mfma_f32_16x16x32_bf16 v[56:59], v[216:219], v[212:215], v[56:59]\n"
    "v_perm_b32 v230, v183, v179, s60\n"
    "v_perm_b32 v231, v191, v187, s60\n"
    "v_mfma_f32_16x16x32_bf16 v[68:71], v[220:223], v[212:215], v[68:71]\n"
    "ds_read_b128 v[200:203], v240 offset:10240\n"
    "v_mfma_f32_16x16x32_bf16 v[80:83], v[224:227], v[212:215], v[80:83]\n"
    "s_waitcnt vmcnt(8)\n"
    "v_perm_b32 v219, v124, v120, s59\n"
    "v_perm_b32 v218, v116, v112, s59\n"
    "v_mfma_f32_16x16x32_bf16 v[92:95], v[228:231], v[212:215], v[92:95]\n"
    "v_perm_b32 v217, v108, v104, s59\n"
    "v_perm_b32 v216, v100, v96, s59\n"
    "s_add_u32 s52, s52, s53\n"
    "buffer_load_dwordx4 v[160:163], v232, s[28:31], s52 offen\n"
    "v_mfma_f32_16x16x32_bf16 v[88:91], v[228:231], v[208:211], v[88:91]\n"
    "buffer_load_dwordx4 v[164:167], v233, s[28:31], s52 offen\n"
    "v_mfma_f32_16x16x32_bf16 v[76:79], v[224:227], v[208:211], v[76:79]\n"
    "v_perm_b32 v220, v100, v96, s60\n"
    "v_perm_b32 v221, v108, v104, s60\n"
    "v_mfma_f32_16x16x32_bf16 v[72:75], v[224:227], v[204:207], v[72:75]\n"
    "v_perm_b32 v222, v116, v112, s60\n"
    "v_perm_b32 v223, v124, v120, s60\n"
    "buffer_load_dwordx4 v[168:171], v234, s[28:31], s52 offen\n"
    "v_mfma_f32_16x16x32_bf16 v[84:87], v[228:231], v[204:207], v[84:87]\n"
    "buffer_load_dwordx4 v[172:175], v235, s[28:31], s52 offen\n"
    "s_waitcnt lgkmcnt(0)\n"
    "v_mfma_f32_16x16x32_bf16 v[0:3], v[216:219], v[192:195], v[0:3]\n"
    "v_perm_b32 v224, v101, v97, s59\n"
    "v_perm_b32 v225, v109, v105, s59\n"
    "v_mfma_f32_16x16x32_bf16 v[12:15], v[220:223], v[192:195], v[12:15]\n"
    "v_perm_b32 v226, v117, v113, s59\n"
    "v_perm_b32 v227, v125, v121, s59\n"
    "buffer_load_dwordx4 v[176:179], v236, s[28:31], s52 offen\n"
    "v_mfma_f32_16x16x32_bf16 v[16:19], v[220:223], v[196:199], v[16:19]\n"
    "buffer_load_dwordx4 v[180:183], v237, s[28:31], s52 offen\n"
    "v_mfma_f32_16x16x32_bf16 v[4:7], v[216:219], v[196:199], v[4:7]\n"
    "v_perm_b32 v228, v101, v97, s60\n"
    "v_perm_b32 v229, v109, v105, s60\n"
    "v_mfma_f32_16x16x32_bf16 v[8:11], v[216:219], v[200:203], v[8:11]\n"
    "v_perm_b32 v230, v117, v113, s60\n"
    "v_perm_b32 v231, v125, v121, s60\n"
    "buffer_load_dwordx4 v[184:187], v238, s[28:31], s52 offen\n"
    "v_mfma_f32_16x16x32_bf16 v[20:23], v[220:223], v[200:203], v[20:23]\n"
    "buffer_load_dwordx4 v[188:191], v239, s[28:31], s52 offen\n"
    "v_mfma_f32_16x16x32_bf16 v[32:35], v[224:227], v[200:203], v[32:35]\n"
    "v_perm_b32 v216, v102, v98, s59\n"
    "v_perm_b32 v217, v110, v106, s59\n"
    "v_mfma_f32_16x16x32_bf16 v[44:47], v[228:231], v[200:203], v[44:47]\n"
    "v_perm_b32 v218, v118, v114, s59\n"
    "v_perm_b32 v219, v126, v122, s59\n"
    "v_mfma_f32_16x16x32_bf16 v[40:43], v[228:231], v[196:199], v[40:43]\n"
    "v_mfma_f32_16x16x32_bf16 v[28:31], v[224:227], v[196:199], v[28:31]\n"
    "v_perm_b32 v220, v102, v98, s60\n"
    "v_perm_b32 v221, v110, v106, s60\n"
    "v_mfma_f32_16x16x32_bf16 v[24:27], v[224:227], v[192:195], v[24:27]\n"
    "v_perm_b32 v222, v118, v114, s60\n"
    "v_perm_b32 v223, v126, v122, s60\n"
    "v_mfma_f32_16x16x32_bf16 v[36:39], v[228:231], v[192:195], v[36:39]\n"
    "ds_read_b128 v[204:207], v241 offset:6144\n"
    "v_mfma_f32_16x16x32_bf16 v[48:51], v[216:219], v[192:195], v[48:51]\n"
    "v_perm_b32 v224, v103, v99, s59\n"
    "v_perm_b32 v225, v111, v107, s59\n"
    "v_mfma_f32_16x16x32_bf16 v[60:63], v[220:223], v[192:195], v[60:63]\n"
    "v_perm_b32 v226, v119, v115, s59\n"
    "v_perm_b32 v227, v127, v123, s59\n"
    "v_mfma_f32_16x16x32_bf16 v[64:67], v[220:223], v[196:199], v[64:67]\n"
    "ds_read_b128 v[208:211], v241 offset:8192\n"
    "v_mfma_f32_16x16x32_bf16 v[52:55], v[216:219], v[196:199], v[52:55]\n"
    "v_perm_b32 v228, v103, v99, s60\n"
    "v_perm_b32 v229, v111, v107, s60\n"
    "v_mfma_f32_16x16x32_bf16 v[56:59], v[216:219], v[200:203], v[56:59]\n"
    "v_perm_b32 v230, v119, v115, s60\n"
    "v_perm_b32 v231, v127, v123, s60\n"
    "v_mfma_f32_16x16x32_bf16 v[68:71], v[220:223], v[200:203], v[68:71]\n"
    "ds_read_b128 v[212:215], v241 offset:10240\n"
    "v_mfma_f32_16x16x32_bf16 v[80:83], v[224:227], v[200:203], v[80:83]\n"
    "s_waitcnt vmcnt(8)\n"
    "v_perm_b32 v219, v156, v152, s59\n"
    "v_perm_b32 v218, v148, v144, s59\n"
    "v_mfma_f32_16x16x32_bf16 v[92:95], v[228:231], v[200:203], v[92:95]\n"
    "v_perm_b32 v217, v140, v136, s59\n"
    "v_perm_b32 v216, v132, v128, s59\n"
    "s_add_u32 s52, s52, s53\n"
    "buffer_load_dwordx4 v[96:99], v232, s[28:31], s52 offen\n"
    "v_mfma_f32_16x16x32_bf16 v[88:91], v[228:231], v[196:199], v[88:91]\n"
    "buffer_load_dwordx4 v[100:103], v233, s[28:31], s52 offen\n"
    "v_mfma_f32_16x16x32_bf16 v[76:79], v[224:227], v[196:199], v[76:79]\n"
    "v_perm_b32 v220, v132, v128, s60\n"
    "v_perm_b32 v221, v140, v136, s60\n"
    "v_mfma_f32_16x16x32_bf16 v[72:75], v[224:227], v[192:195], v[72:75]\n"
    "v_perm_b32 v222, v148, v144, s60\n"
    "v_perm_b32 v223, v156, v152, s60\n"
    "buffer_load_dwordx4 v[104:107], v234, s[28:31], s52 offen\n"
    "v_mfma_f32_16x16x32_bf16 v[84:87], v[228:231], v[192:195], v[84:87]\n"
    "buffer_load_dwordx4 v[108:111], v235, s[28:31], s52 offen\n"
    "s_waitcnt lgkmcnt(0)\n"
    "v_mfma_f32_16x16x32_bf16 v[0:3], v[216:219], v[204:207], v[0:3]\n"
    "v_perm_b32 v224, v133, v129, s59\n"
    "v_perm_b32 v225, v141, v137, s59\n"
    "v_mfma_f32_16x16x32_bf16 v[12:15], v[220:223], v[204:207], v[12:15]\n"
    "v_perm_b32 v226, v149, v145, s59\n"
    "v_perm_b32 v227, v157, v153, s59\n"
    "buffer_load_dwordx4 v[112:115], v236, s[28:31], s52 offen\n"
    "v_mfma_f32_16x16x32_bf16 v[16:19], v[220:223], v[208:211], v[16:19]\n"
    "buffer_load_dwordx4 v[116:119], v237, s[28:31], s52 offen\n"
    "v_mfma_f32_16x16x32_bf16 v[4:7], v[216:219], v[208:211], v[4:7]\n"
    "v_perm_b32 v228, v133, v129, s60\n"
    "v_perm_b32 v229, v141, v137, s60\n"
    "v_mfma_f32_16x16x32_bf16 v[8:11], v[216:219], v[212:215], v[8:11]\n"
    "v_perm_b32 v230, v149, v145, s60\n"
    "v_perm_b32 v231, v157, v153, s60\n"
    "buffer_load_dwordx4 v[120:123], v238, s[28:31], s52 offen\n"
    "v_mfma_f32_16x16x32_bf16 v[20:23], v[220:223], v[212:215], v[20:23]\n"
    "buffer_load_dwordx4 v[124:127], v239, s[28:31], s52 offen\n"
    "v_mfma_f32_16x16x32_bf16 v[32:35], v[224:227], v[212:215], v[32:35]\n"
    "v_perm_b32 v216, v134, v130, s59\n"
    "v_perm_b32 v217, v142, v138, s59\n"
    "v_mfma_f32_16x16x32_bf16 v[44:47], v[228:231], v[212:215], v[44:47]\n"
    "v_perm_b32 v218, v150, v146, s59\n"
    "v_perm_b32 v219, v158, v154, s59\n"
    "v_mfma_f32_16x16x32_bf16 v[40:43], v[228:231], v[208:211], v[40:43]\n"
    "v_add_u32 v240, s54, v240\n"
    "v_mfma_f32_16x16x32_bf16 v[28:31], v[224:227], v[208:211], v[28:31]\n"
    "v_perm_b32 v220, v134, v130, s60\n"
    "v_perm_b32 v221, v142, v138, s60\n"
    "v_mfma_f32_16x16x32_bf16 v[24:27], v[224:227], v[204:207], v[24:27]\n"
    "v_perm_b32 v222, v150, v146, s60\n"
    "v_perm_b32 v223, v158, v154, s60\n"
    "v_mfma_f32_16x16x32_bf16 v[36:39], v[228:231], v[204:207], v[36:39]\n"
    "ds_read_b128 v[192:195], v240 offset:0\n"
    "v_mfma_f32_16x16x32_bf16 v[48:51], v[216:219], v[204:207], v[48:51]\n"
    "v_perm_b32 v224, v135, v131, s59\n"
    "v_perm_b32 v225, v143, v139, s59\n"
    "v_mfma_f32_16x16x32_bf16 v[60:63], v[220:223], v[204:207], v[60:63]\n"
    "v_perm_b32 v226, v151, v147, s59\n"
    "v_perm_b32 v227, v159, v155, s59\n"
    "v_mfma_f32_16x16x32_bf16 v[64:67], v[220:223], v[208:211], v[64:67]\n"
    "ds_read_b128 v[196:199], v240 offset:2048\n"
    "v_mfma_f32_16x16x32_bf16 v[52:55], v[216:219], v[208:211], v[52:55]\n"
    "v_perm_b32 v228, v135, v131, s60\n"
    "v_perm_b32 v229, v143, v139, s60\n"
    "v_mfma_f32_16x16x32_bf16 v[56:59], v[216:219], v[212:215], v[56:59]\n"
    "v_perm_b32 v230, v151, v147, s60\n"
    "v_perm_b32 v231, v159, v155, s60\n"
    "v_mfma_f32_16x16x32_bf16 v[68:71], v[220:223], v[212:215], v[68:71]\n"
    "ds_read_b128 v[200:203], v240 offset:4096\n"
    "v_mfma_f32_16x16x32_bf16 v[80:83], v[224:227], v[212:215], v[80:83]\n"
    "s_waitcnt vmcnt(8)\n"
    "v_perm_b32 v219, v188, v184, s59\n"
    "v_perm_b32 v218, v180, v176, s59\n"
    "v_mfma_f32_16x16x32_bf16 v[92:95], v[228:231], v[212:215], v[92:95]\n"
    "v_perm_b32 v217, v172, v168, s59\n"
    "v_perm_b32 v216, v164, v160, s59\n"
    "s_add_u32 s52, s52, s53\n"
    "buffer_load_dwordx4 v[128:131], v232, s[28:31], s52 offen\n"
    "v_mfma_f32_16x16x32_bf16 v[88:91], v[228:231], v[208:211], v[88:91]\n"
    "buffer_load_dwordx4 v[132:135], v233, s[28:31], s52 offen\n"
    "v_mfma_f32_16x16x32_bf16 v[76:79], v[224:227], v[208:211], v[76:79]\n"
    "v_perm_b32 v220, v164, v160, s60\n"
    "v_perm_b32 v221, v172, v168, s60\n"
    "v_mfma_f32_16x16x32_bf16 v[72:75], v[224:227], v[204:207], v[72:75]\n"
    "v_perm_b32 v222, v180, v176, s60\n"
    "v_perm_b32 v223, v188, v184, s60\n"
    "buffer_load_dwordx4 v[136:139], v234, s[28:31], s52 offen\n"
    "v_mfma_f32_16x16x32_bf16 v[84:87], v[228:231], v[204:207], v[84:87]\n"
    "buffer_load_dwordx4 v[140:143], v235, s[28:31], s52 offen\n"
    "s_waitcnt lgkmcnt(0)\n"
    "v_mfma_f32_16x16x32_bf16 v[0:3], v[216:219], v[192:195], v[0:3]\n"
    "v_perm_b32 v224, v165, v161, s59\n"
    "v_perm_b32 v225, v173, v169, s59\n"
    "v_mfma_f32_16x16x32_bf16 v[12:15], v[220:223], v[192:195], v[12:15]\n"
    "v_perm_b32 v226, v181, v177, s59\n"
    "v_perm_b32 v227, v189, v185, s59\n"
    "buffer_load_dwordx4 v[144:147], v236, s[28:31], s52 offen\n"
    "v_mfma_f32_16x16x32_bf16 v[16:19], v[220:223], v[196:199], v[16:19]\n"
    "buffer_load_dwordx4 v[148:151], v237, s[28:31], s52 offen\n"
    "v_mfma_f32_16x16x32_bf16 v[4:7], v[216:219], v[196:199], v[4:7]\n"
    "v_perm_b32 v228, v165, v161, s60\n"
    "v_perm_b32 v229, v173, v169, s60\n"
    "v_mfma_f32_16x16x32_bf16 v[8:11], v[216:219], v[200:203], v[8:11]\n"
    "v_perm_b32 v230, v181, v177, s60\n"
    "v_perm_b32 v231, v189, v185, s60\n"
    "buffer_load_dwordx4 v[152:155], v238, s[28:31], s52 offen\n"
    "v_mfma_f32_16x16x32_bf16 v[20:23], v[220:223], v[200:203], v[20:23]\n"
    "buffer_load_dwordx4 v[156:159], v239, s[28:31], s52 offen\n"
    "v_mfma_f32_16x16x32_bf16 v[32:35], v[224:227], v[200:203], v[32:35]\n"
    "v_perm_b32 v216, v166, v162, s59\n"
    "v_perm_b32 v217, v174, v170, s59\n"
    "v_mfma_f32_16x16x32_bf16 v[44:47], v[228:231], v[200:203], v[44:47]\n"
    "v_perm_b32 v218, v182, v178, s59\n"
    "v_perm_b32 v219, v190, v186, s59\n"
    "v_mfma_f32_16x16x32_bf16 v[40:43], v[228:231], v[196:199], v[40:43]\n"
    "v_add_u32 v241, s54, v241\n"
    "v_mfma_f32_16x16x32_bf16 v[28:31], v[224:227], v[196:199], v[28:31]\n"
    "v_perm_b32 v220, v166, v162, s60\n"
    "v_perm_b32 v221, v174, v170, s60\n"
    "v_mfma_f32_16x16x32_bf16 v[24:27], v[224:227], v[192:195], v[24:27]\n"
    "v_perm_b32 v222, v182, v178, s60\n"
    "v_perm_b32 v223, v190, v186, s60\n"
    "v_mfma_f32_16x16x32_bf16 v[36:39], v[228:231], v[192:195], v[36:39]\n"
    "ds_read_b128 v[204:207], v241 offset:0\n"
    "v_mfma_f32_16x16x32_bf16 v[48:51], v[216:219], v[192:195], v[48:51]\n"
    "v_perm_b32 v224, v167, v163, s59\n"
    "v_perm_b32 v225, v175, v171, s59\n"
    "v_mfma_f32_16x16x32_bf16 v[60:63], v[220:223], v[192:195], v[60:63]\n"
    "v_perm_b32 v226, v183, v179, s59\n"
    "v_perm_b32 v227, v191, v187, s59\n"
    "v_mfma_f32_16x16x32_bf16 v[64:67], v[220:223], v[196:199], v[64:67]\n"
    "ds_read_b128 v[208:211], v241 offset:2048\n"
    "v_mfma_f32_16x16x32_bf16 v[52:55], v[216:219], v[196:199], v[52:55]\n"
    "v_perm_b32 v228, v167, v163, s60\n"
    "v_perm_b32 v229, v175, v171, s60\n"
    "v_mfma_f32_16x16x32_bf16 v[56:59], v[216:219], v[200:203], v[56:59]\n"
    "v_perm_b32 v230, v183, v179, s60\n"
    "v_perm_b32 v231, v191, v187, s60\n"
    "v_mfma_f32_16x16x32_bf16 v[68:71], v[220:223], v[200:203], v[68:71]\n"
    "ds_read_b128 v[212:215], v241 offset:4096\n"
    "v_mfma_f32_16x16x32_bf16 v[80:83], v[224:227], v[200:203], v[80:83]\n"
    "s_waitcnt vmcnt(8)\n"
    "v_perm_b32 v219, v124, v120, s59\n"
    "v_perm_b32 v218, v116, v112, s59\n"
    "v_mfma_f32_16x16x32_bf16 v[92:95], v[228:231], v[200:203], v[92:95]\n"
    "v_perm_b32 v217, v108, v104, s59\n"
    "v_perm_b32 v216, v100, v96, s59\n"
    "s_add_u32 s52, s52, s53\n"
    "buffer_load_dwordx4 v[160:163], v232, s[28:31], s52 offen\n"
    "v_mfma_f32_16x16x32_bf16 v[88:91], v[228:231], v[196:199], v[88:91]\n"
    "buffer_load_dwordx4 v[164:167], v233, s[28:31], s52 offen\n"
    "v_mfma_f32_16x16x32_bf16 v[76:79], v[224:227], v[196:199], v[76:79]\n"
    "v_perm_b32 v220, v100, v96, s60\n"
    "v_perm_b32 v221, v108, v104, s60\n"
    "v_mfma_f32_16x16x32_bf16 v[72:75], v[224:227], v[192:195], v[72:75]\n"
    "v_perm_b32 v222, v116, v112, s60\n"
    "v_perm_b32 v223, v124, v120, s60\n"
    "buffer_load_dwordx4 v[168:171], v234, s[28:31], s52 offen\n"
    "v_mfma_f32_16x16x32_bf16 v[84:87], v[228:231], v[192:195], v[84:87]\n"
    "buffer_load_dwordx4 v[172:175], v235, s[28:31], s52 offen\n"
    "s_waitcnt lgkmcnt(0)\n"
    "v_mfma_f32_16x16x32_bf16 v[0:3], v[216:219], v[204:207], v[0:3]\n"
    "v_perm_b32 v224, v101, v97, s59\n"
    "v_perm_b32 v225, v109, v105, s59\n"
    "v_mfma_f32_16x16x32_bf16 v[12:15], v[220:223], v[204:207], v[12:15]\n"
    "v_perm_b32 v226, v117, v113, s59\n"
    "v_perm_b32 v227, v125, v121, s59\n"
    "buffer_load_dwordx4 v[176:179], v236, s[28:31], s52 offen\n"
    "v_mfma_f32_16x16x32_bf16 v[16:19], v[220:223], v[208:211], v[16:19]\n"
    "buffer_load_dwordx4 v[180:183], v237, s[28:31], s52 offen\n"
    "v_mfma_f32_16x16x32_bf16 v[4:7], v[216:219], v[208:211], v[4:7]\n"
    "v_perm_b32 v228, v101, v97, s60\n"
    "v_perm_b32 v229, v109, v105, s60\n"
    "v_mfma_f32_16x16x32_bf16 v[8:11], v[216:219], v[212:215], v[8:11]\n"
    "v_perm_b32 v230, v117, v113, s60\n"
    "v_perm_b32 v231, v125, v121, s60\n"
    "buffer_load_dwordx4 v[184:187], v238, s[28:31], s52 offen\n"
    "v_mfma_f32_16x16x32_bf16 v[20:23], v[220:223], v[212:215], v[20:23]\n"
    "buffer_load_dwordx4 v[188:191], v239, s[28:31], s52 offen\n"
    "v_mfma_f32_16x16x32_bf16 v[32:35], v[224:227], v[212:215], v[32:35]\n"
    "v_perm_b32 v216, v102, v98, s59\n"
    "v_perm_b32 v217, v110, v106, s59\n"
    "v_mfma_f32_16x16x32_bf16 v[44:47], v[228:231], v[212:215], v[44:47]\n"
    "v_perm_b32 v218, v118, v114, s59\n"
    "v_perm_b32 v219, v126, v122, s59\n"
    "v_mfma_f32_16x16x32_bf16 v[40:43], v[228:231], v[208:211], v[40:43]\n"
    "v_mfma_f32_16x16x32_bf16 v[28:31], v[224:227], v[208:211], v[28:31]\n"
    "v_perm_b32 v220, v102, v98, s60\n"
    "v_perm_b32 v221, v110, v106, s60\n"
    "v_mfma_f32_16x16x32_bf16 v[24:27], v[224:227], v[204:207], v[24:27]\n"
    "v_perm_b32 v222, v118, v114, s60\n"
    "v_perm_b32 v223, v126, v122, s60\n"
    "v_mfma_f32_16x16x32_bf16 v[36:39], v[228:231], v[204:207], v[36:39]\n"
    "ds_read_b128 v[192:195], v240 offset:6144\n"
    "v_mfma_f32_16x16x32_bf16 v[48:51], v[216:219], v[204:207], v[48:51]\n"
    "v_perm_b32 v224, v103, v99, s59\n"
    "v_perm_b32 v225, v111, v107, s59\n"
    "v_mfma_f32_16x16x32_bf16 v[60:63], v[220:223], v[204:207], v[60:63]\n"
    "v_perm_b32 v226, v119, v115, s59\n"
    "v_perm_b32 v227, v127, v123, s59\n"
    "v_mfma_f32_16x16x32_bf16 v[64:67], v[220:223], v[208:211], v[64:67]\n"
    "ds_read_b128 v[196:199], v240 offset:8192\n"
    "v_mfma_f32_16x16x32_bf16 v[52:55], v[216:219], v[208:211], v[52:55]\n"
    "v_perm_b32 v228, v103, v99, s60\n"
    "v_perm_b32 v229, v111, v107, s60\n"
    "v_mfma_f32_16x16x32_bf16 v[56:59], v[216:219], v[212:215], v[56:59]\n"
    "v_perm_b32 v230, v119, v115, s60\n"
    "v_perm_b32 v231, v127, v123, s60\n"
    "v_mfma_f32_16x16x32_bf16 v[68:71], v[220:223], v[212:215], v[68:71]\n"
    "ds_read_b128 v[200:203], v240 offset:10240\n"
    "v_mfma_f32_16x16x32_bf16 v[80:83], v[224:227], v[212:215], v[80:83]\n"
    "s_waitcnt vmcnt(8)\n"
    "v_perm_b32 v219, v156, v152, s59\n"
    "v_perm_b32 v218, v148, v144, s59\n"
    "v_mfma_f32_16x16x32_bf16 v[92:95], v[228:231], v[212:215], v[92:95]\n"
    "v_perm_b32 v217, v140, v136, s59\n"
    "v_perm_b32 v216, v132, v128, s59\n"
    "s_add_u32 s52, s52, s53\n"
    "buffer_load_dwordx4 v[96:99], v232, s[28:31], s52 offen\n"
    "v_mfma_f32_16x16x32_bf16 v[88:91], v[228:231], v[208:211], v[88:91]\n"
    "buffer_load_dwordx4 v[100:103], v233, s[28:31], s52 offen\n"
    "v_mfma_f32_16x16x32_bf16 v[76:79], v[224:227], v[208:211], v[76:79]\n"
    "v_perm_b32 v220, v132, v128, s60\n"
    "v_perm_b32 v221, v140, v136, s60\n"
    "v_mfma_f32_16x16x32_bf16 v[72:75], v[224:227], v[204:207], v[72:75]\n"
    "v_perm_b32 v222, v148, v144, s60\n"
    "v_perm_b32 v223, v156, v152, s60\n"
    "buffer_load_dwordx4 v[104:107], v234, s[28:31], s52 offen\n"
    "v_mfma_f32_16x16x32_bf16 v[84:87], v[228:231], v[204:207], v[84:87]\n"
    "buffer_load_dwordx4 v[108:111], v235, s[28:31], s52 offen\n"
    "s_waitcnt lgkmcnt(0)\n"
    "v_mfma_f32_16x16x32_bf16 v[0:3], v[216:219], v[192:195], v[0:3]\n"
    "v_perm_b32 v224, v133, v129, s59\n"
    "v_perm_b32 v225, v141, v137, s59\n"
    "v_mfma_f32_16x16x32_bf16 v[12:15], v[220:223], v[192:195], v[12:15]\n"
    "v_perm_b32 v226, v149, v145, s59\n"
    "v_perm_b32 v227, v157, v153, s59\n"
    "buffer_load_dwordx4 v[112:115], v236, s[28:31], s52 offen\n"
    "v_mfma_f32_16x16x32_bf16 v[16:19], v[220:223], v[196:199], v[16:19]\n"
    "buffer_load_dwordx4 v[116:119], v237, s[28:31], s52 offen\n"
    "v_mfma_f32_16x16x32_bf16 v[4:7], v[216:219], v[196:199], v[4:7]\n"
    "v_perm_b32 v228, v133, v129, s60\n"
    "v_perm_b32 v229, v141, v137, s60\n"
    "v_mfma_f32_16x16x32_bf16 v[8:11], v[216:219], v[200:203], v[8:11]\n"
    "v_perm_b32 v230, v149, v145, s60\n"
    "v_perm_b32 v231, v157, v153, s60\n"
    "buffer_load_dwordx4 v[120:123], v238, s[28:31], s52 offen\n"
    "v_mfma_f32_16x16x32_bf16 v[20:23], v[220:223], v[200:203], v[20:23]\n"
    "buffer_load_dwordx4 v[124:127], v239, s[28:31], s52 offen\n"
    "v_mfma_f32_16x16x32_bf16 v[32:35], v[224:227], v[200:203], v[32:35]\n"
    "v_perm_b32 v216, v134, v130, s59\n"
    "v_perm_b32 v217, v142, v138, s59\n"
    "v_mfma_f32_16x16x32_bf16 v[44:47], v[228:231], v[200:203], v[44:47]\n"
    "v_perm_b32 v218, v150, v146, s59\n"
    "v_perm_b32 v219, v158, v154, s59\n"
    "v_mfma_f32_16x16x32_bf16 v[40:43], v[228:231], v[196:199], v[40:43]\n"
    "v_mfma_f32_16x16x32_bf16 v[28:31], v[224:227], v[196:199], v[28:31]\n"
    "v_perm_b32 v220, v134, v130, s60\n"
    "v_perm_b32 v221, v142, v138, s60\n"
    "v_mfma_f32_16x16x32_bf16 v[24:27], v[224:227], v[192:195], v[24:27]\n"
    "v_perm_b32 v222, v150, v146, s60\n"
    "v_perm_b32 v223, v158, v154, s60\n"
    "v_mfma_f32_16x16x32_bf16 v[36:39], v[228:231], v[192:195], v[36:39]\n"
    "ds_read_b128 v[204:207], v241 offset:6144\n"
    "v_mfma_f32_16x16x32_bf16 v[48:51], v[216:219], v[192:195], v[48:51]\n"
    "v_perm_b32 v224, v135, v131, s59\n"
    "v_perm_b32 v225, v143, v139, s59\n"
    "v_mfma_f32_16x16x32_bf16 v[60:63], v[220:223], v[192:195], v[60:63]\n"
    "v_perm_b32 v226, v151, v147, s59\n"
    "v_perm_b32 v227, v159, v155, s59\n"
    "v_mfma_f32_16x16x32_bf16 v[64:67], v[220:223], v[196:199], v[64:67]\n"
    "ds_read_b128 v[208:211], v241 offset:8192\n"
    "v_mfma_f32_16x16x32_bf16 v[52:55], v[216:219], v[196:199], v[52:55]\n"
    "v_perm_b32 v228, v135, v131, s60\n"
    "v_perm_b32 v229, v143, v139, s60\n"
    "v_mfma_f32_16x16x32_bf16 v[56:59], v[216:219], v[200:203], v[56:59]\n"
    "v_perm_b32 v230, v151, v147, s60\n"
    "v_perm_b32 v231, v159, v155, s60\n"
    "v_mfma_f32_16x16x32_bf16 v[68:71], v[220:223], v[200:203], v[68:71]\n"
    "ds_read_b128 v[212:215], v241 offset:10240\n"
    "v_mfma_f32_16x16x32_bf16 v[80:83], v[224:227], v[200:203], v[80:83]\n"
    "s_waitcnt vmcnt(8)\n"
    "v_perm_b32 v219, v188, v184, s59\n"
    "v_perm_b32 v218, v180, v176, s59\n"
    "v_mfma_f32_16x16x32_bf16 v[92:95], v[228:231], v[200:203], v[92:95]\n"
    "v_perm_b32 v217, v172, v168, s59\n"
    "v_perm_b32 v216, v164, v160, s59\n"
    "s_add_u32 s52, s52, s53\n"
    "buffer_load_dwordx4 v[128:131], v232, s[28:31], s52 offen\n"
    "v_mfma_f32_16x16x32_bf16 v[88:91], v[228:231], v[196:199], v[88:91]\n"
    "buffer_load_dwordx4 v[132:135], v233, s[28:31], s52 offen\n"
    "v_mfma_f32_16x16x32_bf16 v[76:79], v[224:227], v[196:199], v[76:79]\n"
    "v_perm_b32 v220, v164, v160, s60\n"
    "v_perm_b32 v221, v172, v168, s60\n"
    "v_mfma_f32_16x16x32_bf16 v[72:75], v[224:227], v[192:195], v[72:75]\n"
    "v_perm_b32 v222, v180, v176, s60\n"
    "v_perm_b32 v223, v188, v184, s60\n"
    "buffer_load_dwordx4 v[136:139], v234, s[28:31], s52 offen\n"
    "v_mfma_f32_16x16x32_bf16 v[84:87], v[228:231], v[192:195], v[84:87]\n"
    "buffer_load_dwordx4 v[140:143], v235, s[28:31], s52 offen\n"
    "s_waitcnt lgkmcnt(0)\n"
    "v_mfma_f32_16x16x32_bf16 v[0:3], v[216:219], v[204:207], v[0:3]\n"
    "v_perm_b32 v224, v165, v161, s59\n"
    "v_perm_b32 v225, v173, v169, s59\n"
    "v_mfma_f32_16x16x32_bf16 v[12:15], v[220:223], v[204:207], v[12:15]\n"
    "v_perm_b32 v226, v181, v177, s59\n"
    "v_perm_b32 v227, v189, v185, s59\n"
    "buffer_load_dwordx4 v[144:147], v236, s[28:31], s52 offen\n"
    "v_mfma_f32_16x16x32_bf16 v[16:19], v[220:223], v[208:211], v[16:19]\n"
    "buffer_load_dwordx4 v[148:151], v237, s[28:31], s52 offen\n"
    "v_mfma_f32_16x16x32_bf16 v[4:7], v[216:219], v[208:211], v[4:7]\n"
    "v_perm_b32 v228, v165, v161, s60\n"
    "v_perm_b32 v229, v173, v169, s60\n"
    "v_mfma_f32_16x16x32_bf16 v[8:11], v[216:219], v[212:215], v[8:11]\n"
    "v_perm_b32 v230, v181, v177, s60\n"
    "v_perm_b32 v231, v189, v185, s60\n"
    "buffer_load_dwordx4 v[152:155], v238, s[28:31], s52 offen\n"
    "v_mfma_f32_16x16x32_bf16 v[20:23], v[220:223], v[212:215], v[20:23]\n"
    "buffer_load_dwordx4 v[156:159], v239, s[28:31], s52 offen\n"
    "v_mfma_f32_16x16x32_bf16 v[32:35], v[224:227], v[212:215], v[32:35]\n"
    "v_perm_b32 v216, v166, v162, s59\n"
    "v_perm_b32 v217, v174, v170, s59\n"
    "v_mfma_f32_16x16x32_bf16 v[44:47], v[228:231], v[212:215], v[44:47]\n"
    "v_perm_b32 v218, v182, v178, s59\n"
    "v_perm_b32 v219, v190, v186, s59\n"
    "v_mfma_f32_16x16x32_bf16 v[40:43], v[228:231], v[208:211], v[40:43]\n"
    "v_add_u32 v240, s54, v240\n"
    "v_mfma_f32_16x16x32_bf16 v[28:31], v[224:227], v[208:211], v[28:31]\n"
    "v_perm_b32 v220, v166, v162, s60\n"
    "v_perm_b32 v221, v174, v170, s60\n"
    "v_mfma_f32_16x16x32_bf16 v[24:27], v[224:227], v[204:207], v[24:27]\n"
    "v_perm_b32 v222, v182, v178, s60\n"
    "v_perm_b32 v223, v190, v186, s60\n"
    "v_mfma_f32_16x16x32_bf16 v[36:39], v[228:231], v[204:207], v[36:39]\n"
    "ds_read_b128 v[192:195], v240 offset:0\n"
    "v_mfma_f32_16x16x32_bf16 v[48:51], v[216:219], v[204:207], v[48:51]\n"
    "v_perm_b32 v224, v167, v163, s59\n"
    "v_perm_b32 v225, v175, v171, s59\n"
    "v_mfma_f32_16x16x32_bf16 v[60:63], v[220:223], v[204:207], v[60:63]\n"
    "v_perm_b32 v226, v183, v179, s59\n"
    "v_perm_b32 v227, v191, v187, s59\n"
    "v_mfma_f32_16x16x32_bf16 v[64:67], v[220:223], v[208:211], v[64:67]\n"
    "ds_read_b128 v[196:199], v240 offset:2048\n"
    "v_mfma_f32_16x16x32_bf16 v[52:55], v[216:219], v[208:211], v[52:55]\n"
    "v_perm_b32 v228, v167, v163, s60\n"
    "v_perm_b32 v229, v175, v171, s60\n"
    "v_mfma_f32_16x16x32_bf16 v[56:59], v[216:219], v[212:215], v[56:59]\n"
    "v_perm_b32 v230, v183, v179, s60\n"
    "v_perm_b32 v231, v191, v187, s60\n"
    "v_mfma_f32_16x16x32_bf16 v[68:71], v[220:223], v[212:215], v[68:71]\n"
    "ds_read_b128 v[200:203], v240 offset:4096\n"
    "v_mfma_f32_16x16x32_bf16 v[80:83], v[224:227], v[212:215], v[80:83]\n"
    "s_waitcnt vmcnt(8)\n"
    "v_perm_b32 v219, v124, v120, s59\n"
    "v_perm_b32 v218, v116, v112, s59\n"
    "v_mfma_f32_16x16x32_bf16 v[92:95], v[228:231], v[212:215], v[92:95]\n"
    "v_perm_b32 v217, v108, v104, s59\n"
    "v_perm_b32 v216, v100, v96, s59\n"
    "s_add_u32 s52, s52, s53\n"
    "buffer_load_dwordx4 v[160:163], v232, s[28:31], s52 offen\n"
    "v_mfma_f32_16x16x32_bf16 v[88:91], v[228:231], v[208:211], v[88:91]\n"
    "buffer_load_dwordx4 v[164:167], v233, s[28:31], s52 offen\n"
    "v_mfma_f32_16x16x32_bf16 v[76:79], v[224:227], v[208:211], v[76:79]\n"
    "v_perm_b32 v220, v100, v96, s60\n"
    "v_perm_b32 v221, v108, v104, s60\n"
    "v_mfma_f32_16x16x32_bf16 v[72:75], v[224:227], v[204:207], v[72:75]\n"
    "v_perm_b32 v222, v116, v112, s60\n"
    "v_perm_b32 v223, v124, v120, s60\n"
    "buffer_load_dwordx4 v[168:171], v234, s[28:31], s52 offen\n"
    "v_mfma_f32_16x16x32_bf16 v[84:87], v[228:231], v[204:207], v[84:87]\n"
    "buffer_load_dwordx4 v[172:175], v235, s[28:31], s52 offen\n"
    "s_waitcnt lgkmcnt(0)\n"
    "v_mfma_f32_16x16x32_bf16 v[0:3], v[216:219], v[192:195], v[0:3]\n"
    "v_perm_b32 v224, v101, v97, s59\n"
    "v_perm_b32 v225, v109, v105, s59\n"
    "v_mfma_f32_16x16x32_bf16 v[12:15], v[220:223], v[192:195], v[12:15]\n"
    "v_perm_b32 v226, v117, v113, s59\n"
    "v_perm_b32 v227, v125, v121, s59\n"
    "buffer_load_dwordx4 v[176:179], v236, s[28:31], s52 offen\n"
    "v_mfma_f32_16x16x32_bf16 v[16:19], v[220:223], v[196:199], v[16:19]\n"
    "buffer_load_dwordx4 v[180:183], v237, s[28:31], s52 offen\n"
    "s_cmp_eq_u32 s61, 0\n"
    "s_cbranch_scc1 .Lqa_17\n"
    "s_mov_b64 exec, 1\n"
    "ds_add_u32 v253, v252 offset:0\n"
    "s_mov_b64 exec, -1\n"
    "s_add_u32 s55, s55, 4\n"
    ".Lqa_17:\n"
    "v_mfma_f32_16x16x32_bf16 v[4:7], v[216:219], v[196:199], v[4:7]\n"
    "v_perm_b32 v228, v101, v97, s60\n"
    "v_perm_b32 v229, v109, v105, s60\n"
    "v_mfma_f32_16x16x32_bf16 v[8:11], v[216:219], v[200:203], v[8:11]\n"
    "v_perm_b32 v230, v117, v113, s60\n"
    "v_perm_b32 v231, v125, v121, s60\n"
    "buffer_load_dwordx4 v[184:187], v238, s[28:31], s52 offen\n"
    "v_mfma_f32_16x16x32_bf16 v[20:23], v[220:223], v[200:203], v[20:23]\n"
    "buffer_load_dwordx4 v[188:191], v239, s[28:31], s52 offen\n"
    "v_mfma_f32_16x16x32_bf16 v[32:35], v[224:227], v[200:203], v[32:35]\n"
    "v_perm_b32 v216, v102, v98, s59\n"
    "v_perm_b32 v217, v110, v106, s59\n"
    "v_mfma_f32_16x16x32_bf16 v[44:47], v[228:231], v[200:203], v[44:47]\n"
    "v_perm_b32 v218, v118, v114, s59\n"
    "v_perm_b32 v219, v126, v122, s59\n"
    "v_mfma_f32_16x16x32_bf16 v[40:43], v[228:231], v[196:199], v[40:43]\n"
    "v_add_u32 v241, s54, v241\n"
    "v_mfma_f32_16x16x32_bf16 v[28:31], v[224:227], v[196:199], v[28:31]\n"
    "v_perm_b32 v220, v102, v98, s60\n"
    "v_perm_b32 v221, v110, v106, s60\n"
    "v_mfma_f32_16x16x32_bf16 v[24:27], v[224:227], v[192:195], v[24:27]\n"
    "v_perm_b32 v222, v118, v114, s60\n"
    "v_perm_b32 v223, v126, v122, s60\n"
    "v_mfma_f32_16x16x32_bf16 v[36:39], v[228:231], v[192:195], v[36:39]\n"
    "ds_read_b128 v[204:207], v241 offset:0\n"
    "v_mfma_f32_16x16x32_bf16 v[48:51], v[216:219], v[192:195], v[48:51]\n"
    "v_perm_b32 v224, v103, v99, s59\n"
    "v_perm_b32 v225, v111, v107, s59\n"
    "v_mfma_f32_16x16x32_bf16 v[60:63], v[220:223], v[192:195], v[60:63]\n"
    "v_perm_b32 v226, v119, v115, s59\n"
    "v_perm_b32 v227, v127, v123, s59\n"
    "v_mfma_f32_16x16x32_bf16 v[64:67], v[220:223], v[196:199], v[64:67]\n"
    "ds_read_b128 v[208:211], v241 offset:2048\n"
    "v_mfma_f32_16x16x32_bf16 v[52:55], v[216:219], v[196:199], v[52:55]\n"
    "v_perm_b32 v228, v103, v99, s60\n"
    "v_perm_b32 v229, v111, v107, s60\n"
    "v_mfma_f32_16x16x32_bf16 v[56:59], v[216:219], v[200:203], v[56:59]\n"
    "v_perm_b32 v230, v119, v115, s60\n"
    "v_perm_b32 v231, v127, v123, s60\n"
    "v_mfma_f32_16x16x32_bf16 v[68:71], v[220:223], v[200:203], v[68:71]\n"
    "ds_read_b128 v[212:215], v241 offset:4096\n"
    "v_mfma_f32_16x16x32_bf16 v[80:83], v[224:227], v[200:203], v[80:83]\n"
    "s_waitcnt vmcnt(8)\n"
    "v_perm_b32 v219, v156, v152, s59\n"
    "v_perm_b32 v218, v148, v144, s59\n"
    "v_mfma_f32_16x16x32_bf16 v[92:95], v[228:231], v[200:203], v[92:95]\n"
    "v_perm_b32 v217, v140, v136, s59\n"
    "v_perm_b32 v216, v132, v128, s59\n"
    "s_add_u32 s52, s52, s53\n"
    "buffer_load_dwordx4 v[96:99], v232, s[28:31], s52 offen\n"
    "v_mfma_f32_16x16x32_bf16 v[88:91], v[228:231], v[196:199], v[88:91]\n"
    "buffer_load_dwordx4 v[100:103], v233, s[28:31], s52 offen\n"
    "v_mfma_f32_16x16x32_bf16 v[76:79], v[224:227], v[196:199], v[76:79]\n"
    "v_perm_b32 v220, v132, v128, s60\n"
    "v_perm_b32 v221, v140, v136, s60\n"
    "v_mfma_f32_16x16x32_bf16 v[72:75], v[224:227], v[192:195], v[72:75]\n"
    "v_perm_b32 v222, v148, v144, s60\n"
    "v_perm_b32 v223, v156, v152, s60\n"
    "buffer_load_dwordx4 v[104:107], v234, s[28:31], s52 offen\n"
    "v_mfma_f32_16x16x32_bf16 v[84:87], v[228:231], v[192:195], v[84:87]\n"
    "buffer_load_dwordx4 v[108:111], v235, s[28:31], s52 offen\n"
    "s_waitcnt lgkmcnt(0)\n"
    "v_mfma_f32_16x16x32_bf16 v[0:3], v[216:219], v[204:207], v[0:3]\n"
    "v_perm_b32 v224, v133, v129, s59\n"
    "v_perm_b32 v225, v141, v137, s59\n"
    "v_mfma_f32_16x16x32_bf16 v[12:15], v[220:223], v[204:207], v[12:15]\n"
    "v_perm_b32 v226, v149, v145, s59\n"
    "v_perm_b32 v227, v157, v153, s59\n"
    "buffer_load_dwordx4 v[112:115], v236, s[28:31], s52 offen\n"
    "v_mfma_f32_16x16x32_bf16 v[16:19], v[220:223], v[208:211], v[16:19]\n"
    "buffer_load_dwordx4 v[116:119], v237, s[28:31], s52 offen\n"
    "v_mfma_f32_16x16x32_bf16 v[4:7], v[216:219], v[208:211], v[4:7]\n"
    "v_perm_b32 v228, v133, v129, s60\n"
    "v_perm_b32 v229, v141, v137, s60\n"
    "v_mfma_f32_16x16x32_bf16 v[8:11], v[216:219], v[212:215], v[8:11]\n"
    "v_perm_b32 v230, v149, v145, s60\n"
    "v_perm_b32 v231, v157, v153, s60\n"
    "buffer_load_dwordx4 v[120:123], v238, s[28:31], s52 offen\n"
    "v_mfma_f32_16x16x32_bf16 v[20:23], v[220:223], v[212:215], v[20:23]\n"
    "buffer_load_dwordx4 v[124:127], v239, s[28:31], s52 offen\n"
    "s_cmp_eq_u32 s61, 0\n"
    "s_cbranch_scc0 .Lqg_18\n"
    "s_cmp_eq_u32 s75, 0\n"
    "s_cbranch_scc0 .Lqe_19\n"
    "v_readlane_b32 s67, v244, 15\n"
    "s_add_u32 m0, s48, 0\n"
    "s_add_u32 s68, s67, 3072\n"
    "buffer_load_dwordx4 v245, s[32:35], s68 offen offset:0 nt lds\n"
    "s_add_u32 s69, s67, 2176\n"
    "buffer_load_dwordx4 v245, s[32:35], s69 offen offset:1024 nt lds\n"
    "s_add_u32 s68, s67, 66560\n"
    "buffer_load_dwordx4 v245, s[32:35], s68 offen offset:2048 nt lds\n"
    "s_add_u32 s69, s67, 65664\n"
    "buffer_load_dwordx4 v245, s[32:35], s69 offen offset:3072 nt lds\n"
    "s_add_u32 m0, s48, 4096\n"
    "s_add_u32 s68, s67, 7168\n"
    "buffer_load_dwordx4 v245, s[32:35], s68 offen offset:0 nt lds\n"
    "s_add_u32 s69, s67, 6272\n"
    "buffer_load_dwordx4 v245, s[32:35], s69 offen offset:1024 nt lds\n"
    "s_add_u32 s68, s67, 70656\n"
    "buffer_load_dwordx4 v245, s[32:35], s68 offen offset:2048 nt lds\n"
    "s_add_u32 s69, s67, 69760\n"
    "buffer_load_dwordx4 v245, s[32:35], s69 offen offset:3072 nt lds\n"
    "s_branch .Lqe_19\n"
    ".Lqg_18:\n"
    "s_mov_b64 exec, 1\n"
    ".Lsp_20:\n"
    "ds_read_b32 v251, v253 offset:0\n"
    "s_waitcnt lgkmcnt(0)\n"
    "v_readfirstlane_b32 s64, v251\n"
    "s_nop 0\n"
    "s_cmp_ge_u32 s64, s55\n"
    "s_cbranch_scc1 .Lok_21\n"
    "s_sleep 1\n"
    "s_branch .Lsp_20\n"
    ".Lok_21:\n"
    "s_mov_b64 exec, -1\n"
    "s_mov_b64 exec, 1\n"
    "ds_sub_u32 v253, v252 offset:8\n"
    "s_mov_b64 exec, -1\n"
    "v_readlane_b32 s67, v244, 13\n"
    "s_add_u32 m0, s51, 98304\n"
    "s_add_u32 s68, s67, 3072\n"
    "s_add_u32 s69, s67, 2176\n"
    "buffer_load_dwordx4 v245, s[32:35], s68 offen offset:0 nt lds\n"
    "buffer_load_dwordx4 v245, s[32:35], s69 offen offset:1024 nt lds\n"
    "s_add_u32 m0, s51, 106496\n"
    "s_nop 0\n"
    "buffer_load_dwordx4 v254, s[36:39], s68 offen offset:0 nt lds\n"
    "buffer_load_dwordx4 v254, s[36:39], s69 offen offset:1024 nt lds\n"
    "v_readlane_b32 s67, v244, 14\n"
    "s_add_u32 m0, s51, 114688\n"
    "s_add_u32 s68, s67, 3072\n"
    "s_add_u32 s69, s67, 2176\n"
    "buffer_load_dwordx4 v245, s[32:35], s68 offen offset:0 nt lds\n"
    "buffer_load_dwordx4 v245, s[32:35], s69 offen offset:1024 nt lds\n"
    "s_add_u32 m0, s51, 122880\n"
    "s_nop 0\n"
    "buffer_load_dwordx4 v254, s[36:39], s68 offen offset:0 nt lds\n"
    "buffer_load_dwordx4 v254, s[36:39], s69 offen offset:1024 nt lds\n"
    ".Lqe_19:\n"
    "v_mfma_f32_16x16x32_bf16 v[32:35], v[224:227], v[212:215], v[32:35]\n"
    "v_perm_b32 v216, v134, v130, s59\n"
    "v_perm_b32 v217, v142, v138, s59\n"
    "v_mfma_f32_16x16x32_bf16 v[44:47], v[228:231], v[212:215], v[44:47]\n"
    "v_perm_b32 v218, v150, v146, s59\n"
    "v_perm_b32 v219, v158, v154, s59\n"
    "v_mfma_f32_16x16x32_bf16 v[40:43], v[228:231], v[208:211], v[40:43]\n"
    "v_mfma_f32_16x16x32_bf16 v[28:31], v[224:227], v[208:211], v[28:31]\n"
    "v_perm_b32 v220, v134, v130, s60\n"
    "v_perm_b32 v221, v142, v138, s60\n"
    "v_mfma_f32_16x16x32_bf16 v[24:27], v[224:227], v[204:207], v[24:27]\n"
    "v_perm_b32 v222, v150, v146, s60\n"
    "v_perm_b32 v223, v158, v154, s60\n"
    "v_mfma_f32_16x16x32_bf16 v[36:39], v[228:231], v[204:207], v[36:39]\n"
    "ds_read_b128 v[192:195], v240 offset:6144\n"
    "v_mfma_f32_16x16x32_bf16 v[48:51], v[216:219], v[204:207], v[48:51]\n"
    "v_perm_b32 v224, v135, v131, s59\n"
    "v_perm_b32 v225, v143, v139, s59\n"
    "v_mfma_f32_16x16x32_bf16 v[60:63], v[220:223], v[204:207], v[60:63]\n"
    "v_perm_b32 v226, v151, v147, s59\n"
    "v_perm_b32 v227, v159, v155, s59\n"
    "v_mfma_f32_16x16x32_bf16 v[64:67], v[220:223], v[208:211], v[64:67]\n"
    "ds_read_b128 v[196:199], v240 offset:8192\n"
    "v_mfma_f32_16x16x32_bf16 v[52:55], v[216:219], v[208:211], v[52:55]\n"
    "v_perm_b32 v228, v135, v131, s60\n"
    "v_perm_b32 v229, v143, v139, s60\n"
    "v_mfma_f32_16x16x32_bf16 v[56:59], v[216:219], v[212:215], v[56:59]\n"
    "v_perm_b32 v230, v151, v147, s60\n"
    "v_perm_b32 v231, v159, v155, s60\n"
    "v_mfma_f32_16x16x32_bf16 v[68:71], v[220:223], v[212:215], v[68:71]\n"
    "ds_read_b128 v[200:203], v240 offset:10240\n"
    "s_cmp_gt_u32 s75, s61\n"
    "s_cbranch_scc0 .Lqw_22\n"
    "s_waitcnt vmcnt(8)\n"
    ".Lqw_22:\n"
    "v_mfma_f32_16x16x32_bf16 v[80:83], v[224:227], v[212:215], v[80:83]\n"
    "s_waitcnt vmcnt(16)\n"
    "v_perm_b32 v219, v188, v184, s59\n"
    "v_perm_b32 v218, v180, v176, s59\n"
    "v_mfma_f32_16x16x32_bf16 v[92:95], v[228:231], v[212:215], v[92:95]\n"
    "v_perm_b32 v217, v172, v168, s59\n"
    "v_perm_b32 v216, v164, v160, s59\n"
    "v_mfma_f32_16x16x32_bf16 v[88:91], v[228:231], v[208:211], v[88:91]\n"
    "v_mfma_f32_16x16x32_bf16 v[76:79], v[224:227], v[208:211], v[76:79]\n"
    "v_perm_b32 v220, v164, v160, s60\n"
    "v_perm_b32 v221, v172, v168, s60\n"
    "v_mfma_f32_16x16x32_bf16 v[72:75], v[224:227], v[204:207], v[72:75]\n"
    "v_perm_b32 v222, v180, v176, s60\n"
    "v_perm_b32 v223, v188, v184, s60\n"
    "v_mfma_f32_16x16x32_bf16 v[84:87], v[228:231], v[204:207], v[84:87]\n"
    "s_waitcnt lgkmcnt(0)\n"
    "v_mfma_f32_16x16x32_bf16 v[0:3], v[216:219], v[192:195], v[0:3]\n"
    "v_perm_b32 v224, v165, v161, s59\n"
    "v_perm_b32 v225, v173, v169, s59\n"
    "v_mfma_f32_16x16x32_bf16 v[12:15], v[220:223], v[192:195], v[12:15]\n"
    "v_perm_b32 v226, v181, v177, s59\n"
    "v_perm_b32 v227, v189, v185, s59\n"
    "v_mfma_f32_16x16x32_bf16 v[16:19], v[220:223], v[196:199], v[16:19]\n"
    "v_mfma_f32_16x16x32_bf16 v[4:7], v[216:219], v[196:199], v[4:7]\n"
    "v_perm_b32 v228, v165, v161, s60\n"
    "v_perm_b32 v229, v173, v169, s60\n"
    "v_mfma_f32_16x16x32_bf16 v[8:11], v[216:219], v[200:203], v[8:11]\n"
    "v_perm_b32 v230, v181, v177, s60\n"
    "v_perm_b32 v231, v189, v185, s60\n"
    "v_mfma_f32_16x16x32_bf16 v[20:23], v[220:223], v[200:203], v[20:23]\n"
    "v_mfma_f32_16x16x32_bf16 v[32:35], v[224:227], v[200:203], v[32:35]\n"
    "v_perm_b32 v216, v166, v162, s59\n"
    "v_perm_b32 v217, v174, v170, s59\n"
    "v_mfma_f32_16x16x32_bf16 v[44:47], v[228:231], v[200:203], v[44:47]\n"
    "v_perm_b32 v218, v182, v178, s59\n"
    "v_perm_b32 v219, v190, v186, s59\n"
    "v_mfma_f32_16x16x32_bf16 v[40:43], v[228:231], v[196:199], v[40:43]\n"
    "v_mfma_f32_16x16x32_bf16 v[28:31], v[224:227], v[196:199], v[28:31]\n"
    "v_perm_b32 v220, v166, v162, s60\n"
    "v_perm_b32 v221, v174, v170, s60\n"
    "v_mfma_f32_16x16x32_bf16 v[24:27], v[224:227], v[192:195], v[24:27]\n"
    "v_perm_b32 v222, v182, v178, s60\n"
    "v_perm_b32 v223, v190, v186, s60\n"
    "v_mfma_f32_16x16x32_bf16 v[36:39], v[228:231], v[192:195], v[36:39]\n"
    "ds_read_b128 v[204:207], v241 offset:6144\n"
    "v_mfma_f32_16x16x32_bf16 v[48:51], v[216:219], v[192:195], v[48:51]\n"
    "v_perm_b32 v224, v167, v163, s59\n"
    "v_perm_b32 v225, v175, v171, s59\n"
    "v_mfma_f32_16x16x32_bf16 v[60:63], v[220:223], v[192:195], v[60:63]\n"
    "v_perm_b32 v226, v183, v179, s59\n"
    "v_perm_b32 v227, v191, v187, s59\n"
    "v_mfma_f32_16x16x32_bf16 v[64:67], v[220:223], v[196:199], v[64:67]\n"
    "ds_read_b128 v[208:211], v241 offset:8192\n"
    "v_mfma_f32_16x16x32_bf16 v[52:55], v[216:219], v[196:199], v[52:55]\n"
    "v_perm_b32 v228, v167, v163, s60\n"
    "v_perm_b32 v229, v175, v171, s60\n"
    "v_mfma_f32_16x16x32_bf16 v[56:59], v[216:219], v[200:203], v[56:59]\n"
    "v_perm_b32 v230, v183, v179, s60\n"
    "v_perm_b32 v231, v191, v187, s60\n"
    "v_mfma_f32_16x16x32_bf16 v[68:71], v[220:223], v[200:203], v[68:71]\n"
    "ds_read_b128 v[212:215], v241 offset:10240\n"
    "s_cmp_gt_u32 s75, s61\n"
    "s_cbranch_scc0 .Lqw_23\n"
    "s_waitcnt vmcnt(0)\n"
    ".Lqw_23:\n"
    "v_mfma_f32_16x16x32_bf16 v[80:83], v[224:227], v[200:203], v[80:83]\n"
    "s_waitcnt vmcnt(8)\n"
    "v_perm_b32 v219, v124, v120, s59\n"
    "v_perm_b32 v218, v116, v112, s59\n"
    "v_mfma_f32_16x16x32_bf16 v[92:95], v[228:231], v[200:203], v[92:95]\n"
    "v_perm_b32 v217, v108, v104, s59\n"
    "v_perm_b32 v216, v100, v96, s59\n"
    "v_mfma_f32_16x16x32_bf16 v[88:91], v[228:231], v[196:199], v[88:91]\n"
    "v_mfma_f32_16x16x32_bf16 v[76:79], v[224:227], v[196:199], v[76:79]\n"
    "v_perm_b32 v220, v100, v96, s60\n"
    "v_perm_b32 v221, v108, v104, s60\n"
    "v_mfma_f32_16x16x32_bf16 v[72:75], v[224:227], v[192:195], v[72:75]\n"
    "v_perm_b32 v222, v116, v112, s60\n"
    "v_perm_b32 v223, v124, v120, s60\n"
    "v_mfma_f32_16x16x32_bf16 v[84:87], v[228:231], v[192:195], v[84:87]\n"
    "s_waitcnt lgkmcnt(0)\n"
    "v_mfma_f32_16x16x32_bf16 v[0:3], v[216:219], v[204:207], v[0:3]\n"
    "v_perm_b32 v224, v101, v97, s59\n"
    "v_perm_b32 v225, v109, v105, s59\n"
    "v_mfma_f32_16x16x32_bf16 v[12:15], v[220:223], v[204:207], v[12:15]\n"
    "v_perm_b32 v226, v117, v113, s59\n"
    "v_perm_b32 v227, v125, v121, s59\n"
    "v_mfma_f32_16x16x32_bf16 v[16:19], v[220:223], v[208:211], v[16:19]\n"
    "v_mfma_f32_16x16x32_bf16 v[4:7], v[216:219], v[208:211], v[4:7]\n"
    "v_perm_b32 v228, v101, v97, s60\n"
    "v_perm_b32 v229, v109, v105, s60\n"
    "v_mfma_f32_16x16x32_bf16 v[8:11], v[216:219], v[212:215], v[8:11]\n"
    "v_perm_b32 v230, v117, v113, s60\n"
    "v_perm_b32 v231, v125, v121, s60\n"
    "v_mfma_f32_16x16x32_bf16 v[20:23], v[220:223], v[212:215], v[20:23]\n"
    "v_mfma_f32_16x16x32_bf16 v[32:35], v[224:227], v[212:215], v[32:35]\n"
    "v_perm_b32 v216, v102, v98, s59\n"
    "v_perm_b32 v217, v110, v106, s59\n"
    "v_mfma_f32_16x16x32_bf16 v[44:47], v[228:231], v[212:215], v[44:47]\n"
    "v_perm_b32 v218, v118, v114, s59\n"
    "v_perm_b32 v219, v126, v122, s59\n"
    "v_mfma_f32_16x16x32_bf16 v[40:43], v[228:231], v[208:211], v[40:43]\n"
    "v_mfma_f32_16x16x32_bf16 v[28:31], v[224:227], v[208:211], v[28:31]\n"
    "v_perm_b32 v220, v102, v98, s60\n"
    "v_perm_b32 v221, v110, v106, s60\n"
    "v_mfma_f32_16x16x32_bf16 v[24:27], v[224:227], v[204:207], v[24:27]\n"
    "v_perm_b32 v222, v118, v114, s60\n"
    "v_perm_b32 v223, v126, v122, s60\n"
    "v_mfma_f32_16x16x32_bf16 v[36:39], v[228:231], v[204:207], v[36:39]\n"
    "v_mfma_f32_16x16x32_bf16 v[48:51], v[216:219], v[204:207], v[48:51]\n"
    "v_perm_b32 v224, v103, v99, s59\n"
    "v_perm_b32 v225, v111, v107, s59\n"
    "v_mfma_f32_16x16x32_bf16 v[60:63], v[220:223], v[204:207], v[60:63]\n"
    "v_perm_b32 v226, v119, v115, s59\n"
    "v_perm_b32 v227, v127, v123, s59\n"
    "v_mfma_f32_16x16x32_bf16 v[64:67], v[220:223], v[208:211], v[64:67]\n"
    "v_mfma_f32_16x16x32_bf16 v[52:55], v[216:219], v[208:211], v[52:55]\n"
    "v_perm_b32 v228, v103, v99, s60\n"
    "v_perm_b32 v229, v111, v107, s60\n"
    "v_mfma_f32_16x16x32_bf16 v[56:59], v[216:219], v[212:215], v[56:59]\n"
    "v_perm_b32 v230, v119, v115, s60\n"
    "v_perm_b32 v231, v127, v123, s60\n"
    "v_mfma_f32_16x16x32_bf16 v[68:71], v[220:223], v[212:215], v[68:71]\n"
    "v_mfma_f32_16x16x32_bf16 v[80:83], v[224:227], v[212:215], v[80:83]\n"
    "v_mfma_f32_16x16x32_bf16 v[92:95], v[228:231], v[212:215], v[92:95]\n"
    "v_mfma_f32_16x16x32_bf16 v[88:91], v[228:231], v[208:211], v[88:91]\n"
    "v_mfma_f32_16x16x32_bf16 v[76:79], v[224:227], v[208:211], v[76:79]\n"
    "v_mfma_f32_16x16x32_bf16 v[72:75], v[224:227], v[204:207], v[72:75]\n"
    "v_mfma_f32_16x16x32_bf16 v[84:87], v[228:231], v[204:207], v[84:87]\n"
    "s_cmp_eq_u32 s61, 0\n"
    "s_cselect_b32 s64, s48, 98304\n"
    "v_bfe_u32 v246, v255, 1, 2\n"
    "v_lshlrev_b32 v247, 4, v246\n"
    "v_bfe_u32 v246, v255, 4, 1\n"
    "v_lshl_add_u32 v247, v246, 6, v247\n"
    "v_and_b32 v246, 7, v255\n"
    "v_lshl_add_u32 v247, v246, 7, v247\n"
    "v_lshrrev_b32 v246, 5, v255\n"
    "v_lshl_add_u32 v247, v246, 10, v247\n"
    "v_bfe_u32 v246, v255, 3, 1\n"
    "v_lshl_add_u32 v247, v246, 11, v247\n"
    "v_add_u32 v238, s64, v247\n"
    "v_xor_b32 v239, 16, v238\n"
    "v_xor_b32 v240, 32, v238\n"
    "v_xor_b32 v241, 48, v238\n"
    "s_cbranch_scc0 .Lqpack\n"
    "s_mov_b64 exec, 1\n"
    "ds_add_u32 v253, v252 offset:16\n"
    "s_mov_b64 exec, -1\n"
    "s_cmp_eq_u32 s75, 0\n"
    "s_cbranch_scc0 .Lqa\n"
    "v_readlane_b32 s67, v244, 15\n"
    "s_add_u32 m0, s48, 8192\n"
    "s_add_u32 s68, s67, 3072\n"
    "buffer_load_dwordx4 v254, s[36:39], s68 offen offset:0 nt lds\n"
    "s_add_u32 s69, s67, 2176\n"
    "buffer_load_dwordx4 v254, s[36:39], s69 offen offset:1024 nt lds\n"
    "s_add_u32 s68, s67, 66560\n"
    "buffer_load_dwordx4 v254, s[36:39], s68 offen offset:2048 nt lds\n"
    "s_add_u32 s69, s67, 65664\n"
    "buffer_load_dwordx4 v254, s[36:39], s69 offen offset:3072 nt lds\n"
    "s_add_u32 m0, s48, 12288\n"
    "s_add_u32 s68, s67, 7168\n"
    "buffer_load_dwordx4 v254, s[36:39], s68 offen offset:0 nt lds\n"
    "s_add_u32 s69, s67, 6272\n"
    "buffer_load_dwordx4 v254, s[36:39], s69 offen offset:1024 nt lds\n"
    "s_add_u32 s68, s67, 70656\n"
    "buffer_load_dwordx4 v254, s[36:39], s68 offen offset:2048 nt lds\n"
    "s_add_u32 s69, s67, 69760\n"
    "buffer_load_dwordx4 v254, s[36:39], s69 offen offset:3072 nt lds\n"
    "s_branch .Lqpack\n"
    ".Lqa:\n"
    "ds_read_b128 v[144:147], v238 offset:0\n"
    "ds_read_b128 v[148:151], v239 offset:0\n"
    "ds_read_b128 v[152:155], v240 offset:0\n"
    "ds_read_b128 v[156:159], v241 offset:0\n"
    "ds_read_b128 v[160:163], v241 offset:4096\n"
    "ds_read_b128 v[164:167], v240 offset:4096\n"
    "ds_read_b128 v[168:171], v239 offset:4096\n"
    "ds_read_b128 v[172:175], v238 offset:4096\n"
    "s_waitcnt lgkmcnt(0)\n"
    "v_readlane_b32 s67, v244, 16\n"
    "s_add_u32 m0, s48, 0\n"
    "s_add_u32 s68, s67, 3072\n"
    "buffer_load_dwordx4 v254, s[36:39], s68 offen offset:0 nt lds\n"
    "s_add_u32 s69, s67, 2176\n"
    "buffer_load_dwordx4 v254, s[36:39], s69 offen offset:1024 nt lds\n"
    "s_add_u32 s68, s67, 66560\n"
    "buffer_load_dwordx4 v254, s[36:39], s68 offen offset:2048 nt lds\n"
    "s_add_u32 s69, s67, 65664\n"
    "buffer_load_dwordx4 v254, s[36:39], s69 offen offset:3072 nt lds\n"
    "s_add_u32 m0, s48, 4096\n"
    "s_add_u32 s68, s67, 7168\n"
    "buffer_load_dwordx4 v254, s[36:39], s68 offen offset:0 nt lds\n"
    "s_add_u32 s69, s67, 6272\n"
    "buffer_load_dwordx4 v254, s[36:39], s69 offen offset:1024 nt lds\n"
    "s_add_u32 s68, s67, 70656\n"
    "buffer_load_dwordx4 v254, s[36:39], s68 offen offset:2048 nt lds\n"
    "s_add_u32 s69, s67, 69760\n"
    "buffer_load_dwordx4 v254, s[36:39], s69 offen offset:3072 nt lds\n"
    "s_mov_b64 exec, 1\n"
    "ds_add_u32 v253, v252 offset:12\n"
    "s_mov_b64 exec, -1\n"
    ".Lqpack:\n"
    "s_mov_b32 s62, 0x3e0293ee\n"
    "s_mov_b32 s63, 0x3e0293ee\n"
    "v_bfe_u32 v246, v255, 2, 3\n"
    "v_lshrrev_b32 v247, 1, v246\n"
    "v_lshlrev_b32 v247, 5, v247\n"
    "v_lshl_add_u32 v247, v246, 7, v247\n"
    "v_and_b32 v246, 3, v255\n"
    "v_lshl_add_u32 v247, v246, 3, v247\n"
    "v_lshrrev_b32 v246, 5, v255\n"
    "v_lshl_add_u32 v247, v246, 11, v247\n"
    "v_add_u32 v242, s64, v247\n"
    "v_xor_b32 v243, 32, v242\n"
    "v_xor_b32 v248, 64, v242\n"
    "v_xor_b32 v249, 96, v242\n"
    "v_pk_mul_f32 v[0:1], v[0:1], s[62:63]\n"
    "v_pk_mul_f32 v[2:3], v[2:3], s[62:63]\n"
    "v_pk_mul_f32 v[12:13], v[12:13], s[62:63]\n"
    "v_pk_mul_f32 v[14:15], v[14:15], s[62:63]\n"
    "v_cvt_pk_bf16_f32 v96, v0, v12\n"
    "v_cvt_pk_bf16_f32 v108, v1, v13\n"
    "v_cvt_pk_bf16_f32 v120, v2, v14\n"
    "v_cvt_pk_bf16_f32 v132, v3, v15\n"
    "v_pk_mul_f32 v[4:5], v[4:5], s[62:63]\n"
    "v_pk_mul_f32 v[6:7], v[6:7], s[62:63]\n"
    "v_pk_mul_f32 v[16:17], v[16:17], s[62:63]\n"
    "v_pk_mul_f32 v[18:19], v[18:19], s[62:63]\n"
    "v_cvt_pk_bf16_f32 v100, v4, v16\n"
    "v_cvt_pk_bf16_f32 v112, v5, v17\n"
    "v_cvt_pk_bf16_f32 v124, v6, v18\n"
    "v_cvt_pk_bf16_f32 v136, v7, v19\n"
    "v_pk_mul_f32 v[8:9], v[8:9], s[62:63]\n"
    "v_pk_mul_f32 v[10:11], v[10:11], s[62:63]\n"
    "v_pk_mul_f32 v[20:21], v[20:21], s[62:63]\n"
    "v_pk_mul_f32 v[22:23], v[22:23], s[62:63]\n"
    "v_cvt_pk_bf16_f32 v104, v8, v20\n"
    "v_cvt_pk_bf16_f32 v116, v9, v21\n"
    "v_cvt_pk_bf16_f32 v128, v10, v22\n"
    "v_cvt_pk_bf16_f32 v140, v11, v23\n"
    "v_pk_mul_f32 v[24:25], v[24:25], s[62:63]\n"
    "v_pk_mul_f32 v[26:27], v[26:27], s[62:63]\n"
    "v_pk_mul_f32 v[36:37], v[36:37], s[62:63]\n"
    "v_pk_mul_f32 v[38:39], v[38:39], s[62:63]\n"
    "v_cvt_pk_bf16_f32 v97, v24, v36\n"
    "v_cvt_pk_bf16_f32 v109, v25, v37\n"
    "v_cvt_pk_bf16_f32 v121, v26, v38\n"
    "v_cvt_pk_bf16_f32 v133, v27, v39\n"
    "v_pk_mul_f32 v[28:29], v[28:29], s[62:63]\n"
    "v_pk_mul_f32 v[30:31], v[30:31], s[62:63]\n"
    "v_pk_mul_f32 v[40:41], v[40:41], s[62:63]\n"
    "v_pk_mul_f32 v[42:43], v[42:43], s[62:63]\n"
    "v_cvt_pk_bf16_f32 v101, v28, v40\n"
    "v_cvt_pk_bf16_f32 v113, v29, v41\n"
    "v_cvt_pk_bf16_f32 v125, v30, v42\n"
    "v_cvt_pk_bf16_f32 v137, v31, v43\n"
    "v_pk_mul_f32 v[32:33], v[32:33], s[62:63]\n"
    "v_pk_mul_f32 v[34:35], v[34:35], s[62:63]\n"
    "v_pk_mul_f32 v[44:45], v[44:45], s[62:63]\n"
    "v_pk_mul_f32 v[46:47], v[46:47], s[62:63]\n"
    "v_cvt_pk_bf16_f32 v105, v32, v44\n"
    "v_cvt_pk_bf16_f32 v117, v33, v45\n"
    "v_cvt_pk_bf16_f32 v129, v34, v46\n"
    "v_cvt_pk_bf16_f32 v141, v35, v47\n"
    "v_pk_mul_f32 v[48:49], v[48:49], s[62:63]\n"
    "v_pk_mul_f32 v[50:51], v[50:51], s[62:63]\n"
    "v_pk_mul_f32 v[60:61], v[60:61], s[62:63]\n"
    "v_pk_mul_f32 v[62:63], v[62:63], s[62:63]\n"
    "v_cvt_pk_bf16_f32 v98, v48, v60\n"
    "v_cvt_pk_bf16_f32 v110, v49, v61\n"
    "v_cvt_pk_bf16_f32 v122, v50, v62\n"
    "v_cvt_pk_bf16_f32 v134, v51, v63\n"
    "v_pk_mul_f32 v[52:53], v[52:53], s[62:63]\n"
    "v_pk_mul_f32 v[54:55], v[54:55], s[62:63]\n"
    "v_pk_mul_f32 v[64:65], v[64:65], s[62:63]\n"
    "v_pk_mul_f32 v[66:67], v[66:67], s[62:63]\n"
    "v_cvt_pk_bf16_f32 v102, v52, v64\n"
    "v_cvt_pk_bf16_f32 v114, v53, v65\n"
    "v_cvt_pk_bf16_f32 v126, v54, v66\n"
    "v_cvt_pk_bf16_f32 v138, v55, v67\n"
    "v_pk_mul_f32 v[56:57], v[56:57], s[62:63]\n"
    "v_pk_mul_f32 v[58:59], v[58:59], s[62:63]\n"
    "v_pk_mul_f32 v[68:69], v[68:69], s[62:63]\n"
    "v_pk_mul_f32 v[70:71], v[70:71], s[62:63]\n"
    "v_cvt_pk_bf16_f32 v106, v56, v68\n"
    "v_cvt_pk_bf16_f32 v118, v57, v69\n"
    "v_cvt_pk_bf16_f32 v130, v58, v70\n"
    "v_cvt_pk_bf16_f32 v142, v59, v71\n"
    "v_pk_mul_f32 v[72:73], v[72:73], s[62:63]\n"
    "v_pk_mul_f32 v[74:75], v[74:75], s[62:63]\n"
    "v_pk_mul_f32 v[84:85], v[84:85], s[62:63]\n"
    "v_pk_mul_f32 v[86:87], v[86:87], s[62:63]\n"
    "v_cvt_pk_bf16_f32 v99, v72, v84\n"
    "v_cvt_pk_bf16_f32 v111, v73, v85\n"
    "v_cvt_pk_bf16_f32 v123, v74, v86\n"
    "v_cvt_pk_bf16_f32 v135, v75, v87\n"
    "v_pk_mul_f32 v[76:77], v[76:77], s[62:63]\n"
    "v_pk_mul_f32 v[78:79], v[78:79], s[62:63]\n"
    "v_pk_mul_f32 v[88:89], v[88:89], s[62:63]\n"
    "v_pk_mul_f32 v[90:91], v[90:91], s[62:63]\n"
    "v_cvt_pk_bf16_f32 v103, v76, v88\n"
    "v_cvt_pk_bf16_f32 v115, v77, v89\n"
    "v_cvt_pk_bf16_f32 v127, v78, v90\n"
    "v_cvt_pk_bf16_f32 v139, v79, v91\n"
    "v_pk_mul_f32 v[80:81], v[80:81], s[62:63]\n"
    "v_pk_mul_f32 v[82:83], v[82:83], s[62:63]\n"
    "v_pk_mul_f32 v[92:93], v[92:93], s[62:63]\n"
    "v_pk_mul_f32 v[94:95], v[94:95], s[62:63]\n"
    "v_cvt_pk_bf16_f32 v107, v80, v92\n"
    "v_cvt_pk_bf16_f32 v119, v81, v93\n"
    "v_cvt_pk_bf16_f32 v131, v82, v94\n"
    "v_cvt_pk_bf16_f32 v143, v83, v95\n"
    "s_mov_b32 s49, 0\n"
    "s_mov_b32 s57, 0x43800000\n"
    "s_cmp_eq_u32 s61, 0\n"
    "s_cbranch_scc0 .Latt_g\n"
    "s_cmp_lt_u32 s45, 2\n"
    "s_cselect_b64 s[88:89], s[12:13], s[22:23]\n"
    "s_cselect_b32 s66, s79, s46\n"
    "s_cselect_b32 s90, 0, 879\n"
    "s_add_u32 s90, s90, 2382\n"
    "s_mul_i32 s64, s47, 7\n"
    "s_add_u32 s90, s90, s64\n"
    "s_cmp_eq_u32 s75, 0\n"
    "s_cselect_b32 s90, s90, -1\n"
    "s_and_b32 s64, s45, 1\n"
    "s_add_u32 s64, s64, s47\n"
    "s_lshl_b32 s64, s64, 14\n"
    "s_lshl_b32 s65, s66, 19\n"
    "s_add_u32 s64, s64, s65\n"
    "v_lshlrev_b32 v236, 8, v255\n"
    "v_add_u32 v236, s64, v236\n"
    "s_mul_i32 s64, s75, 158\n"
    "s_sub_u32 s91, 396, s64\n"
    "s_sub_u32 s92, 443, s64\n"
    "s_sub_u32 s93, 327, s64\n"
    "s_lshl_b32 s55, s75, 2\n"
    "s_add_u32 s55, s55, s45\n"
    "s_sub_u32 s55, s55, 1\n"
    "s_mov_b32 s63, 0\n"
    "s_cmp_eq_u32 s75, 0\n"
    "s_cbranch_scc0 .Lprimed\n"
    "v_readlane_b32 s67, v244, 16\n"
    "s_add_u32 m0, s48, 16384\n"
    "s_add_u32 s68, s67, 3072\n"
    "buffer_load_dwordx4 v245, s[32:35], s68 offen offset:0 nt lds\n"
    "s_add_u32 s69, s67, 2176\n"
    "buffer_load_dwordx4 v245, s[32:35], s69 offen offset:1024 nt lds\n"
    "s_add_u32 s68, s67, 66560\n"
    "buffer_load_dwordx4 v245, s[32:35], s68 offen offset:2048 nt lds\n"
    "s_add_u32 s69, s67, 65664\n"
    "buffer_load_dwordx4 v245, s[32:35], s69 offen offset:3072 nt lds\n"
    "s_add_u32 m0, s48, 20480\n"
    "s_add_u32 s68, s67, 7168\n"
    "buffer_load_dwordx4 v245, s[32:35], s68 offen offset:0 nt lds\n"
    "s_add_u32 s69, s67, 6272\n"
    "buffer_load_dwordx4 v245, s[32:35], s69 offen offset:1024 nt lds\n"
    "s_add_u32 s68, s67, 70656\n"
    "buffer_load_dwordx4 v245, s[32:35], s68 offen offset:2048 nt lds\n"
    "s_add_u32 s69, s67, 69760\n"
    "buffer_load_dwordx4 v245, s[32:35], s69 offen offset:3072 nt lds\n"
    "s_waitcnt vmcnt(16)\n"
    "ds_read_b128 v[144:147], v238 offset:0\n"
    "ds_read_b128 v[148:151], v239 offset:0\n"
    "ds_read_b128 v[152:155], v240 offset:0\n"
    "ds_read_b128 v[156:159], v241 offset:0\n"
    "ds_read_b128 v[160:163], v241 offset:4096\n"
    "ds_read_b128 v[164:167], v240 offset:4096\n"
    "ds_read_b128 v[168:171], v239 offset:4096\n"
    "ds_read_b128 v[172:175], v238 offset:4096\n"
    ".Lprimed:\n"
    "s_waitcnt lgkmcnt(6)\n"
    "v_mfma_f32_16x16x32_bf16 v[176:179], v[144:147], v[96:99], 0\n"
    "v_readlane_b32 s67, v244, s49\n"
    "v_mfma_f32_16x16x32_bf16 v[176:179], v[148:151], v[108:111], v[176:179]\n"
    "s_add_u32 s4, s67, 3072\n"
    "s_waitcnt lgkmcnt(4)\n"
    "v_mfma_f32_16x16x32_bf16 v[176:179], v[152:155], v[120:123], v[176:179]\n"
    "s_add_u32 s5, s67, 2176\n"
    "v_mfma_f32_16x16x32_bf16 v[176:179], v[156:159], v[132:135], v[176:179]\n"
    "s_add_u32 s6, s67, 66560\n"
    "s_add_u32 s7, s67, 65664\n"
    "s_waitcnt lgkmcnt(2)\n"
    "v_mfma_f32_16x16x32_bf16 v[180:183], v[160:163], v[132:135], 0\n"
    "s_add_u32 s8, s67, 7168\n"
    "v_mfma_f32_16x16x32_bf16 v[180:183], v[164:167], v[120:123], v[180:183]\n"
    "s_add_u32 s9, s67, 6272\n"
    "s_waitcnt lgkmcnt(0)\n"
    "v_mfma_f32_16x16x32_bf16 v[180:183], v[168:171], v[108:111], v[180:183]\n"
    "s_add_u32 s10, s67, 70656\n"
    "v_mfma_f32_16x16x32_bf16 v[180:183], v[172:175], v[96:99], v[180:183]\n"
    "s_add_u32 s11, s67, 69760\n"
    "v_mfma_f32_16x16x32_bf16 v[188:191], v[172:175], v[100:103], 0\n"
    "v_mfma_f32_16x16x32_bf16 v[188:191], v[168:171], v[112:115], v[188:191]\n"
    "v_mfma_f32_16x16x32_bf16 v[188:191], v[164:167], v[124:127], v[188:191]\n"
    "v_max3_f32 v200, v176, v177, v178\n"
    "s_nop 2\n"
    "v_max3_f32 v201, v180, v181, v182\n"
    "v_max_f32 v200, v200, v179\n"
    "v_max3_f32 v200, v200, v201, v183\n"
    "v_mov_b32 v201, v200\n"
    "s_nop 1\n"
    "v_permlane16_swap_b32 v200, v201\n"
    "v_max_f32 v200, v200, v201\n"
    "v_mov_b32 v201, v200\n"
    "v_mfma_f32_16x16x32_bf16 v[188:191], v[160:163], v[136:139], v[188:191]\n"
    "s_nop 0\n"
    "v_permlane32_swap_b32 v200, v201\n"
    "v_max_f32 v200, v200, v201\n"
    "v_sub_f32 v176, v176, v200\n"
    "v_sub_f32 v177, v177, v200\n"
    "v_sub_f32 v178, v178, v200\n"
    "v_sub_f32 v179, v179, v200\n"
    "v_sub_f32 v180, v180, v200\n"
    "v_sub_f32 v181, v181, v200\n"
    "v_mfma_f32_16x16x32_bf16 v[184:187], v[156:159], v[136:139], 0\n"
    "v_sub_f32 v182, v182, v200\n"
    "v_sub_f32 v183, v183, v200\n"
    "v_sub_f32 v212, 0, v200\n"
    "v_sub_f32 v213, 0, v200\n"
    "v_sub_f32 v214, 0, v200\n"
    "v_sub_f32 v215, 0, v200\n"
    "v_exp_f32 v176, v176\n"
    "v_mfma_f32_16x16x32_bf16 v[184:187], v[152:155], v[124:127], v[184:187]\n"
    "v_exp_f32 v177, v177\n"
    "v_exp_f32 v178, v178\n"
    "v_add_f32 v224, v176, v177\n"
    "v_exp_f32 v179, v179\n"
    "v_cvt_pk_bf16_f32 v200, v176, v177\n"
    "v_add_f32 v224, v224, v178\n"
    "v_mfma_f32_16x16x32_bf16 v[184:187], v[148:151], v[112:115], v[184:187]\n"
    "v_exp_f32 v180, v180\n"
    "v_add_f32 v224, v224, v179\n"
    "v_exp_f32 v181, v181\n"
    "v_cvt_pk_bf16_f32 v201, v178, v179\n"
    "v_add_f32 v224, v224, v180\n"
    "v_mfma_f32_16x16x32_bf16 v[184:187], v[144:147], v[100:103], v[184:187]\n"
    "v_exp_f32 v182, v182\n"
    "v_add_f32 v224, v224, v181\n"
    "v_exp_f32 v183, v183\n"
    "v_cvt_pk_bf16_f32 v202, v180, v181\n"
    "v_add_f32 v224, v224, v182\n"
    "v_add_f32 v224, v224, v183\n"
    "v_cvt_pk_bf16_f32 v203, v182, v183\n"
    "s_waitcnt vmcnt(8)\n"
    "v_mfma_f32_16x16x32_bf16 v[192:195], v[144:147], v[104:107], 0\n"
    "ds_read_b64_tr_b16 v[144:145], v242 offset:8192\n"
    "ds_read_b64_tr_b16 v[146:147], v242 offset:12288\n"
    "v_max3_f32 v204, v184, v185, v186\n"
    "v_max3_f32 v205, v188, v189, v190\n"
    "v_mfma_f32_16x16x32_bf16 v[192:195], v[148:151], v[116:119], v[192:195]\n"
    "ds_read_b64_tr_b16 v[148:149], v243 offset:8192\n"
    "ds_read_b64_tr_b16 v[150:151], v243 offset:12288\n"
    "v_max_f32 v204, v204, v187\n"
    "v_max3_f32 v204, v204, v205, v191\n"
    "v_mov_b32 v205, v204\n"
    "v_mfma_f32_16x16x32_bf16 v[192:195], v[152:155], v[128:131], v[192:195]\n"
    "ds_read_b64_tr_b16 v[152:153], v248 offset:8192\n"
    "ds_read_b64_tr_b16 v[154:155], v248 offset:12288\n"
    "v_permlane16_swap_b32 v204, v205\n"
    "v_max_f32 v204, v204, v205\n"
    "v_mfma_f32_16x16x32_bf16 v[192:195], v[156:159], v[140:143], v[192:195]\n"
    "ds_read_b64_tr_b16 v[156:157], v249 offset:8192\n"
    "ds_read_b64_tr_b16 v[158:159], v249 offset:12288\n"
    "v_mov_b32 v205, v204\n"
    "s_nop 1\n"
    "v_permlane32_swap_b32 v204, v205\n"
    "v_mfma_f32_16x16x32_bf16 v[196:199], v[160:163], v[140:143], 0\n"
    "ds_read_b64_tr_b16 v[160:161], v242 offset:9216\n"
    "ds_read_b64_tr_b16 v[162:163], v242 offset:13312\n"
    "v_max_f32 v204, v204, v205\n"
    "v_sub_f32 v184, v184, v204\n"
    "v_mfma_f32_16x16x32_bf16 v[196:199], v[164:167], v[128:131], v[196:199]\n"
    "ds_read_b64_tr_b16 v[164:165], v243 offset:9216\n"
    "ds_read_b64_tr_b16 v[166:167], v243 offset:13312\n"
    "v_sub_f32 v185, v185, v204\n"
    "v_sub_f32 v186, v186, v204\n"
    "v_sub_f32 v187, v187, v204\n"
    "v_mfma_f32_16x16x32_bf16 v[196:199], v[168:171], v[116:119], v[196:199]\n"
    "ds_read_b64_tr_b16 v[168:169], v248 offset:9216\n"
    "ds_read_b64_tr_b16 v[170:171], v248 offset:13312\n"
    "v_sub_f32 v188, v188, v204\n"
    "v_sub_f32 v189, v189, v204\n"
    "v_mfma_f32_16x16x32_bf16 v[196:199], v[172:175], v[104:107], v[196:199]\n"
    "ds_read_b64_tr_b16 v[172:173], v249 offset:9216\n"
    "ds_read_b64_tr_b16 v[174:175], v249 offset:13312\n"
    "v_sub_f32 v190, v190, v204\n"
    "v_sub_f32 v191, v191, v204\n"
    "s_waitcnt lgkmcnt(8)\n"
    "v_mfma_f32_16x16x32_bf16 v[0:3], v[144:147], v[200:203], 0\n"
    "v_sub_f32 v216, 0, v204\n"
    "v_sub_f32 v217, 0, v204\n"
    "v_sub_f32 v218, 0, v204\n"
    "v_sub_f32 v219, 0, v204\n"
    "v_exp_f32 v188, v188\n"
    "v_mfma_f32_16x16x32_bf16 v[12:15], v[148:151], v[200:203], 0\n"
    "v_exp_f32 v189, v189\n"
    "v_exp_f32 v190, v190\n"
    "v_add_f32 v225, v188, v189\n"
    "v_mfma_f32_16x16x32_bf16 v[24:27], v[152:155], v[200:203], 0\n"
    "v_exp_f32 v191, v191\n"
    "v_cvt_pk_bf16_f32 v206, v188, v189\n"
    "v_add_f32 v225, v225, v190\n"
    "v_exp_f32 v184, v184\n"
    "v_mfma_f32_16x16x32_bf16 v[36:39], v[156:159], v[200:203], 0\n"
    "v_add_f32 v225, v225, v191\n"
    "v_exp_f32 v185, v185\n"
    "v_cvt_pk_bf16_f32 v207, v190, v191\n"
    "v_add_f32 v225, v225, v184\n"
    "s_waitcnt lgkmcnt(0)\n"
    "v_mfma_f32_16x16x32_bf16 v[48:51], v[160:163], v[200:203], 0\n"
    "v_exp_f32 v186, v186\n"
    "v_add_f32 v225, v225, v185\n"
    "v_exp_f32 v187, v187\n"
    "v_mfma_f32_16x16x32_bf16 v[60:63], v[164:167], v[200:203], 0\n"
    "v_cvt_pk_bf16_f32 v204, v184, v185\n"
    "v_add_f32 v225, v225, v186\n"
    "v_add_f32 v225, v225, v187\n"
    "v_cvt_pk_bf16_f32 v205, v186, v187\n"
    "v_max3_f32 v208, v192, v193, v194\n"
    "v_mfma_f32_16x16x32_bf16 v[72:75], v[168:171], v[200:203], 0\n"
    "v_max3_f32 v209, v196, v197, v198\n"
    "v_max_f32 v208, v208, v195\n"
    "v_max3_f32 v208, v208, v209, v199\n"
    "v_mov_b32 v209, v208\n"
    "s_nop 1\n"
    "v_permlane16_swap_b32 v208, v209\n"
    "v_max_f32 v208, v208, v209\n"
    "v_mfma_f32_16x16x32_bf16 v[84:87], v[172:175], v[200:203], 0\n"
    "v_mov_b32 v209, v208\n"
    "s_nop 1\n"
    "v_permlane32_swap_b32 v208, v209\n"
    "v_max_f32 v208, v208, v209\n"
    "v_sub_f32 v192, v192, v208\n"
    "v_sub_f32 v193, v193, v208\n"
    "s_cmp_eq_u32 s75, 0\n"
    "s_cbranch_scc0 .Li_24\n"
    "s_mov_b64 exec, 1\n"
    ".Lpa_25:\n"
    "ds_read_b64 v[246:247], v253 offset:8\n"
    "s_memrealtime s[70:71]\n"
    "s_waitcnt lgkmcnt(0)\n"
    "v_readfirstlane_b32 s64, v246\n"
    "v_readfirstlane_b32 s68, v247\n"
    "s_sub_u32 s65, s70, s86\n"
    "s_cmp_ge_i32 s68, s55\n"
    "s_cbranch_scc0 .Lpc_27\n"
    "s_cmp_eq_u32 s64, 0\n"
    "s_cbranch_scc0 .Lpd_28\n"
    ".Lpb_26:\n"
    "s_mov_b64 exec, -1\n"
    "s_mov_b32 s63, s65\n"
    "s_cmp_ge_u32 s65, s90\n"
    "s_cbranch_scc1 .Ltw_29\n"
    ".Ltb_30:\n"
    "s_add_u32 m0, s48, 0\n"
    "s_add_u32 s55, s55, 4\n"
    "buffer_load_dwordx4 v254, s[36:39], s4 offen offset:0 nt lds\n"
    "buffer_load_dwordx4 v254, s[36:39], s5 offen offset:1024 nt lds\n"
    "buffer_load_dwordx4 v254, s[36:39], s6 offen offset:2048 nt lds\n"
    "buffer_load_dwordx4 v254, s[36:39], s7 offen offset:3072 nt lds\n"
    "s_add_u32 m0, s48, 4096\n"
    "s_nop 0\n"
    "buffer_load_dwordx4 v254, s[36:39], s8 offen offset:0 nt lds\n"
    "buffer_load_dwordx4 v254, s[36:39], s9 offen offset:1024 nt lds\n"
    "buffer_load_dwordx4 v254, s[36:39], s10 offen offset:2048 nt lds\n"
    "buffer_load_dwordx4 v254, s[36:39], s11 offen offset:3072 nt lds\n"
    "s_mov_b64 exec, 1\n"
    "ds_add_u32 v253, v252 offset:12\n"
    "s_mov_b64 exec, -1\n"
    ".Li_24:\n"
    "v_mfma_f32_16x16x32_bf16 v[88:91], v[172:175], v[204:207], 0\n"
    "v_sub_f32 v194, v194, v208\n"
    "v_sub_f32 v195, v195, v208\n"
    "v_sub_f32 v196, v196, v208\n"
    "v_sub_f32 v197, v197, v208\n"
    "v_sub_f32 v198, v198, v208\n"
    "v_mfma_f32_16x16x32_bf16 v[76:79], v[168:171], v[204:207], 0\n"
    "v_sub_f32 v199, v199, v208\n"
    "v_sub_f32 v220, 0, v208\n"
    "v_sub_f32 v221, 0, v208\n"
    "v_sub_f32 v222, 0, v208\n"
    "v_mfma_f32_16x16x32_bf16 v[64:67], v[164:167], v[204:207], 0\n"
    "v_sub_f32 v223, 0, v208\n"
    "v_exp_f32 v192, v192\n"
    "v_exp_f32 v193, v193\n"
    "v_mfma_f32_16x16x32_bf16 v[52:55], v[160:163], v[204:207], 0\n"
    "v_exp_f32 v194, v194\n"
    "v_add_f32 v226, v192, v193\n"
    "v_exp_f32 v195, v195\n"
    "v_mfma_f32_16x16x32_bf16 v[40:43], v[156:159], v[204:207], 0\n"
    "v_cvt_pk_bf16_f32 v208, v192, v193\n"
    "v_add_f32 v226, v226, v194\n"
    "v_exp_f32 v196, v196\n"
    "v_mfma_f32_16x16x32_bf16 v[28:31], v[152:155], v[204:207], 0\n"
    "v_add_f32 v226, v226, v195\n"
    "v_exp_f32 v197, v197\n"
    "v_cvt_pk_bf16_f32 v209, v194, v195\n"
    "v_add_f32 v226, v226, v196\n"
    "v_mfma_f32_16x16x32_bf16 v[16:19], v[148:151], v[204:207], 0\n"
    "v_exp_f32 v198, v198\n"
    "v_add_f32 v226, v226, v197\n"
    "v_exp_f32 v199, v199\n"
    "v_mfma_f32_16x16x32_bf16 v[4:7], v[144:147], v[204:207], 0\n"
    "v_cvt_pk_bf16_f32 v210, v196, v197\n"
    "v_add_f32 v226, v226, v198\n"
    "v_add_f32 v226, v226, v199\n"
    "v_cvt_pk_bf16_f32 v211, v198, v199\n"
    "s_waitcnt vmcnt(8)\n"
    "s_nop 0\n"
    "v_mfma_f32_16x16x32_bf16 v[8:11], v[144:147], v[208:211], 0\n"
    "ds_read_b128 v[144:147], v238 offset:16384\n"
    "v_mfma_f32_16x16x32_bf16 v[20:23], v[148:151], v[208:211], 0\n"
    "ds_read_b128 v[148:151], v239 offset:16384\n"
    "v_mfma_f32_16x16x32_bf16 v[32:35], v[152:155], v[208:211], 0\n"
    "ds_read_b128 v[152:155], v240 offset:16384\n"
    "v_mfma_f32_16x16x32_bf16 v[44:47], v[156:159], v[208:211], 0\n"
    "ds_read_b128 v[156:159], v241 offset:16384\n"
    "s_add_u32 s49, s49, 1\n"
    "v_mfma_f32_16x16x32_bf16 v[56:59], v[160:163], v[208:211], 0\n"
    "ds_read_b128 v[160:163], v241 offset:20480\n"
    "v_mfma_f32_16x16x32_bf16 v[68:71], v[164:167], v[208:211], 0\n"
    "ds_read_b128 v[164:167], v240 offset:20480\n"
    "v_mfma_f32_16x16x32_bf16 v[80:83], v[168:171], v[208:211], 0\n"
    "ds_read_b128 v[168:171], v239 offset:20480\n"
    "v_mfma_f32_16x16x32_bf16 v[92:95], v[172:175], v[208:211], 0\n"
    "ds_read_b128 v[172:175], v238 offset:20480\n"
    ".Lloopm:\n"
    "s_waitcnt lgkmcnt(6)\n"
    "v_mfma_f32_16x16x32_bf16 v[176:179], v[144:147], v[96:99], v[212:215]\n"
    "v_readlane_b32 s67, v244, s49\n"
    "v_mfma_f32_16x16x32_bf16 v[176:179], v[148:151], v[108:111], v[176:179]\n"
    "s_add_u32 s4, s67, 3072\n"
    "s_waitcnt lgkmcnt(4)\n"
    "v_mfma_f32_16x16x32_bf16 v[176:179], v[152:155], v[120:123], v[176:179]\n"
    "s_add_u32 s5, s67, 2176\n"
    "v_mfma_f32_16x16x32_bf16 v[176:179], v[156:159], v[132:135], v[176:179]\n"
    "s_add_u32 s6, s67, 66560\n"
    "s_add_u32 s7, s67, 65664\n"
    "s_waitcnt lgkmcnt(2)\n"
    "v_mfma_f32_16x16x32_bf16 v[180:183], v[160:163], v[132:135], v[212:215]\n"
    "s_add_u32 s8, s67, 7168\n"
    "v_mfma_f32_16x16x32_bf16 v[180:183], v[164:167], v[120:123], v[180:183]\n"
    "s_add_u32 s9, s67, 6272\n"
    "s_waitcnt lgkmcnt(0)\n"
    "v_mfma_f32_16x16x32_bf16 v[180:183], v[168:171], v[108:111], v[180:183]\n"
    "s_add_u32 s10, s67, 70656\n"
    "v_mfma_f32_16x16x32_bf16 v[180:183], v[172:175], v[96:99], v[180:183]\n"
    "s_add_u32 s11, s67, 69760\n"
    "s_mov_b64 exec, 1\n"
    ".Lpa_32:\n"
    "ds_read_b64 v[246:247], v253 offset:8\n"
    "s_memrealtime s[70:71]\n"
    "s_waitcnt lgkmcnt(0)\n"
    "v_readfirstlane_b32 s64, v246\n"
    "v_readfirstlane_b32 s68, v247\n"
    "s_sub_u32 s65, s70, s86\n"
    "s_cmp_ge_i32 s68, s55\n"
    "s_cbranch_scc0 .Lpc_34\n"
    "s_cmp_eq_u32 s64, 0\n"
    "s_cbranch_scc0 .Lpd_35\n"
    ".Lpb_33:\n"
    "s_mov_b64 exec, -1\n"
    "s_mov_b32 s63, s65\n"
    "s_add_u32 m0, s48, 8192\n"
    "s_add_u32 s55, s55, 4\n"
    "buffer_load_dwordx4 v245, s[32:35], s4 offen offset:0 nt lds\n"
    "buffer_load_dwordx4 v245, s[32:35], s5 offen offset:1024 nt lds\n"
    "buffer_load_dwordx4 v245, s[32:35], s6 offen offset:2048 nt lds\n"
    "buffer_load_dwordx4 v245, s[32:35], s7 offen offset:3072 nt lds\n"
    "s_add_u32 m0, s48, 12288\n"
    "s_nop 0\n"
    "buffer_load_dwordx4 v245, s[32:35], s8 offen offset:0 nt lds\n"
    "buffer_load_dwordx4 v245, s[32:35], s9 offen offset:1024 nt lds\n"
    "buffer_load_dwordx4 v245, s[32:35], s10 offen offset:2048 nt lds\n"
    "buffer_load_dwordx4 v245, s[32:35], s11 offen offset:3072 nt lds\n"
    "s_mov_b64 exec, 1\n"
    "ds_add_u32 v253, v252 offset:12\n"
    "s_mov_b64 exec, -1\n"
    ".Li_31:\n"
    "v_mfma_f32_16x16x32_bf16 v[188:191], v[172:175], v[100:103], v[216:219]\n"
    "v_exp_f32 v230, v176\n"
    "v_exp_f32 v231, v177\n"
    "v_mfma_f32_16x16x32_bf16 v[188:191], v[168:171], v[112:115], v[188:191]\n"
    "v_exp_f32 v232, v178\n"
    "v_add_f32 v227, v230, v231\n"
    "v_mfma_f32_16x16x32_bf16 v[188:191], v[164:167], v[124:127], v[188:191]\n"
    "v_exp_f32 v233, v179\n"
    "v_cvt_pk_bf16_f32 v200, v230, v231\n"
    "v_add_f32 v227, v227, v232\n"
    "v_mfma_f32_16x16x32_bf16 v[188:191], v[160:163], v[136:139], v[188:191]\n"
    "v_exp_f32 v234, v180\n"
    "v_add_f32 v227, v227, v233\n"
    "v_mfma_f32_16x16x32_bf16 v[184:187], v[156:159], v[136:139], v[216:219]\n"
    "v_exp_f32 v235, v181\n"
    "v_cvt_pk_bf16_f32 v201, v232, v233\n"
    "v_add_f32 v227, v227, v234\n"
    "v_mfma_f32_16x16x32_bf16 v[184:187], v[152:155], v[124:127], v[184:187]\n"
    "v_exp_f32 v230, v182\n"
    "v_add_f32 v227, v227, v235\n"
    "v_mfma_f32_16x16x32_bf16 v[184:187], v[148:151], v[112:115], v[184:187]\n"
    "v_exp_f32 v231, v183\n"
    "v_cvt_pk_bf16_f32 v202, v234, v235\n"
    "v_add_f32 v227, v227, v230\n"
    "v_mfma_f32_16x16x32_bf16 v[184:187], v[144:147], v[100:103], v[184:187]\n"
    "v_add_f32 v227, v227, v231\n"
    "v_cvt_pk_bf16_f32 v203, v230, v231\n"
    "v_cmp_nge_f32 vcc, s57, v227\n"
    "s_cbranch_vccnz .Lslow0_0\n"
    ".Lback0_0:\n"
    "s_waitcnt vmcnt(8)\n"
    "v_mfma_f32_16x16x32_bf16 v[192:195], v[144:147], v[104:107], v[220:223]\n"
    "ds_read_b64_tr_b16 v[144:145], v242 offset:0\n"
    "ds_read_b64_tr_b16 v[146:147], v242 offset:4096\n"
    "v_add_f32 v224, v224, v227\n"
    "v_mfma_f32_16x16x32_bf16 v[192:195], v[148:151], v[116:119], v[192:195]\n"
    "ds_read_b64_tr_b16 v[148:149], v243 offset:0\n"
    "ds_read_b64_tr_b16 v[150:151], v243 offset:4096\n"
    "v_exp_f32 v232, v188\n"
    "v_mfma_f32_16x16x32_bf16 v[192:195], v[152:155], v[128:131], v[192:195]\n"
    "ds_read_b64_tr_b16 v[152:153], v248 offset:0\n"
    "ds_read_b64_tr_b16 v[154:155], v248 offset:4096\n"
    "v_exp_f32 v233, v189\n"
    "v_mfma_f32_16x16x32_bf16 v[192:195], v[156:159], v[140:143], v[192:195]\n"
    "ds_read_b64_tr_b16 v[156:157], v249 offset:0\n"
    "ds_read_b64_tr_b16 v[158:159], v249 offset:4096\n"
    "v_exp_f32 v234, v190\n"
    "v_mfma_f32_16x16x32_bf16 v[196:199], v[160:163], v[140:143], v[220:223]\n"
    "ds_read_b64_tr_b16 v[160:161], v242 offset:1024\n"
    "ds_read_b64_tr_b16 v[162:163], v242 offset:5120\n"
    "v_add_f32 v228, v232, v233\n"
    "v_mfma_f32_16x16x32_bf16 v[196:199], v[164:167], v[128:131], v[196:199]\n"
    "ds_read_b64_tr_b16 v[164:165], v243 offset:1024\n"
    "ds_read_b64_tr_b16 v[166:167], v243 offset:5120\n"
    "v_exp_f32 v235, v191\n"
    "v_cvt_pk_bf16_f32 v206, v232, v233\n"
    "v_mfma_f32_16x16x32_bf16 v[196:199], v[168:171], v[116:119], v[196:199]\n"
    "ds_read_b64_tr_b16 v[168:169], v248 offset:1024\n"
    "ds_read_b64_tr_b16 v[170:171], v248 offset:5120\n"
    "v_add_f32 v228, v228, v234\n"
    "v_mfma_f32_16x16x32_bf16 v[196:199], v[172:175], v[104:107], v[196:199]\n"
    "ds_read_b64_tr_b16 v[172:173], v249 offset:1024\n"
    "ds_read_b64_tr_b16 v[174:175], v249 offset:5120\n"
    "v_exp_f32 v230, v184\n"
    "s_waitcnt lgkmcnt(8)\n"
    "v_mfma_f32_16x16x32_bf16 v[0:3], v[144:147], v[200:203], v[0:3]\n"
    "v_add_f32 v228, v228, v235\n"
    "v_exp_f32 v231, v185\n"
    "v_mfma_f32_16x16x32_bf16 v[12:15], v[148:151], v[200:203], v[12:15]\n"
    "v_cvt_pk_bf16_f32 v207, v234, v235\n"
    "v_add_f32 v228, v228, v230\n"
    "v_exp_f32 v232, v186\n"
    "v_mfma_f32_16x16x32_bf16 v[24:27], v[152:155], v[200:203], v[24:27]\n"
    "v_add_f32 v228, v228, v231\n"
    "v_exp_f32 v233, v187\n"
    "v_mfma_f32_16x16x32_bf16 v[36:39], v[156:159], v[200:203], v[36:39]\n"
    "v_cvt_pk_bf16_f32 v204, v230, v231\n"
    "v_add_f32 v228, v228, v232\n"
    "s_waitcnt lgkmcnt(0)\n"
    "v_mfma_f32_16x16x32_bf16 v[48:51], v[160:163], v[200:203], v[48:51]\n"
    "v_add_f32 v228, v228, v233\n"
    "v_cvt_pk_bf16_f32 v205, v232, v233\n"
    "v_cmp_nge_f32 vcc, s57, v228\n"
    "v_mfma_f32_16x16x32_bf16 v[60:63], v[164:167], v[200:203], v[60:63]\n"
    "v_exp_f32 v234, v192\n"
    "v_exp_f32 v235, v193\n"
    "v_mfma_f32_16x16x32_bf16 v[72:75], v[168:171], v[200:203], v[72:75]\n"
    "v_exp_f32 v230, v194\n"
    "v_mfma_f32_16x16x32_bf16 v[84:87], v[172:175], v[200:203], v[84:87]\n"
    "v_add_f32 v229, v234, v235\n"
    "v_exp_f32 v231, v195\n"
    "s_cbranch_vccnz .Lslow1_0\n"
    ".Lback1_0:\n"
    "s_mov_b64 exec, 1\n"
    ".Lpa_37:\n"
    "ds_read_b64 v[246:247], v253 offset:8\n"
    "s_memrealtime s[70:71]\n"
    "s_waitcnt lgkmcnt(0)\n"
    "v_readfirstlane_b32 s64, v246\n"
    "v_readfirstlane_b32 s68, v247\n"
    "s_sub_u32 s65, s70, s86\n"
    "s_cmp_ge_i32 s68, s55\n"
    "s_cbranch_scc0 .Lpc_39\n"
    "s_cmp_eq_u32 s64, 0\n"
    "s_cbranch_scc0 .Lpd_40\n"
    ".Lpb_38:\n"
    "s_mov_b64 exec, -1\n"
    "s_mov_b32 s63, s65\n"
    "s_cmp_ge_u32 s65, s90\n"
    "s_cbranch_scc1 .Ltw_41\n"
    ".Ltb_42:\n"
    "s_add_u32 m0, s48, 16384\n"
    "s_add_u32 s55, s55, 4\n"
    "buffer_load_dwordx4 v254, s[36:39], s4 offen offset:0 nt lds\n"
    "buffer_load_dwordx4 v254, s[36:39], s5 offen offset:1024 nt lds\n"
    "buffer_load_dwordx4 v254, s[36:39], s6 offen offset:2048 nt lds\n"
    "buffer_load_dwordx4 v254, s[36:39], s7 offen offset:3072 nt lds\n"
    "s_add_u32 m0, s48, 20480\n"
    "s_nop 0\n"
    "buffer_load_dwordx4 v254, s[36:39], s8 offen offset:0 nt lds\n"
    "buffer_load_dwordx4 v254, s[36:39], s9 offen offset:1024 nt lds\n"
    "buffer_load_dwordx4 v254, s[36:39], s10 offen offset:2048 nt lds\n"
    "buffer_load_dwordx4 v254, s[36:39], s11 offen offset:3072 nt lds\n"
    "s_mov_b64 exec, 1\n"
    "ds_add_u32 v253, v252 offset:12\n"
    "s_mov_b64 exec, -1\n"
    ".Li_36:\n"
    "v_mfma_f32_16x16x32_bf16 v[88:91], v[172:175], v[204:207], v[88:91]\n"
    "v_add_f32 v225, v225, v228\n"
    "v_cvt_pk_bf16_f32 v208, v234, v235\n"
    "v_add_f32 v229, v229, v230\n"
    "v_mfma_f32_16x16x32_bf16 v[76:79], v[168:171], v[204:207], v[76:79]\n"
    "v_exp_f32 v232, v196\n"
    "v_mfma_f32_16x16x32_bf16 v[64:67], v[164:167], v[204:207], v[64:67]\n"
    "v_add_f32 v229, v229, v231\n"
    "v_exp_f32 v233, v197\n"
    "v_mfma_f32_16x16x32_bf16 v[52:55], v[160:163], v[204:207], v[52:55]\n"
    "v_cvt_pk_bf16_f32 v209, v230, v231\n"
    "v_add_f32 v229, v229, v232\n"
    "v_mfma_f32_16x16x32_bf16 v[40:43], v[156:159], v[204:207], v[40:43]\n"
    "v_exp_f32 v234, v198\n"
    "v_add_f32 v229, v229, v233\n"
    "v_mfma_f32_16x16x32_bf16 v[28:31], v[152:155], v[204:207], v[28:31]\n"
    "v_exp_f32 v235, v199\n"
    "v_mfma_f32_16x16x32_bf16 v[16:19], v[148:151], v[204:207], v[16:19]\n"
    "v_cvt_pk_bf16_f32 v210, v232, v233\n"
    "v_add_f32 v229, v229, v234\n"
    "v_add_f32 v229, v229, v235\n"
    "v_mfma_f32_16x16x32_bf16 v[4:7], v[144:147], v[204:207], v[4:7]\n"
    "v_cvt_pk_bf16_f32 v211, v234, v235\n"
    "v_cmp_nge_f32 vcc, s57, v229\n"
    "s_cbranch_vccnz .Lslow2_0\n"
    ".Lback2_0:\n"
    "s_waitcnt vmcnt(8)\n"
    "v_mfma_f32_16x16x32_bf16 v[8:11], v[144:147], v[208:211], v[8:11]\n"
    "ds_read_b128 v[144:147], v238 offset:8192\n"
    "v_mfma_f32_16x16x32_bf16 v[20:23], v[148:151], v[208:211], v[20:23]\n"
    "ds_read_b128 v[148:151], v239 offset:8192\n"
    "v_add_f32 v226, v226, v229\n"
    "v_mfma_f32_16x16x32_bf16 v[32:35], v[152:155], v[208:211], v[32:35]\n"
    "ds_read_b128 v[152:155], v240 offset:8192\n"
    "v_mfma_f32_16x16x32_bf16 v[44:47], v[156:159], v[208:211], v[44:47]\n"
    "ds_read_b128 v[156:159], v241 offset:8192\n"
    "v_mfma_f32_16x16x32_bf16 v[56:59], v[160:163], v[208:211], v[56:59]\n"
    "ds_read_b128 v[160:163], v241 offset:12288\n"
    "v_mfma_f32_16x16x32_bf16 v[68:71], v[164:167], v[208:211], v[68:71]\n"
    "ds_read_b128 v[164:167], v240 offset:12288\n"
    "s_add_u32 s49, s49, 1\n"
    "v_mfma_f32_16x16x32_bf16 v[80:83], v[168:171], v[208:211], v[80:83]\n"
    "ds_read_b128 v[168:171], v239 offset:12288\n"
    "v_mfma_f32_16x16x32_bf16 v[92:95], v[172:175], v[208:211], v[92:95]\n"
    "ds_read_b128 v[172:175], v238 offset:12288\n"
    "s_waitcnt lgkmcnt(6)\n"
    "v_mfma_f32_16x16x32_bf16 v[176:179], v[144:147], v[96:99], v[212:215]\n"
    "v_readlane_b32 s67, v244, s49\n"
    "v_mfma_f32_16x16x32_bf16 v[176:179], v[148:151], v[108:111], v[176:179]\n"
    "s_add_u32 s4, s67, 3072\n"
    "s_waitcnt lgkmcnt(4)\n"
    "v_mfma_f32_16x16x32_bf16 v[176:179], v[152:155], v[120:123], v[176:179]\n"
    "s_add_u32 s5, s67, 2176\n"
    "v_mfma_f32_16x16x32_bf16 v[176:179], v[156:159], v[132:135], v[176:179]\n"
    "s_add_u32 s6, s67, 66560\n"
    "s_add_u32 s7, s67, 65664\n"
    "s_waitcnt lgkmcnt(2)\n"
    "v_mfma_f32_16x16x32_bf16 v[180:183], v[160:163], v[132:135], v[212:215]\n"
    "s_add_u32 s8, s67, 7168\n"
    "v_mfma_f32_16x16x32_bf16 v[180:183], v[164:167], v[120:123], v[180:183]\n"
    "s_add_u32 s9, s67, 6272\n"
    "s_waitcnt lgkmcnt(0)\n"
    "v_mfma_f32_16x16x32_bf16 v[180:183], v[168:171], v[108:111], v[180:183]\n"
    "s_add_u32 s10, s67, 70656\n"
    "v_mfma_f32_16x16x32_bf16 v[180:183], v[172:175], v[96:99], v[180:183]\n"
    "s_add_u32 s11, s67, 69760\n"
    "s_mov_b64 exec, 1\n"
    ".Lpa_44:\n"
    "ds_read_b64 v[246:247], v253 offset:8\n"
    "s_memrealtime s[70:71]\n"
    "s_waitcnt lgkmcnt(0)\n"
    "v_readfirstlane_b32 s64, v246\n"
    "v_readfirstlane_b32 s68, v247\n"
    "s_sub_u32 s65, s70, s86\n"
    "s_cmp_ge_i32 s68, s55\n"
    "s_cbranch_scc0 .Lpc_46\n"
    "s_cmp_eq_u32 s64, 0\n"
    "s_cbranch_scc0 .Lpd_47\n"
    ".Lpb_45:\n"
    "s_mov_b64 exec, -1\n"
    "s_mov_b32 s63, s65\n"
    "s_add_u32 m0, s48, 0\n"
    "s_add_u32 s55, s55, 4\n"
    "buffer_load_dwordx4 v245, s[32:35], s4 offen offset:0 nt lds\n"
    "buffer_load_dwordx4 v245, s[32:35], s5 offen offset:1024 nt lds\n"
    "buffer_load_dwordx4 v245, s[32:35], s6 offen offset:2048 nt lds\n"
    "buffer_load_dwordx4 v245, s[32:35], s7 offen offset:3072 nt lds\n"
    "s_add_u32 m0, s48, 4096\n"
    "s_nop 0\n"
    "buffer_load_dwordx4 v245, s[32:35], s8 offen offset:0 nt lds\n"
    "buffer_load_dwordx4 v245, s[32:35], s9 offen offset:1024 nt lds\n"
    "buffer_load_dwordx4 v245, s[32:35], s10 offen offset:2048 nt lds\n"
    "buffer_load_dwordx4 v245, s[32:35], s11 offen offset:3072 nt lds\n"
    "s_mov_b64 exec, 1\n"
    "ds_add_u32 v253, v252 offset:12\n"
    "s_mov_b64 exec, -1\n"
    ".Li_43:\n"
    "v_mfma_f32_16x16x32_bf16 v[188:191], v[172:175], v[100:103], v[216:219]\n"
    "v_exp_f32 v230, v176\n"
    "v_exp_f32 v231, v177\n"
    "v_mfma_f32_16x16x32_bf16 v[188:191], v[168:171], v[112:115], v[188:191]\n"
    "v_exp_f32 v232, v178\n"
    "v_add_f32 v227, v230, v231\n"
    "v_mfma_f32_16x16x32_bf16 v[188:191], v[164:167], v[124:127], v[188:191]\n"
    "v_exp_f32 v233, v179\n"
    "v_cvt_pk_bf16_f32 v200, v230, v231\n"
    "v_add_f32 v227, v227, v232\n"
    "v_mfma_f32_16x16x32_bf16 v[188:191], v[160:163], v[136:139], v[188:191]\n"
    "v_exp_f32 v234, v180\n"
    "v_add_f32 v227, v227, v233\n"
    "v_mfma_f32_16x16x32_bf16 v[184:187], v[156:159], v[136:139], v[216:219]\n"
    "v_exp_f32 v235, v181\n"
    "v_cvt_pk_bf16_f32 v201, v232, v233\n"
    "v_add_f32 v227, v227, v234\n"
    "v_mfma_f32_16x16x32_bf16 v[184:187], v[152:155], v[124:127], v[184:187]\n"
    "v_exp_f32 v230, v182\n"
    "v_add_f32 v227, v227, v235\n"
    "v_mfma_f32_16x16x32_bf16 v[184:187], v[148:151], v[112:115], v[184:187]\n"
    "v_exp_f32 v231, v183\n"
    "v_cvt_pk_bf16_f32 v202, v234, v235\n"
    "v_add_f32 v227, v227, v230\n"
    "v_mfma_f32_16x16x32_bf16 v[184:187], v[144:147], v[100:103], v[184:187]\n"
    "v_add_f32 v227, v227, v231\n"
    "v_cvt_pk_bf16_f32 v203, v230, v231\n"
    "v_cmp_nge_f32 vcc, s57, v227\n"
    "s_cbranch_vccnz .Lslow0_1\n"
    ".Lback0_1:\n"
    "s_waitcnt vmcnt(8)\n"
    "v_mfma_f32_16x16x32_bf16 v[192:195], v[144:147], v[104:107], v[220:223]\n"
    "ds_read_b64_tr_b16 v[144:145], v242 offset:16384\n"
    "ds_read_b64_tr_b16 v[146:147], v242 offset:20480\n"
    "v_add_f32 v224, v224, v227\n"
    "v_mfma_f32_16x16x32_bf16 v[192:195], v[148:151], v[116:119], v[192:195]\n"
    "ds_read_b64_tr_b16 v[148:149], v243 offset:16384\n"
    "ds_read_b64_tr_b16 v[150:151], v243 offset:20480\n"
    "v_exp_f32 v232, v188\n"
    "v_mfma_f32_16x16x32_bf16 v[192:195], v[152:155], v[128:131], v[192:195]\n"
    "ds_read_b64_tr_b16 v[152:153], v248 offset:16384\n"
    "ds_read_b64_tr_b16 v[154:155], v248 offset:20480\n"
    "v_exp_f32 v233, v189\n"
    "v_mfma_f32_16x16x32_bf16 v[192:195], v[156:159], v[140:143], v[192:195]\n"
    "ds_read_b64_tr_b16 v[156:157], v249 offset:16384\n"
    "ds_read_b64_tr_b16 v[158:159], v249 offset:20480\n"
    "v_exp_f32 v234, v190\n"
    "v_mfma_f32_16x16x32_bf16 v[196:199], v[160:163], v[140:143], v[220:223]\n"
    "ds_read_b64_tr_b16 v[160:161], v242 offset:17408\n"
    "ds_read_b64_tr_b16 v[162:163], v242 offset:21504\n"
    "v_add_f32 v228, v232, v233\n"
    "v_mfma_f32_16x16x32_bf16 v[196:199], v[164:167], v[128:131], v[196:199]\n"
    "ds_read_b64_tr_b16 v[164:165], v243 offset:17408\n"
    "ds_read_b64_tr_b16 v[166:167], v243 offset:21504\n"
    "v_exp_f32 v235, v191\n"
    "v_cvt_pk_bf16_f32 v206, v232, v233\n"
    "v_mfma_f32_16x16x32_bf16 v[196:199], v[168:171], v[116:119], v[196:199]\n"
    "ds_read_b64_tr_b16 v[168:169], v248 offset:17408\n"
    "ds_read_b64_tr_b16 v[170:171], v248 offset:21504\n"
    "v_add_f32 v228, v228, v234\n"
    "v_mfma_f32_16x16x32_bf16 v[196:199], v[172:175], v[104:107], v[196:199]\n"
    "ds_read_b64_tr_b16 v[172:173], v249 offset:17408\n"
    "ds_read_b64_tr_b16 v[174:175], v249 offset:21504\n"
    "v_exp_f32 v230, v184\n"
    "s_waitcnt lgkmcnt(8)\n"
    "v_mfma_f32_16x16x32_bf16 v[0:3], v[144:147], v[200:203], v[0:3]\n"
    "v_add_f32 v228, v228, v235\n"
    "v_exp_f32 v231, v185\n"
    "v_mfma_f32_16x16x32_bf16 v[12:15], v[148:151], v[200:203], v[12:15]\n"
    "v_cvt_pk_bf16_f32 v207, v234, v235\n"
    "v_add_f32 v228, v228, v230\n"
    "v_exp_f32 v232, v186\n"
    "v_mfma_f32_16x16x32_bf16 v[24:27], v[152:155], v[200:203], v[24:27]\n"
    "v_add_f32 v228, v228, v231\n"
    "v_exp_f32 v233, v187\n"
    "v_mfma_f32_16x16x32_bf16 v[36:39], v[156:159], v[200:203], v[36:39]\n"
    "v_cvt_pk_bf16_f32 v204, v230, v231\n"
    "v_add_f32 v228, v228, v232\n"
    "s_waitcnt lgkmcnt(0)\n"
    "v_mfma_f32_16x16x32_bf16 v[48:51], v[160:163], v[200:203], v[48:51]\n"
    "v_add_f32 v228, v228, v233\n"
    "v_cvt_pk_bf16_f32 v205, v232, v233\n"
    "v_cmp_nge_f32 vcc, s57, v228\n"
    "v_mfma_f32_16x16x32_bf16 v[60:63], v[164:167], v[200:203], v[60:63]\n"
    "v_exp_f32 v234, v192\n"
    "v_exp_f32 v235, v193\n"
    "v_mfma_f32_16x16x32_bf16 v[72:75], v[168:171], v[200:203], v[72:75]\n"
    "v_exp_f32 v230, v194\n"
    "v_mfma_f32_16x16x32_bf16 v[84:87], v[172:175], v[200:203], v[84:87]\n"
    "v_add_f32 v229, v234, v235\n"
    "v_exp_f32 v231, v195\n"
    "s_cbranch_vccnz .Lslow1_1\n"
    ".Lback1_1:\n"
    "s_mov_b64 exec, 1\n"
    ".Lpa_49:\n"
    "ds_read_b64 v[246:247], v253 offset:8\n"
    "s_memrealtime s[70:71]\n"
    "s_waitcnt lgkmcnt(0)\n"
    "v_readfirstlane_b32 s64, v246\n"
    "v_readfirstlane_b32 s68, v247\n"
    "s_sub_u32 s65, s70, s86\n"
    "s_cmp_ge_i32 s68, s55\n"
    "s_cbranch_scc0 .Lpc_51\n"
    "s_cmp_eq_u32 s64, 0\n"
    "s_cbranch_scc0 .Lpd_52\n"
    ".Lpb_50:\n"
    "s_mov_b64 exec, -1\n"
    "s_mov_b32 s63, s65\n"
    "s_cmp_ge_u32 s65, s90\n"
    "s_cbranch_scc1 .Ltw_53\n"
    ".Ltb_54:\n"
    "s_add_u32 m0, s48, 8192\n"
    "s_add_u32 s55, s55, 4\n"
    "buffer_load_dwordx4 v254, s[36:39], s4 offen offset:0 nt lds\n"
    "buffer_load_dwordx4 v254, s[36:39], s5 offen offset:1024 nt lds\n"
    "buffer_load_dwordx4 v254, s[36:39], s6 offen offset:2048 nt lds\n"
    "buffer_load_dwordx4 v254, s[36:39], s7 offen offset:3072 nt lds\n"
    "s_add_u32 m0, s48, 12288\n"
    "s_nop 0\n"
    "buffer_load_dwordx4 v254, s[36:39], s8 offen offset:0 nt lds\n"
    "buffer_load_dwordx4 v254, s[36:39], s9 offen offset:1024 nt lds\n"
    "buffer_load_dwordx4 v254, s[36:39], s10 offen offset:2048 nt lds\n"
    "buffer_load_dwordx4 v254, s[36:39], s11 offen offset:3072 nt lds\n"
    "s_mov_b64 exec, 1\n"
    "ds_add_u32 v253, v252 offset:12\n"
    "s_mov_b64 exec, -1\n"
    ".Li_48:\n"
    "v_mfma_f32_16x16x32_bf16 v[88:91], v[172:175], v[204:207], v[88:91]\n"
    "v_add_f32 v225, v225, v228\n"
    "v_cvt_pk_bf16_f32 v208, v234, v235\n"
    "v_add_f32 v229, v229, v230\n"
    "v_mfma_f32_16x16x32_bf16 v[76:79], v[168:171], v[204:207], v[76:79]\n"
    "v_exp_f32 v232, v196\n"
    "v_mfma_f32_16x16x32_bf16 v[64:67], v[164:167], v[204:207], v[64:67]\n"
    "v_add_f32 v229, v229, v231\n"
    "v_exp_f32 v233, v197\n"
    "v_mfma_f32_16x16x32_bf16 v[52:55], v[160:163], v[204:207], v[52:55]\n"
    "v_cvt_pk_bf16_f32 v209, v230, v231\n"
    "v_add_f32 v229, v229, v232\n"
    "v_mfma_f32_16x16x32_bf16 v[40:43], v[156:159], v[204:207], v[40:43]\n"
    "v_exp_f32 v234, v198\n"
    "v_add_f32 v229, v229, v233\n"
    "v_mfma_f32_16x16x32_bf16 v[28:31], v[152:155], v[204:207], v[28:31]\n"
    "v_exp_f32 v235, v199\n"
    "v_mfma_f32_16x16x32_bf16 v[16:19], v[148:151], v[204:207], v[16:19]\n"
    "v_cvt_pk_bf16_f32 v210, v232, v233\n"
    "v_add_f32 v229, v229, v234\n"
    "v_add_f32 v229, v229, v235\n"
    "v_mfma_f32_16x16x32_bf16 v[4:7], v[144:147], v[204:207], v[4:7]\n"
    "v_cvt_pk_bf16_f32 v211, v234, v235\n"
    "v_cmp_nge_f32 vcc, s57, v229\n"
    "s_cbranch_vccnz .Lslow2_1\n"
    ".Lback2_1:\n"
    "s_waitcnt vmcnt(8)\n"
    "v_mfma_f32_16x16x32_bf16 v[8:11], v[144:147], v[208:211], v[8:11]\n"
    "ds_read_b128 v[144:147], v238 offset:0\n"
    "v_mfma_f32_16x16x32_bf16 v[20:23], v[148:151], v[208:211], v[20:23]\n"
    "ds_read_b128 v[148:151], v239 offset:0\n"
    "v_add_f32 v226, v226, v229\n"
    "v_mfma_f32_16x16x32_bf16 v[32:35], v[152:155], v[208:211], v[32:35]\n"
    "ds_read_b128 v[152:155], v240 offset:0\n"
    "v_mfma_f32_16x16x32_bf16 v[44:47], v[156:159], v[208:211], v[44:47]\n"
    "ds_read_b128 v[156:159], v241 offset:0\n"
    "s_add_u32 s49, s49, 1\n"
    "v_mfma_f32_16x16x32_bf16 v[56:59], v[160:163], v[208:211], v[56:59]\n"
    "ds_read_b128 v[160:163], v241 offset:4096\n"
    "v_mfma_f32_16x16x32_bf16 v[68:71], v[164:167], v[208:211], v[68:71]\n"
    "ds_read_b128 v[164:167], v240 offset:4096\n"
    "v_mfma_f32_16x16x32_bf16 v[80:83], v[168:171], v[208:211], v[80:83]\n"
    "ds_read_b128 v[168:171], v239 offset:4096\n"
    "s_cmp_lt_u32 s49, 15\n"
    "v_mfma_f32_16x16x32_bf16 v[92:95], v[172:175], v[208:211], v[92:95]\n"
    "ds_read_b128 v[172:175], v238 offset:4096\n"
    "s_cbranch_scc0 .Lexitm\n"
    "s_waitcnt lgkmcnt(6)\n"
    "v_mfma_f32_16x16x32_bf16 v[176:179], v[144:147], v[96:99], v[212:215]\n"
    "v_readlane_b32 s67, v244, s49\n"
    "v_mfma_f32_16x16x32_bf16 v[176:179], v[148:151], v[108:111], v[176:179]\n"
    "s_add_u32 s4, s67, 3072\n"
    "s_waitcnt lgkmcnt(4)\n"
    "v_mfma_f32_16x16x32_bf16 v[176:179], v[152:155], v[120:123], v[176:179]\n"
    "s_add_u32 s5, s67, 2176\n"
    "v_mfma_f32_16x16x32_bf16 v[176:179], v[156:159], v[132:135], v[176:179]\n"
    "s_add_u32 s6, s67, 66560\n"
    "s_add_u32 s7, s67, 65664\n"
    "s_waitcnt lgkmcnt(2)\n"
    "v_mfma_f32_16x16x32_bf16 v[180:183], v[160:163], v[132:135], v[212:215]\n"
    "s_add_u32 s8, s67, 7168\n"
    "v_mfma_f32_16x16x32_bf16 v[180:183], v[164:167], v[120:123], v[180:183]\n"
    "s_add_u32 s9, s67, 6272\n"
    "s_waitcnt lgkmcnt(0)\n"
    "v_mfma_f32_16x16x32_bf16 v[180:183], v[168:171], v[108:111], v[180:183]\n"
    "s_add_u32 s10, s67, 70656\n"
    "v_mfma_f32_16x16x32_bf16 v[180:183], v[172:175], v[96:99], v[180:183]\n"
    "s_add_u32 s11, s67, 69760\n"
    "s_mov_b64 exec, 1\n"
    ".Lpa_56:\n"
    "ds_read_b64 v[246:247], v253 offset:8\n"
    "s_memrealtime s[70:71]\n"
    "s_waitcnt lgkmcnt(0)\n"
    "v_readfirstlane_b32 s64, v246\n"
    "v_readfirstlane_b32 s68, v247\n"
    "s_sub_u32 s65, s70, s86\n"
    "s_cmp_ge_i32 s68, s55\n"
    "s_cbranch_scc0 .Lpc_58\n"
    "s_cmp_eq_u32 s64, 0\n"
    "s_cbranch_scc0 .Lpd_59\n"
    ".Lpb_57:\n"
    "s_mov_b64 exec, -1\n"
    "s_mov_b32 s63, s65\n"
    "s_add_u32 m0, s48, 16384\n"
    "s_add_u32 s55, s55, 4\n"
    "buffer_load_dwordx4 v245, s[32:35], s4 offen offset:0 nt lds\n"
    "buffer_load_dwordx4 v245, s[32:35], s5 offen offset:1024 nt lds\n"
    "buffer_load_dwordx4 v245, s[32:35], s6 offen offset:2048 nt lds\n"
    "buffer_load_dwordx4 v245, s[32:35], s7 offen offset:3072 nt lds\n"
    "s_add_u32 m0, s48, 20480\n"
    "s_nop 0\n"
    "buffer_load_dwordx4 v245, s[32:35], s8 offen offset:0 nt lds\n"
    "buffer_load_dwordx4 v245, s[32:35], s9 offen offset:1024 nt lds\n"
    "buffer_load_dwordx4 v245, s[32:35], s10 offen offset:2048 nt lds\n"
    "buffer_load_dwordx4 v245, s[32:35], s11 offen offset:3072 nt lds\n"
    "s_mov_b64 exec, 1\n"
    "ds_add_u32 v253, v252 offset:12\n"
    "s_mov_b64 exec, -1\n"
    ".Li_55:\n"
    "v_mfma_f32_16x16x32_bf16 v[188:191], v[172:175], v[100:103], v[216:219]\n"
    "v_exp_f32 v230, v176\n"
    "v_exp_f32 v231, v177\n"
    "v_mfma_f32_16x16x32_bf16 v[188:191], v[168:171], v[112:115], v[188:191]\n"
    "v_exp_f32 v232, v178\n"
    "v_add_f32 v227, v230, v231\n"
    "v_mfma_f32_16x16x32_bf16 v[188:191], v[164:167], v[124:127], v[188:191]\n"
    "v_exp_f32 v233, v179\n"
    "v_cvt_pk_bf16_f32 v200, v230, v231\n"
    "v_add_f32 v227, v227, v232\n"
    "v_mfma_f32_16x16x32_bf16 v[188:191], v[160:163], v[136:139], v[188:191]\n"
    "v_exp_f32 v234, v180\n"
    "v_add_f32 v227, v227, v233\n"
    "v_mfma_f32_16x16x32_bf16 v[184:187], v[156:159], v[136:139], v[216:219]\n"
    "v_exp_f32 v235, v181\n"
    "v_cvt_pk_bf16_f32 v201, v232, v233\n"
    "v_add_f32 v227, v227, v234\n"
    "v_mfma_f32_16x16x32_bf16 v[184:187], v[152:155], v[124:127], v[184:187]\n"
    "v_exp_f32 v230, v182\n"
    "v_add_f32 v227, v227, v235\n"
    "v_mfma_f32_16x16x32_bf16 v[184:187], v[148:151], v[112:115], v[184:187]\n"
    "v_exp_f32 v231, v183\n"
    "v_cvt_pk_bf16_f32 v202, v234, v235\n"
    "v_add_f32 v227, v227, v230\n"
    "v_mfma_f32_16x16x32_bf16 v[184:187], v[144:147], v[100:103], v[184:187]\n"
    "v_add_f32 v227, v227, v231\n"
    "v_cvt_pk_bf16_f32 v203, v230, v231\n"
    "v_cmp_nge_f32 vcc, s57, v227\n"
    "s_cbranch_vccnz .Lslow0_2\n"
    ".Lback0_2:\n"
    "s_waitcnt vmcnt(8)\n"
    "v_mfma_f32_16x16x32_bf16 v[192:195], v[144:147], v[104:107], v[220:223]\n"
    "ds_read_b64_tr_b16 v[144:145], v242 offset:8192\n"
    "ds_read_b64_tr_b16 v[146:147], v242 offset:12288\n"
    "v_add_f32 v224, v224, v227\n"
    "v_mfma_f32_16x16x32_bf16 v[192:195], v[148:151], v[116:119], v[192:195]\n"
    "ds_read_b64_tr_b16 v[148:149], v243 offset:8192\n"
    "ds_read_b64_tr_b16 v[150:151], v243 offset:12288\n"
    "v_exp_f32 v232, v188\n"
    "v_mfma_f32_16x16x32_bf16 v[192:195], v[152:155], v[128:131], v[192:195]\n"
    "ds_read_b64_tr_b16 v[152:153], v248 offset:8192\n"
    "ds_read_b64_tr_b16 v[154:155], v248 offset:12288\n"
    "v_exp_f32 v233, v189\n"
    "v_mfma_f32_16x16x32_bf16 v[192:195], v[156:159], v[140:143], v[192:195]\n"
    "ds_read_b64_tr_b16 v[156:157], v249 offset:8192\n"
    "ds_read_b64_tr_b16 v[158:159], v249 offset:12288\n"
    "v_exp_f32 v234, v190\n"
    "v_mfma_f32_16x16x32_bf16 v[196:199], v[160:163], v[140:143], v[220:223]\n"
    "ds_read_b64_tr_b16 v[160:161], v242 offset:9216\n"
    "ds_read_b64_tr_b16 v[162:163], v242 offset:13312\n"
    "v_add_f32 v228, v232, v233\n"
    "v_mfma_f32_16x16x32_bf16 v[196:199], v[164:167], v[128:131], v[196:199]\n"
    "ds_read_b64_tr_b16 v[164:165], v243 offset:9216\n"
    "ds_read_b64_tr_b16 v[166:167], v243 offset:13312\n"
    "v_exp_f32 v235, v191\n"
    "v_cvt_pk_bf16_f32 v206, v232, v233\n"
    "v_mfma_f32_16x16x32_bf16 v[196:199], v[168:171], v[116:119], v[196:199]\n"
    "ds_read_b64_tr_b16 v[168:169], v248 offset:9216\n"
    "ds_read_b64_tr_b16 v[170:171], v248 offset:13312\n"
    "v_add_f32 v228, v228, v234\n"
    "v_mfma_f32_16x16x32_bf16 v[196:199], v[172:175], v[104:107], v[196:199]\n"
    "ds_read_b64_tr_b16 v[172:173], v249 offset:9216\n"
    "ds_read_b64_tr_b16 v[174:175], v249 offset:13312\n"
    "v_exp_f32 v230, v184\n"
    "s_waitcnt lgkmcnt(8)\n"
    "v_mfma_f32_16x16x32_bf16 v[0:3], v[144:147], v[200:203], v[0:3]\n"
    "v_add_f32 v228, v228, v235\n"
    "v_exp_f32 v231, v185\n"
    "v_mfma_f32_16x16x32_bf16 v[12:15], v[148:151], v[200:203], v[12:15]\n"
    "v_cvt_pk_bf16_f32 v207, v234, v235\n"
    "v_add_f32 v228, v228, v230\n"
    "v_exp_f32 v232, v186\n"
    "v_mfma_f32_16x16x32_bf16 v[24:27], v[152:155], v[200:203], v[24:27]\n"
    "v_add_f32 v228, v228, v231\n"
    "v_exp_f32 v233, v187\n"
    "v_mfma_f32_16x16x32_bf16 v[36:39], v[156:159], v[200:203], v[36:39]\n"
    "v_cvt_pk_bf16_f32 v204, v230, v231\n"
    "v_add_f32 v228, v228, v232\n"
    "s_waitcnt lgkmcnt(0)\n"
    "v_mfma_f32_16x16x32_bf16 v[48:51], v[160:163], v[200:203], v[48:51]\n"
    "v_add_f32 v228, v228, v233\n"
    "v_cvt_pk_bf16_f32 v205, v232, v233\n"
    "v_cmp_nge_f32 vcc, s57, v228\n"
    "v_mfma_f32_16x16x32_bf16 v[60:63], v[164:167], v[200:203], v[60:63]\n"
    "v_exp_f32 v234, v192\n"
    "v_exp_f32 v235, v193\n"
    "v_mfma_f32_16x16x32_bf16 v[72:75], v[168:171], v[200:203], v[72:75]\n"
    "v_exp_f32 v230, v194\n"
    "v_mfma_f32_16x16x32_bf16 v[84:87], v[172:175], v[200:203], v[84:87]\n"
    "v_add_f32 v229, v234, v235\n"
    "v_exp_f32 v231, v195\n"
    "s_cbranch_vccnz .Lslow1_2\n"
    ".Lback1_2:\n"
    "s_mov_b64 exec, 1\n"
    ".Lpa_61:\n"
    "ds_read_b64 v[246:247], v253 offset:8\n"
    "s_memrealtime s[70:71]\n"
    "s_waitcnt lgkmcnt(0)\n"
    "v_readfirstlane_b32 s64, v246\n"
    "v_readfirstlane_b32 s68, v247\n"
    "s_sub_u32 s65, s70, s86\n"
    "s_cmp_ge_i32 s68, s55\n"
    "s_cbranch_scc0 .Lpc_63\n"
    "s_cmp_eq_u32 s64, 0\n"
    "s_cbranch_scc0 .Lpd_64\n"
    ".Lpb_62:\n"
    "s_mov_b64 exec, -1\n"
    "s_mov_b32 s63, s65\n"
    "s_cmp_ge_u32 s65, s90\n"
    "s_cbranch_scc1 .Ltw_65\n"
    ".Ltb_66:\n"
    "s_add_u32 m0, s48, 0\n"
    "s_add_u32 s55, s55, 4\n"
    "buffer_load_dwordx4 v254, s[36:39], s4 offen offset:0 nt lds\n"
    "buffer_load_dwordx4 v254, s[36:39], s5 offen offset:1024 nt lds\n"
    "buffer_load_dwordx4 v254, s[36:39], s6 offen offset:2048 nt lds\n"
    "buffer_load_dwordx4 v254, s[36:39], s7 offen offset:3072 nt lds\n"
    "s_add_u32 m0, s48, 4096\n"
    "s_nop 0\n"
    "buffer_load_dwordx4 v254, s[36:39], s8 offen offset:0 nt lds\n"
    "buffer_load_dwordx4 v254, s[36:39], s9 offen offset:1024 nt lds\n"
    "buffer_load_dwordx4 v254, s[36:39], s10 offen offset:2048 nt lds\n"
    "buffer_load_dwordx4 v254, s[36:39], s11 offen offset:3072 nt lds\n"
    "s_mov_b64 exec, 1\n"
    "ds_add_u32 v253, v252 offset:12\n"
    "s_mov_b64 exec, -1\n"
    ".Li_60:\n"
    "v_mfma_f32_16x16x32_bf16 v[88:91], v[172:175], v[204:207], v[88:91]\n"
    "v_add_f32 v225, v225, v228\n"
    "v_cvt_pk_bf16_f32 v208, v234, v235\n"
    "v_add_f32 v229, v229, v230\n"
    "v_mfma_f32_16x16x32_bf16 v[76:79], v[168:171], v[204:207], v[76:79]\n"
    "v_exp_f32 v232, v196\n"
    "v_mfma_f32_16x16x32_bf16 v[64:67], v[164:167], v[204:207], v[64:67]\n"
    "v_add_f32 v229, v229, v231\n"
    "v_exp_f32 v233, v197\n"
    "v_mfma_f32_16x16x32_bf16 v[52:55], v[160:163], v[204:207], v[52:55]\n"
    "v_cvt_pk_bf16_f32 v209, v230, v231\n"
    "v_add_f32 v229, v229, v232\n"
    "v_mfma_f32_16x16x32_bf16 v[40:43], v[156:159], v[204:207], v[40:43]\n"
    "v_exp_f32 v234, v198\n"
    "v_add_f32 v229, v229, v233\n"
    "v_mfma_f32_16x16x32_bf16 v[28:31], v[152:155], v[204:207], v[28:31]\n"
    "v_exp_f32 v235, v199\n"
    "v_mfma_f32_16x16x32_bf16 v[16:19], v[148:151], v[204:207], v[16:19]\n"
    "v_cvt_pk_bf16_f32 v210, v232, v233\n"
    "v_add_f32 v229, v229, v234\n"
    "v_add_f32 v229, v229, v235\n"
    "v_mfma_f32_16x16x32_bf16 v[4:7], v[144:147], v[204:207], v[4:7]\n"
    "v_cvt_pk_bf16_f32 v211, v234, v235\n"
    "v_cmp_nge_f32 vcc, s57, v229\n"
    "s_cbranch_vccnz .Lslow2_2\n"
    ".Lback2_2:\n"
    "s_waitcnt vmcnt(8)\n"
    "v_mfma_f32_16x16x32_bf16 v[8:11], v[144:147], v[208:211], v[8:11]\n"
    "ds_read_b128 v[144:147], v238 offset:16384\n"
    "v_mfma_f32_16x16x32_bf16 v[20:23], v[148:151], v[208:211], v[20:23]\n"
    "ds_read_b128 v[148:151], v239 offset:16384\n"
    "v_add_f32 v226, v226, v229\n"
    "v_mfma_f32_16x16x32_bf16 v[32:35], v[152:155], v[208:211], v[32:35]\n"
    "ds_read_b128 v[152:155], v240 offset:16384\n"
    "v_mfma_f32_16x16x32_bf16 v[44:47], v[156:159], v[208:211], v[44:47]\n"
    "ds_read_b128 v[156:159], v241 offset:16384\n"
    "v_mfma_f32_16x16x32_bf16 v[56:59], v[160:163], v[208:211], v[56:59]\n"
    "ds_read_b128 v[160:163], v241 offset:20480\n"
    "v_mfma_f32_16x16x32_bf16 v[68:71], v[164:167], v[208:211], v[68:71]\n"
    "ds_read_b128 v[164:167], v240 offset:20480\n"
    "s_add_u32 s49, s49, 1\n"
    "v_mfma_f32_16x16x32_bf16 v[80:83], v[168:171], v[208:211], v[80:83]\n"
    "ds_read_b128 v[168:171], v239 offset:20480\n"
    "v_mfma_f32_16x16x32_bf16 v[92:95], v[172:175], v[208:211], v[92:95]\n"
    "ds_read_b128 v[172:175], v238 offset:20480\n"
    "s_branch .Lloopm\n"
    ".Lexitm:\n"
    "s_mov_b32 s65, s48\n"
    "s_branch .Llast\n"
    ".Latt_g:\n"
    "s_waitcnt vmcnt(4)\n"
    "s_mov_b64 exec, 1\n"
    "ds_add_u32 v253, v252 offset:0\n"
    "s_mov_b64 exec, -1\n"
    "s_add_u32 s55, s55, 4\n"
    "s_mov_b64 exec, 1\n"
    ".Lsp_67:\n"
    "ds_read_b32 v251, v253 offset:0\n"
    "s_waitcnt lgkmcnt(0)\n"
    "v_readfirstlane_b32 s64, v251\n"
    "s_nop 0\n"
    "s_cmp_ge_u32 s64, s55\n"
    "s_cbranch_scc1 .Lok_68\n"
    "s_sleep 1\n"
    "s_branch .Lsp_67\n"
    ".Lok_68:\n"
    "s_mov_b64 exec, -1\n"
    "v_readlane_b32 s67, v244, 15\n"
    "s_add_u32 m0, s51, 131072\n"
    "s_add_u32 s68, s67, 3072\n"
    "s_add_u32 s69, s67, 2176\n"
    "buffer_load_dwordx4 v245, s[32:35], s68 offen offset:0 nt lds\n"
    "buffer_load_dwordx4 v245, s[32:35], s69 offen offset:1024 nt lds\n"
    "s_add_u32 m0, s51, 139264\n"
    "s_nop 0\n"
    "buffer_load_dwordx4 v254, s[36:39], s68 offen offset:0 nt lds\n"
    "buffer_load_dwordx4 v254, s[36:39], s69 offen offset:1024 nt lds\n"
    "s_memrealtime s[70:71]\n"
    "v_mov_b32 v246, 256\n"
    "s_waitcnt lgkmcnt(0)\n"
    "s_sub_u32 s63, s70, s86\n"
    "s_mov_b64 exec, 1\n"
    "ds_add_u32 v253, v246 offset:8\n"
    "s_mov_b64 exec, -1\n"
    "ds_read_b128 v[144:147], v238 offset:0\n"
    "ds_read_b128 v[148:151], v239 offset:0\n"
    "ds_read_b128 v[152:155], v240 offset:0\n"
    "ds_read_b128 v[156:159], v241 offset:0\n"
    "ds_read_b128 v[160:163], v241 offset:4096\n"
    "ds_read_b128 v[164:167], v240 offset:4096\n"
    "ds_read_b128 v[168:171], v239 offset:4096\n"
    "ds_read_b128 v[172:175], v238 offset:4096\n"
    "s_waitcnt lgkmcnt(6)\n"
    "v_mfma_f32_16x16x32_bf16 v[176:179], v[144:147], v[96:99], 0\n"
    "v_readlane_b32 s67, v244, s49\n"
    "v_mfma_f32_16x16x32_bf16 v[176:179], v[148:151], v[108:111], v[176:179]\n"
    "s_waitcnt lgkmcnt(4)\n"
    "v_mfma_f32_16x16x32_bf16 v[176:179], v[152:155], v[120:123], v[176:179]\n"
    "s_add_u32 s63, s63, s76\n"
    "v_mfma_f32_16x16x32_bf16 v[176:179], v[156:159], v[132:135], v[176:179]\n"
    "s_waitcnt lgkmcnt(2)\n"
    "v_mfma_f32_16x16x32_bf16 v[180:183], v[160:163], v[132:135], 0\n"
    "s_add_u32 s4, s67, 3072\n"
    "v_mfma_f32_16x16x32_bf16 v[180:183], v[164:167], v[120:123], v[180:183]\n"
    "s_waitcnt lgkmcnt(0)\n"
    "v_mfma_f32_16x16x32_bf16 v[180:183], v[168:171], v[108:111], v[180:183]\n"
    "s_add_u32 s5, s67, 2176\n"
    "v_mfma_f32_16x16x32_bf16 v[180:183], v[172:175], v[96:99], v[180:183]\n"
    "v_mfma_f32_16x16x32_bf16 v[188:191], v[172:175], v[100:103], 0\n"
    "v_mfma_f32_16x16x32_bf16 v[188:191], v[168:171], v[112:115], v[188:191]\n"
    "v_mfma_f32_16x16x32_bf16 v[188:191], v[164:167], v[124:127], v[188:191]\n"
    "v_max3_f32 v200, v176, v177, v178\n"
    "s_nop 3\n"
    "v_max3_f32 v201, v180, v181, v182\n"
    "v_max_f32 v200, v200, v179\n"
    "v_max3_f32 v200, v200, v201, v183\n"
    "v_mov_b32 v201, v200\n"
    "s_nop 1\n"
    "v_permlane16_swap_b32 v200, v201\n"
    "v_max_f32 v200, v200, v201\n"
    "v_mov_b32 v201, v200\n"
    "v_mfma_f32_16x16x32_bf16 v[188:191], v[160:163], v[136:139], v[188:191]\n"
    "s_nop 0\n"
    "v_permlane32_swap_b32 v200, v201\n"
    "v_max_f32 v200, v200, v201\n"
    "v_sub_f32 v176, v176, v200\n"
    "v_sub_f32 v177, v177, v200\n"
    "v_sub_f32 v178, v178, v200\n"
    "v_sub_f32 v179, v179, v200\n"
    "v_sub_f32 v180, v180, v200\n"
    "v_sub_f32 v181, v181, v200\n"
    "v_mfma_f32_16x16x32_bf16 v[184:187], v[156:159], v[136:139], 0\n"
    "v_sub_f32 v182, v182, v200\n"
    "v_sub_f32 v183, v183, v200\n"
    "v_sub_f32 v212, 0, v200\n"
    "v_sub_f32 v213, 0, v200\n"
    "v_sub_f32 v214, 0, v200\n"
    "v_sub_f32 v215, 0, v200\n"
    "v_exp_f32 v176, v176\n"
    "v_mfma_f32_16x16x32_bf16 v[184:187], v[152:155], v[124:127], v[184:187]\n"
    "v_exp_f32 v177, v177\n"
    "v_exp_f32 v178, v178\n"
    "v_add_f32 v224, v176, v177\n"
    "v_exp_f32 v179, v179\n"
    "v_cvt_pk_bf16_f32 v200, v176, v177\n"
    "v_add_f32 v224, v224, v178\n"
    "v_mfma_f32_16x16x32_bf16 v[184:187], v[148:151], v[112:115], v[184:187]\n"
    "v_exp_f32 v180, v180\n"
    "v_add_f32 v224, v224, v179\n"
    "v_exp_f32 v181, v181\n"
    "v_cvt_pk_bf16_f32 v201, v178, v179\n"
    "v_add_f32 v224, v224, v180\n"
    "v_mfma_f32_16x16x32_bf16 v[184:187], v[144:147], v[100:103], v[184:187]\n"
    "v_exp_f32 v182, v182\n"
    "v_add_f32 v224, v224, v181\n"
    "v_exp_f32 v183, v183\n"
    "v_cvt_pk_bf16_f32 v202, v180, v181\n"
    "v_add_f32 v224, v224, v182\n"
    "v_add_f32 v224, v224, v183\n"
    "v_cvt_pk_bf16_f32 v203, v182, v183\n"
    "v_mfma_f32_16x16x32_bf16 v[192:195], v[144:147], v[104:107], 0\n"
    "ds_read_b64_tr_b16 v[144:145], v242 offset:8192\n"
    "ds_read_b64_tr_b16 v[146:147], v242 offset:12288\n"
    "v_max3_f32 v204, v184, v185, v186\n"
    "v_max3_f32 v205, v188, v189, v190\n"
    "v_mfma_f32_16x16x32_bf16 v[192:195], v[148:151], v[116:119], v[192:195]\n"
    "ds_read_b64_tr_b16 v[148:149], v243 offset:8192\n"
    "ds_read_b64_tr_b16 v[150:151], v243 offset:12288\n"
    "v_max_f32 v204, v204, v187\n"
    "v_max3_f32 v204, v204, v205, v191\n"
    "v_mov_b32 v205, v204\n"
    "v_mfma_f32_16x16x32_bf16 v[192:195], v[152:155], v[128:131], v[192:195]\n"
    "ds_read_b64_tr_b16 v[152:153], v248 offset:8192\n"
    "ds_read_b64_tr_b16 v[154:155], v248 offset:12288\n"
    "v_permlane16_swap_b32 v204, v205\n"
    "v_max_f32 v204, v204, v205\n"
    "v_mfma_f32_16x16x32_bf16 v[192:195], v[156:159], v[140:143], v[192:195]\n"
    "ds_read_b64_tr_b16 v[156:157], v249 offset:8192\n"
    "ds_read_b64_tr_b16 v[158:159], v249 offset:12288\n"
    "v_mov_b32 v205, v204\n"
    "s_nop 1\n"
    "v_permlane32_swap_b32 v204, v205\n"
    "v_mfma_f32_16x16x32_bf16 v[196:199], v[160:163], v[140:143], 0\n"
    "ds_read_b64_tr_b16 v[160:161], v242 offset:9216\n"
    "ds_read_b64_tr_b16 v[162:163], v242 offset:13312\n"
    "v_max_f32 v204, v204, v205\n"
    "v_sub_f32 v184, v184, v204\n"
    "v_mfma_f32_16x16x32_bf16 v[196:199], v[164:167], v[128:131], v[196:199]\n"
    "ds_read_b64_tr_b16 v[164:165], v243 offset:9216\n"
    "ds_read_b64_tr_b16 v[166:167], v243 offset:13312\n"
    "v_sub_f32 v185, v185, v204\n"
    "v_sub_f32 v186, v186, v204\n"
    "v_sub_f32 v187, v187, v204\n"
    "v_mfma_f32_16x16x32_bf16 v[196:199], v[168:171], v[116:119], v[196:199]\n"
    "ds_read_b64_tr_b16 v[168:169], v248 offset:9216\n"
    "ds_read_b64_tr_b16 v[170:171], v248 offset:13312\n"
    "v_sub_f32 v188, v188, v204\n"
    "v_sub_f32 v189, v189, v204\n"
    "v_mfma_f32_16x16x32_bf16 v[196:199], v[172:175], v[104:107], v[196:199]\n"
    "ds_read_b64_tr_b16 v[172:173], v249 offset:9216\n"
    "ds_read_b64_tr_b16 v[174:175], v249 offset:13312\n"
    "v_sub_f32 v190, v190, v204\n"
    "v_sub_f32 v191, v191, v204\n"
    "s_waitcnt vmcnt(4)\n"
    "s_mov_b64 exec, 1\n"
    "ds_add_u32 v253, v252 offset:0\n"
    "s_mov_b64 exec, -1\n"
    "s_add_u32 s55, s55, 4\n"
    "s_waitcnt lgkmcnt(9)\n"
    "v_mfma_f32_16x16x32_bf16 v[0:3], v[144:147], v[200:203], 0\n"
    "v_sub_f32 v216, 0, v204\n"
    "v_sub_f32 v217, 0, v204\n"
    "v_sub_f32 v218, 0, v204\n"
    "v_sub_f32 v219, 0, v204\n"
    "v_exp_f32 v188, v188\n"
    "v_mfma_f32_16x16x32_bf16 v[12:15], v[148:151], v[200:203], 0\n"
    "v_exp_f32 v189, v189\n"
    "v_exp_f32 v190, v190\n"
    "v_add_f32 v225, v188, v189\n"
    "v_mfma_f32_16x16x32_bf16 v[24:27], v[152:155], v[200:203], 0\n"
    "v_exp_f32 v191, v191\n"
    "v_cvt_pk_bf16_f32 v206, v188, v189\n"
    "v_add_f32 v225, v225, v190\n"
    "v_exp_f32 v184, v184\n"
    "v_mfma_f32_16x16x32_bf16 v[36:39], v[156:159], v[200:203], 0\n"
    "v_add_f32 v225, v225, v191\n"
    "v_exp_f32 v185, v185\n"
    "v_cvt_pk_bf16_f32 v207, v190, v191\n"
    "v_add_f32 v225, v225, v184\n"
    "s_waitcnt lgkmcnt(1)\n"
    "v_mfma_f32_16x16x32_bf16 v[48:51], v[160:163], v[200:203], 0\n"
    "v_exp_f32 v186, v186\n"
    "v_add_f32 v225, v225, v185\n"
    "v_exp_f32 v187, v187\n"
    "v_mfma_f32_16x16x32_bf16 v[60:63], v[164:167], v[200:203], 0\n"
    "v_cvt_pk_bf16_f32 v204, v184, v185\n"
    "v_add_f32 v225, v225, v186\n"
    "v_add_f32 v225, v225, v187\n"
    "v_cvt_pk_bf16_f32 v205, v186, v187\n"
    "v_max3_f32 v208, v192, v193, v194\n"
    "v_mfma_f32_16x16x32_bf16 v[72:75], v[168:171], v[200:203], 0\n"
    "v_max3_f32 v209, v196, v197, v198\n"
    "v_max_f32 v208, v208, v195\n"
    "v_max3_f32 v208, v208, v209, v199\n"
    "v_mov_b32 v209, v208\n"
    "s_nop 1\n"
    "v_permlane16_swap_b32 v208, v209\n"
    "v_max_f32 v208, v208, v209\n"
    "v_mfma_f32_16x16x32_bf16 v[84:87], v[172:175], v[200:203], 0\n"
    "v_mov_b32 v209, v208\n"
    "s_nop 1\n"
    "v_permlane32_swap_b32 v208, v209\n"
    "v_max_f32 v208, v208, v209\n"
    "v_sub_f32 v192, v192, v208\n"
    "v_sub_f32 v193, v193, v208\n"
    "v_mfma_f32_16x16x32_bf16 v[88:91], v[172:175], v[204:207], 0\n"
    "v_sub_f32 v194, v194, v208\n"
    "v_sub_f32 v195, v195, v208\n"
    "v_sub_f32 v196, v196, v208\n"
    "v_sub_f32 v197, v197, v208\n"
    "v_sub_f32 v198, v198, v208\n"
    "v_mfma_f32_16x16x32_bf16 v[76:79], v[168:171], v[204:207], 0\n"
    "v_sub_f32 v199, v199, v208\n"
    "v_sub_f32 v220, 0, v208\n"
    "v_sub_f32 v221, 0, v208\n"
    "v_sub_f32 v222, 0, v208\n"
    "v_sub_f32 v223, 0, v208\n"
    "v_mfma_f32_16x16x32_bf16 v[64:67], v[164:167], v[204:207], 0\n"
    "v_exp_f32 v192, v192\n"
    "v_exp_f32 v193, v193\n"
    "v_mfma_f32_16x16x32_bf16 v[52:55], v[160:163], v[204:207], 0\n"
    "v_exp_f32 v194, v194\n"
    "v_add_f32 v226, v192, v193\n"
    "v_exp_f32 v195, v195\n"
    "v_cvt_pk_bf16_f32 v208, v192, v193\n"
    "v_mfma_f32_16x16x32_bf16 v[40:43], v[156:159], v[204:207], 0\n"
    "v_add_f32 v226, v226, v194\n"
    "v_exp_f32 v196, v196\n"
    "ds_read_b32 v251, v253 offset:0\n"
    "v_mfma_f32_16x16x32_bf16 v[28:31], v[152:155], v[204:207], 0\n"
    "s_memrealtime s[70:71]\n"
    "v_add_f32 v226, v226, v195\n"
    "v_exp_f32 v197, v197\n"
    "v_cvt_pk_bf16_f32 v209, v194, v195\n"
    "v_mfma_f32_16x16x32_bf16 v[16:19], v[148:151], v[204:207], 0\n"
    "v_add_f32 v226, v226, v196\n"
    "v_exp_f32 v198, v198\n"
    "v_add_f32 v226, v226, v197\n"
    "v_exp_f32 v199, v199\n"
    "v_mfma_f32_16x16x32_bf16 v[4:7], v[144:147], v[204:207], 0\n"
    "v_cvt_pk_bf16_f32 v210, v196, v197\n"
    "v_add_f32 v226, v226, v198\n"
    "v_add_f32 v226, v226, v199\n"
    "v_cvt_pk_bf16_f32 v211, v198, v199\n"
    "s_waitcnt lgkmcnt(0)\n"
    "v_readfirstlane_b32 s64, v251\n"
    "s_sub_u32 s69, s70, s86\n"
    "s_cmp_ge_u32 s69, s63\n"
    "s_cselect_b32 s64, s64, 0\n"
    "s_cmp_ge_u32 s64, s55\n"
    "s_cbranch_scc0 .Lgw_69\n"
    ".Lgb_70:\n"
    "v_mfma_f32_16x16x32_bf16 v[8:11], v[144:147], v[208:211], 0\n"
    "ds_read_b128 v[144:147], v238 offset:16384\n"
    "s_add_u32 m0, s51, 98304\n"
    "v_mfma_f32_16x16x32_bf16 v[20:23], v[148:151], v[208:211], 0\n"
    "ds_read_b128 v[148:151], v239 offset:16384\n"
    "buffer_load_dwordx4 v245, s[32:35], s4 offen offset:0 nt lds\n"
    "v_mfma_f32_16x16x32_bf16 v[32:35], v[152:155], v[208:211], 0\n"
    "ds_read_b128 v[152:155], v240 offset:16384\n"
    "buffer_load_dwordx4 v245, s[32:35], s5 offen offset:1024 nt lds\n"
    "v_mfma_f32_16x16x32_bf16 v[44:47], v[156:159], v[208:211], 0\n"
    "ds_read_b128 v[156:159], v241 offset:16384\n"
    "s_add_u32 m0, s51, 106496\n"
    "v_mfma_f32_16x16x32_bf16 v[56:59], v[160:163], v[208:211], 0\n"
    "ds_read_b128 v[160:163], v241 offset:20480\n"
    "v_mfma_f32_16x16x32_bf16 v[68:71], v[164:167], v[208:211], 0\n"
    "ds_read_b128 v[164:167], v240 offset:20480\n"
    "buffer_load_dwordx4 v254, s[36:39], s4 offen offset:0 nt lds\n"
    "v_mfma_f32_16x16x32_bf16 v[80:83], v[168:171], v[208:211], 0\n"
    "ds_read_b128 v[168:171], v239 offset:20480\n"
    "buffer_load_dwordx4 v254, s[36:39], s5 offen offset:1024 nt lds\n"
    "v_mfma_f32_16x16x32_bf16 v[92:95], v[172:175], v[208:211], 0\n"
    "ds_read_b128 v[172:175], v238 offset:20480\n"
    "s_add_u32 s49, s49, 1\n"
    ".Lloopg:\n"
    "s_waitcnt lgkmcnt(6)\n"
    "v_mfma_f32_16x16x32_bf16 v[176:179], v[144:147], v[96:99], v[212:215]\n"
    "v_readlane_b32 s67, v244, s49\n"
    "v_mfma_f32_16x16x32_bf16 v[176:179], v[148:151], v[108:111], v[176:179]\n"
    "s_cmp_lt_u32 s49, 13\n"
    "s_waitcnt lgkmcnt(4)\n"
    "v_mfma_f32_16x16x32_bf16 v[176:179], v[152:155], v[120:123], v[176:179]\n"
    "s_cselect_b32 s34, s34, 0\n"
    "v_mfma_f32_16x16x32_bf16 v[176:179], v[156:159], v[132:135], v[176:179]\n"
    "s_cselect_b32 s38, s38, 0\n"
    "s_waitcnt lgkmcnt(2)\n"
    "v_mfma_f32_16x16x32_bf16 v[180:183], v[160:163], v[132:135], v[212:215]\n"
    "s_cselect_b32 s76, s76, 0\n"
    "v_mfma_f32_16x16x32_bf16 v[180:183], v[164:167], v[120:123], v[180:183]\n"
    "s_add_u32 s63, s63, s76\n"
    "s_waitcnt lgkmcnt(0)\n"
    "v_mfma_f32_16x16x32_bf16 v[180:183], v[168:171], v[108:111], v[180:183]\n"
    "s_add_u32 s4, s67, 3072\n"
    "v_mfma_f32_16x16x32_bf16 v[180:183], v[172:175], v[96:99], v[180:183]\n"
    "s_add_u32 s5, s67, 2176\n"
    "v_mfma_f32_16x16x32_bf16 v[188:191], v[172:175], v[100:103], v[216:219]\n"
    "v_exp_f32 v230, v176\n"
    "v_exp_f32 v231, v177\n"
    "v_mfma_f32_16x16x32_bf16 v[188:191], v[168:171], v[112:115], v[188:191]\n"
    "v_exp_f32 v232, v178\n"
    "v_add_f32 v227, v230, v231\n"
    "v_mfma_f32_16x16x32_bf16 v[188:191], v[164:167], v[124:127], v[188:191]\n"
    "v_exp_f32 v233, v179\n"
    "v_cvt_pk_bf16_f32 v200, v230, v231\n"
    "v_add_f32 v227, v227, v232\n"
    "v_mfma_f32_16x16x32_bf16 v[188:191], v[160:163], v[136:139], v[188:191]\n"
    "v_exp_f32 v234, v180\n"
    "v_add_f32 v227, v227, v233\n"
    "v_mfma_f32_16x16x32_bf16 v[184:187], v[156:159], v[136:139], v[216:219]\n"
    "v_exp_f32 v235, v181\n"
    "v_cvt_pk_bf16_f32 v201, v232, v233\n"
    "v_add_f32 v227, v227, v234\n"
    "v_mfma_f32_16x16x32_bf16 v[184:187], v[152:155], v[124:127], v[184:187]\n"
    "v_exp_f32 v230, v182\n"
    "v_add_f32 v227, v227, v235\n"
    "v_mfma_f32_16x16x32_bf16 v[184:187], v[148:151], v[112:115], v[184:187]\n"
    "v_exp_f32 v231, v183\n"
    "v_cvt_pk_bf16_f32 v202, v234, v235\n"
    "v_add_f32 v227, v227, v230\n"
    "v_mfma_f32_16x16x32_bf16 v[184:187], v[144:147], v[100:103], v[184:187]\n"
    "v_add_f32 v227, v227, v231\n"
    "v_cvt_pk_bf16_f32 v203, v230, v231\n"
    "v_cmp_nge_f32 vcc, s57, v227\n"
    "s_cbranch_vccnz .Lslow0_3\n"
    ".Lback0_3:\n"
    "v_mfma_f32_16x16x32_bf16 v[192:195], v[144:147], v[104:107], v[220:223]\n"
    "ds_read_b64_tr_b16 v[144:145], v242 offset:24576\n"
    "ds_read_b64_tr_b16 v[146:147], v242 offset:28672\n"
    "v_add_f32 v224, v224, v227\n"
    "v_mfma_f32_16x16x32_bf16 v[192:195], v[148:151], v[116:119], v[192:195]\n"
    "ds_read_b64_tr_b16 v[148:149], v243 offset:24576\n"
    "ds_read_b64_tr_b16 v[150:151], v243 offset:28672\n"
    "v_exp_f32 v232, v188\n"
    "v_mfma_f32_16x16x32_bf16 v[192:195], v[152:155], v[128:131], v[192:195]\n"
    "ds_read_b64_tr_b16 v[152:153], v248 offset:24576\n"
    "ds_read_b64_tr_b16 v[154:155], v248 offset:28672\n"
    "v_exp_f32 v233, v189\n"
    "v_mfma_f32_16x16x32_bf16 v[192:195], v[156:159], v[140:143], v[192:195]\n"
    "ds_read_b64_tr_b16 v[156:157], v249 offset:24576\n"
    "ds_read_b64_tr_b16 v[158:159], v249 offset:28672\n"
    "v_exp_f32 v234, v190\n"
    "v_mfma_f32_16x16x32_bf16 v[196:199], v[160:163], v[140:143], v[220:223]\n"
    "ds_read_b64_tr_b16 v[160:161], v242 offset:25600\n"
    "ds_read_b64_tr_b16 v[162:163], v242 offset:29696\n"
    "v_add_f32 v228, v232, v233\n"
    "v_mfma_f32_16x16x32_bf16 v[196:199], v[164:167], v[128:131], v[196:199]\n"
    "ds_read_b64_tr_b16 v[164:165], v243 offset:25600\n"
    "ds_read_b64_tr_b16 v[166:167], v243 offset:29696\n"
    "v_exp_f32 v235, v191\n"
    "v_cvt_pk_bf16_f32 v206, v232, v233\n"
    "v_mfma_f32_16x16x32_bf16 v[196:199], v[168:171], v[116:119], v[196:199]\n"
    "ds_read_b64_tr_b16 v[168:169], v248 offset:25600\n"
    "ds_read_b64_tr_b16 v[170:171], v248 offset:29696\n"
    "v_add_f32 v228, v228, v234\n"
    "v_mfma_f32_16x16x32_bf16 v[196:199], v[172:175], v[104:107], v[196:199]\n"
    "ds_read_b64_tr_b16 v[172:173], v249 offset:25600\n"
    "ds_read_b64_tr_b16 v[174:175], v249 offset:29696\n"
    "v_exp_f32 v230, v184\n"
    "s_waitcnt vmcnt(4)\n"
    "s_mov_b64 exec, 1\n"
    "ds_add_u32 v253, v252 offset:0\n"
    "s_mov_b64 exec, -1\n"
    "s_add_u32 s55, s55, 4\n"
    "s_waitcnt lgkmcnt(9)\n"
    "v_mfma_f32_16x16x32_bf16 v[0:3], v[144:147], v[200:203], v[0:3]\n"
    "v_add_f32 v228, v228, v235\n"
    "v_exp_f32 v231, v185\n"
    "v_mfma_f32_16x16x32_bf16 v[12:15], v[148:151], v[200:203], v[12:15]\n"
    "v_cvt_pk_bf16_f32 v207, v234, v235\n"
    "v_add_f32 v228, v228, v230\n"
    "v_exp_f32 v232, v186\n"
    "v_mfma_f32_16x16x32_bf16 v[24:27], v[152:155], v[200:203], v[24:27]\n"
    "v_add_f32 v228, v228, v231\n"
    "v_exp_f32 v233, v187\n"
    "v_mfma_f32_16x16x32_bf16 v[36:39], v[156:159], v[200:203], v[36:39]\n"
    "v_cvt_pk_bf16_f32 v204, v230, v231\n"
    "v_add_f32 v228, v228, v232\n"
    "s_waitcnt lgkmcnt(1)\n"
    "v_mfma_f32_16x16x32_bf16 v[48:51], v[160:163], v[200:203], v[48:51]\n"
    "v_add_f32 v228, v228, v233\n"
    "v_cvt_pk_bf16_f32 v205, v232, v233\n"
    "v_cmp_nge_f32 vcc, s57, v228\n"
    "v_mfma_f32_16x16x32_bf16 v[60:63], v[164:167], v[200:203], v[60:63]\n"
    "v_exp_f32 v234, v192\n"
    "v_exp_f32 v235, v193\n"
    "v_mfma_f32_16x16x32_bf16 v[72:75], v[168:171], v[200:203], v[72:75]\n"
    "v_exp_f32 v230, v194\n"
    "v_mfma_f32_16x16x32_bf16 v[84:87], v[172:175], v[200:203], v[84:87]\n"
    "v_add_f32 v229, v234, v235\n"
    "v_exp_f32 v231, v195\n"
    "s_cbranch_vccnz .Lslow1_3\n"
    ".Lback1_3:\n"
    "v_mfma_f32_16x16x32_bf16 v[88:91], v[172:175], v[204:207], v[88:91]\n"
    "v_add_f32 v225, v225, v228\n"
    "v_cvt_pk_bf16_f32 v208, v234, v235\n"
    "v_add_f32 v229, v229, v230\n"
    "v_mfma_f32_16x16x32_bf16 v[76:79], v[168:171], v[204:207], v[76:79]\n"
    "v_exp_f32 v232, v196\n"
    "v_add_f32 v229, v229, v231\n"
    "v_mfma_f32_16x16x32_bf16 v[64:67], v[164:167], v[204:207], v[64:67]\n"
    "v_exp_f32 v233, v197\n"
    "v_mfma_f32_16x16x32_bf16 v[52:55], v[160:163], v[204:207], v[52:55]\n"
    "ds_read_b32 v251, v253 offset:0\n"
    "s_memrealtime s[70:71]\n"
    "v_cvt_pk_bf16_f32 v209, v230, v231\n"
    "v_mfma_f32_16x16x32_bf16 v[40:43], v[156:159], v[204:207], v[40:43]\n"
    "v_add_f32 v229, v229, v232\n"
    "v_exp_f32 v234, v198\n"
    "v_mfma_f32_16x16x32_bf16 v[28:31], v[152:155], v[204:207], v[28:31]\n"
    "v_add_f32 v229, v229, v233\n"
    "v_exp_f32 v235, v199\n"
    "v_mfma_f32_16x16x32_bf16 v[16:19], v[148:151], v[204:207], v[16:19]\n"
    "v_cvt_pk_bf16_f32 v210, v232, v233\n"
    "v_add_f32 v229, v229, v234\n"
    "v_mfma_f32_16x16x32_bf16 v[4:7], v[144:147], v[204:207], v[4:7]\n"
    "v_add_f32 v229, v229, v235\n"
    "v_cvt_pk_bf16_f32 v211, v234, v235\n"
    "v_cmp_nge_f32 vcc, s57, v229\n"
    "s_cbranch_vccnz .Lslow2_3\n"
    ".Lback2_3:\n"
    "s_waitcnt lgkmcnt(0)\n"
    "v_readfirstlane_b32 s64, v251\n"
    "s_sub_u32 s69, s70, s86\n"
    "s_cmp_ge_u32 s69, s63\n"
    "s_cselect_b32 s64, s64, 0\n"
    "s_cmp_ge_u32 s64, s55\n"
    "s_cbranch_scc0 .Lgw_71\n"
    ".Lgb_72:\n"
    "v_mfma_f32_16x16x32_bf16 v[8:11], v[144:147], v[208:211], v[8:11]\n"
    "ds_read_b128 v[144:147], v238 offset:32768\n"
    "v_add_f32 v226, v226, v229\n"
    "s_add_u32 m0, s51, 114688\n"
    "v_mfma_f32_16x16x32_bf16 v[20:23], v[148:151], v[208:211], v[20:23]\n"
    "ds_read_b128 v[148:151], v239 offset:32768\n"
    "buffer_load_dwordx4 v245, s[32:35], s4 offen offset:0 nt lds\n"
    "v_mfma_f32_16x16x32_bf16 v[32:35], v[152:155], v[208:211], v[32:35]\n"
    "ds_read_b128 v[152:155], v240 offset:32768\n"
    "v_mfma_f32_16x16x32_bf16 v[44:47], v[156:159], v[208:211], v[44:47]\n"
    "ds_read_b128 v[156:159], v241 offset:32768\n"
    "buffer_load_dwordx4 v245, s[32:35], s5 offen offset:1024 nt lds\n"
    "v_mfma_f32_16x16x32_bf16 v[56:59], v[160:163], v[208:211], v[56:59]\n"
    "ds_read_b128 v[160:163], v241 offset:36864\n"
    "s_add_u32 m0, s51, 122880\n"
    "v_mfma_f32_16x16x32_bf16 v[68:71], v[164:167], v[208:211], v[68:71]\n"
    "ds_read_b128 v[164:167], v240 offset:36864\n"
    "buffer_load_dwordx4 v254, s[36:39], s4 offen offset:0 nt lds\n"
    "v_mfma_f32_16x16x32_bf16 v[80:83], v[168:171], v[208:211], v[80:83]\n"
    "ds_read_b128 v[168:171], v239 offset:36864\n"
    "buffer_load_dwordx4 v254, s[36:39], s5 offen offset:1024 nt lds\n"
    "v_mfma_f32_16x16x32_bf16 v[92:95], v[172:175], v[208:211], v[92:95]\n"
    "ds_read_b128 v[172:175], v238 offset:36864\n"
    "s_add_u32 s49, s49, 1\n"
    "s_waitcnt lgkmcnt(6)\n"
    "v_mfma_f32_16x16x32_bf16 v[176:179], v[144:147], v[96:99], v[212:215]\n"
    "v_readlane_b32 s67, v244, s49\n"
    "v_mfma_f32_16x16x32_bf16 v[176:179], v[148:151], v[108:111], v[176:179]\n"
    "s_waitcnt lgkmcnt(4)\n"
    "v_mfma_f32_16x16x32_bf16 v[176:179], v[152:155], v[120:123], v[176:179]\n"
    "s_add_u32 s63, s63, s76\n"
    "v_mfma_f32_16x16x32_bf16 v[176:179], v[156:159], v[132:135], v[176:179]\n"
    "s_waitcnt lgkmcnt(2)\n"
    "v_mfma_f32_16x16x32_bf16 v[180:183], v[160:163], v[132:135], v[212:215]\n"
    "s_add_u32 s4, s67, 3072\n"
    "v_mfma_f32_16x16x32_bf16 v[180:183], v[164:167], v[120:123], v[180:183]\n"
    "s_waitcnt lgkmcnt(0)\n"
    "v_mfma_f32_16x16x32_bf16 v[180:183], v[168:171], v[108:111], v[180:183]\n"
    "s_add_u32 s5, s67, 2176\n"
    "v_mfma_f32_16x16x32_bf16 v[180:183], v[172:175], v[96:99], v[180:183]\n"
    "v_mfma_f32_16x16x32_bf16 v[188:191], v[172:175], v[100:103], v[216:219]\n"
    "v_exp_f32 v230, v176\n"
    "v_exp_f32 v231, v177\n"
    "v_mfma_f32_16x16x32_bf16 v[188:191], v[168:171], v[112:115], v[188:191]\n"
    "v_exp_f32 v232, v178\n"
    "v_add_f32 v227, v230, v231\n"
    "v_mfma_f32_16x16x32_bf16 v[188:191], v[164:167], v[124:127], v[188:191]\n"
    "v_exp_f32 v233, v179\n"
    "v_cvt_pk_bf16_f32 v200, v230, v231\n"
    "v_add_f32 v227, v227, v232\n"
    "v_mfma_f32_16x16x32_bf16 v[188:191], v[160:163], v[136:139], v[188:191]\n"
    "v_exp_f32 v234, v180\n"
    "v_add_f32 v227, v227, v233\n"
    "v_mfma_f32_16x16x32_bf16 v[184:187], v[156:159], v[136:139], v[216:219]\n"
    "v_exp_f32 v235, v181\n"
    "v_cvt_pk_bf16_f32 v201, v232, v233\n"
    "v_add_f32 v227, v227, v234\n"
    "v_mfma_f32_16x16x32_bf16 v[184:187], v[152:155], v[124:127], v[184:187]\n"
    "v_exp_f32 v230, v182\n"
    "v_add_f32 v227, v227, v235\n"
    "v_mfma_f32_16x16x32_bf16 v[184:187], v[148:151], v[112:115], v[184:187]\n"
    "v_exp_f32 v231, v183\n"
    "v_cvt_pk_bf16_f32 v202, v234, v235\n"
    "v_add_f32 v227, v227, v230\n"
    "v_mfma_f32_16x16x32_bf16 v[184:187], v[144:147], v[100:103], v[184:187]\n"
    "v_add_f32 v227, v227, v231\n"
    "v_cvt_pk_bf16_f32 v203, v230, v231\n"
    "v_cmp_nge_f32 vcc, s57, v227\n"
    "s_cbranch_vccnz .Lslow0_4\n"
    ".Lback0_4:\n"
    "v_mfma_f32_16x16x32_bf16 v[192:195], v[144:147], v[104:107], v[220:223]\n"
    "ds_read_b64_tr_b16 v[144:145], v242 offset:40960\n"
    "ds_read_b64_tr_b16 v[146:147], v242 offset:45056\n"
    "v_add_f32 v224, v224, v227\n"
    "v_mfma_f32_16x16x32_bf16 v[192:195], v[148:151], v[116:119], v[192:195]\n"
    "ds_read_b64_tr_b16 v[148:149], v243 offset:40960\n"
    "ds_read_b64_tr_b16 v[150:151], v243 offset:45056\n"
    "v_exp_f32 v232, v188\n"
    "v_mfma_f32_16x16x32_bf16 v[192:195], v[152:155], v[128:131], v[192:195]\n"
    "ds_read_b64_tr_b16 v[152:153], v248 offset:40960\n"
    "ds_read_b64_tr_b16 v[154:155], v248 offset:45056\n"
    "v_exp_f32 v233, v189\n"
    "v_mfma_f32_16x16x32_bf16 v[192:195], v[156:159], v[140:143], v[192:195]\n"
    "ds_read_b64_tr_b16 v[156:157], v249 offset:40960\n"
    "ds_read_b64_tr_b16 v[158:159], v249 offset:45056\n"
    "v_exp_f32 v234, v190\n"
    "v_mfma_f32_16x16x32_bf16 v[196:199], v[160:163], v[140:143], v[220:223]\n"
    "ds_read_b64_tr_b16 v[160:161], v242 offset:41984\n"
    "ds_read_b64_tr_b16 v[162:163], v242 offset:46080\n"
    "v_add_f32 v228, v232, v233\n"
    "v_mfma_f32_16x16x32_bf16 v[196:199], v[164:167], v[128:131], v[196:199]\n"
    "ds_read_b64_tr_b16 v[164:165], v243 offset:41984\n"
    "ds_read_b64_tr_b16 v[166:167], v243 offset:46080\n"
    "v_exp_f32 v235, v191\n"
    "v_cvt_pk_bf16_f32 v206, v232, v233\n"
    "v_mfma_f32_16x16x32_bf16 v[196:199], v[168:171], v[116:119], v[196:199]\n"
    "ds_read_b64_tr_b16 v[168:169], v248 offset:41984\n"
    "ds_read_b64_tr_b16 v[170:171], v248 offset:46080\n"
    "v_add_f32 v228, v228, v234\n"
    "v_mfma_f32_16x16x32_bf16 v[196:199], v[172:175], v[104:107], v[196:199]\n"
    "ds_read_b64_tr_b16 v[172:173], v249 offset:41984\n"
    "ds_read_b64_tr_b16 v[174:175], v249 offset:46080\n"
    "v_exp_f32 v230, v184\n"
    "s_cmp_lg_u32 s49, 14\n"
    "s_cbranch_scc1 .Lw_73\n"
    "s_waitcnt vmcnt(0)\n"
    ".Lw_73:\n"
    "s_waitcnt vmcnt(4)\n"
    "s_mov_b64 exec, 1\n"
    "ds_add_u32 v253, v252 offset:0\n"
    "s_mov_b64 exec, -1\n"
    "s_add_u32 s55, s55, 4\n"
    "s_waitcnt lgkmcnt(9)\n"
    "v_mfma_f32_16x16x32_bf16 v[0:3], v[144:147], v[200:203], v[0:3]\n"
    "v_add_f32 v228, v228, v235\n"
    "v_exp_f32 v231, v185\n"
    "v_mfma_f32_16x16x32_bf16 v[12:15], v[148:151], v[200:203], v[12:15]\n"
    "v_cvt_pk_bf16_f32 v207, v234, v235\n"
    "v_add_f32 v228, v228, v230\n"
    "v_exp_f32 v232, v186\n"
    "v_mfma_f32_16x16x32_bf16 v[24:27], v[152:155], v[200:203], v[24:27]\n"
    "v_add_f32 v228, v228, v231\n"
    "v_exp_f32 v233, v187\n"
    "v_mfma_f32_16x16x32_bf16 v[36:39], v[156:159], v[200:203], v[36:39]\n"
    "v_cvt_pk_bf16_f32 v204, v230, v231\n"
    "v_add_f32 v228, v228, v232\n"
    "s_waitcnt lgkmcnt(1)\n"
    "v_mfma_f32_16x16x32_bf16 v[48:51], v[160:163], v[200:203], v[48:51]\n"
    "v_add_f32 v228, v228, v233\n"
    "v_cvt_pk_bf16_f32 v205, v232, v233\n"
    "v_cmp_nge_f32 vcc, s57, v228\n"
    "v_mfma_f32_16x16x32_bf16 v[60:63], v[164:167], v[200:203], v[60:63]\n"
    "v_exp_f32 v234, v192\n"
    "v_exp_f32 v235, v193\n"
    "v_mfma_f32_16x16x32_bf16 v[72:75], v[168:171], v[200:203], v[72:75]\n"
    "v_exp_f32 v230, v194\n"
    "v_mfma_f32_16x16x32_bf16 v[84:87], v[172:175], v[200:203], v[84:87]\n"
    "v_add_f32 v229, v234, v235\n"
    "v_exp_f32 v231, v195\n"
    "s_cbranch_vccnz .Lslow1_4\n"
    ".Lback1_4:\n"
    "v_mfma_f32_16x16x32_bf16 v[88:91], v[172:175], v[204:207], v[88:91]\n"
    "v_add_f32 v225, v225, v228\n"
    "v_cvt_pk_bf16_f32 v208, v234, v235\n"
    "v_add_f32 v229, v229, v230\n"
    "v_mfma_f32_16x16x32_bf16 v[76:79], v[168:171], v[204:207], v[76:79]\n"
    "v_exp_f32 v232, v196\n"
    "v_add_f32 v229, v229, v231\n"
    "v_mfma_f32_16x16x32_bf16 v[64:67], v[164:167], v[204:207], v[64:67]\n"
    "v_exp_f32 v233, v197\n"
    "v_mfma_f32_16x16x32_bf16 v[52:55], v[160:163], v[204:207], v[52:55]\n"
    "ds_read_b32 v251, v253 offset:0\n"
    "s_memrealtime s[70:71]\n"
    "v_cvt_pk_bf16_f32 v209, v230, v231\n"
    "v_mfma_f32_16x16x32_bf16 v[40:43], v[156:159], v[204:207], v[40:43]\n"
    "v_add_f32 v229, v229, v232\n"
    "v_exp_f32 v234, v198\n"
    "v_mfma_f32_16x16x32_bf16 v[28:31], v[152:155], v[204:207], v[28:31]\n"
    "v_add_f32 v229, v229, v233\n"
    "v_exp_f32 v235, v199\n"
    "v_mfma_f32_16x16x32_bf16 v[16:19], v[148:151], v[204:207], v[16:19]\n"
    "v_cvt_pk_bf16_f32 v210, v232, v233\n"
    "v_add_f32 v229, v229, v234\n"
    "v_mfma_f32_16x16x32_bf16 v[4:7], v[144:147], v[204:207], v[4:7]\n"
    "v_add_f32 v229, v229, v235\n"
    "v_cvt_pk_bf16_f32 v211, v234, v235\n"
    "v_cmp_nge_f32 vcc, s57, v229\n"
    "s_cbranch_vccnz .Lslow2_4\n"
    ".Lback2_4:\n"
    "s_waitcnt lgkmcnt(0)\n"
    "v_readfirstlane_b32 s64, v251\n"
    "s_sub_u32 s69, s70, s86\n"
    "s_cmp_ge_u32 s69, s63\n"
    "s_cselect_b32 s64, s64, 0\n"
    "s_cmp_ge_u32 s64, s55\n"
    "s_cbranch_scc0 .Lgw_74\n"
    ".Lgb_75:\n"
    "v_mfma_f32_16x16x32_bf16 v[8:11], v[144:147], v[208:211], v[8:11]\n"
    "ds_read_b128 v[144:147], v238 offset:0\n"
    "v_add_f32 v226, v226, v229\n"
    "s_add_u32 m0, s51, 131072\n"
    "v_mfma_f32_16x16x32_bf16 v[20:23], v[148:151], v[208:211], v[20:23]\n"
    "ds_read_b128 v[148:151], v239 offset:0\n"
    "buffer_load_dwordx4 v245, s[32:35], s4 offen offset:0 nt lds\n"
    "v_mfma_f32_16x16x32_bf16 v[32:35], v[152:155], v[208:211], v[32:35]\n"
    "ds_read_b128 v[152:155], v240 offset:0\n"
    "v_mfma_f32_16x16x32_bf16 v[44:47], v[156:159], v[208:211], v[44:47]\n"
    "ds_read_b128 v[156:159], v241 offset:0\n"
    "buffer_load_dwordx4 v245, s[32:35], s5 offen offset:1024 nt lds\n"
    "s_add_u32 m0, s51, 139264\n"
    "v_mfma_f32_16x16x32_bf16 v[56:59], v[160:163], v[208:211], v[56:59]\n"
    "ds_read_b128 v[160:163], v241 offset:4096\n"
    "buffer_load_dwordx4 v254, s[36:39], s4 offen offset:0 nt lds\n"
    "v_mfma_f32_16x16x32_bf16 v[68:71], v[164:167], v[208:211], v[68:71]\n"
    "ds_read_b128 v[164:167], v240 offset:4096\n"
    "v_mfma_f32_16x16x32_bf16 v[80:83], v[168:171], v[208:211], v[80:83]\n"
    "ds_read_b128 v[168:171], v239 offset:4096\n"
    "buffer_load_dwordx4 v254, s[36:39], s5 offen offset:1024 nt lds\n"
    "v_mfma_f32_16x16x32_bf16 v[92:95], v[172:175], v[208:211], v[92:95]\n"
    "ds_read_b128 v[172:175], v238 offset:4096\n"
    "s_add_u32 s49, s49, 1\n"
    "s_cmp_lt_u32 s49, 15\n"
    "s_cbranch_scc0 .Lexitg\n"
    "s_waitcnt lgkmcnt(6)\n"
    "v_mfma_f32_16x16x32_bf16 v[176:179], v[144:147], v[96:99], v[212:215]\n"
    "v_readlane_b32 s67, v244, s49\n"
    "v_mfma_f32_16x16x32_bf16 v[176:179], v[148:151], v[108:111], v[176:179]\n"
    "s_waitcnt lgkmcnt(4)\n"
    "v_mfma_f32_16x16x32_bf16 v[176:179], v[152:155], v[120:123], v[176:179]\n"
    "s_add_u32 s63, s63, s76\n"
    "v_mfma_f32_16x16x32_bf16 v[176:179], v[156:159], v[132:135], v[176:179]\n"
    "s_waitcnt lgkmcnt(2)\n"
    "v_mfma_f32_16x16x32_bf16 v[180:183], v[160:163], v[132:135], v[212:215]\n"
    "s_add_u32 s4, s67, 3072\n"
    "v_mfma_f32_16x16x32_bf16 v[180:183], v[164:167], v[120:123], v[180:183]\n"
    "s_waitcnt lgkmcnt(0)\n"
    "v_mfma_f32_16x16x32_bf16 v[180:183], v[168:171], v[108:111], v[180:183]\n"
    "s_add_u32 s5, s67, 2176\n"
    "v_mfma_f32_16x16x32_bf16 v[180:183], v[172:175], v[96:99], v[180:183]\n"
    "v_mfma_f32_16x16x32_bf16 v[188:191], v[172:175], v[100:103], v[216:219]\n"
    "v_exp_f32 v230, v176\n"
    "v_exp_f32 v231, v177\n"
    "v_mfma_f32_16x16x32_bf16 v[188:191], v[168:171], v[112:115], v[188:191]\n"
    "v_exp_f32 v232, v178\n"
    "v_add_f32 v227, v230, v231\n"
    "v_mfma_f32_16x16x32_bf16 v[188:191], v[164:167], v[124:127], v[188:191]\n"
    "v_exp_f32 v233, v179\n"
    "v_cvt_pk_bf16_f32 v200, v230, v231\n"
    "v_add_f32 v227, v227, v232\n"
    "v_mfma_f32_16x16x32_bf16 v[188:191], v[160:163], v[136:139], v[188:191]\n"
    "v_exp_f32 v234, v180\n"
    "v_add_f32 v227, v227, v233\n"
    "v_mfma_f32_16x16x32_bf16 v[184:187], v[156:159], v[136:139], v[216:219]\n"
    "v_exp_f32 v235, v181\n"
    "v_cvt_pk_bf16_f32 v201, v232, v233\n"
    "v_add_f32 v227, v227, v234\n"
    "v_mfma_f32_16x16x32_bf16 v[184:187], v[152:155], v[124:127], v[184:187]\n"
    "v_exp_f32 v230, v182\n"
    "v_add_f32 v227, v227, v235\n"
    "v_mfma_f32_16x16x32_bf16 v[184:187], v[148:151], v[112:115], v[184:187]\n"
    "v_exp_f32 v231, v183\n"
    "v_cvt_pk_bf16_f32 v202, v234, v235\n"
    "v_add_f32 v227, v227, v230\n"
    "v_mfma_f32_16x16x32_bf16 v[184:187], v[144:147], v[100:103], v[184:187]\n"
    "v_add_f32 v227, v227, v231\n"
    "v_cvt_pk_bf16_f32 v203, v230, v231\n"
    "v_cmp_nge_f32 vcc, s57, v227\n"
    "s_cbranch_vccnz .Lslow0_5\n"
    ".Lback0_5:\n"
    "v_mfma_f32_16x16x32_bf16 v[192:195], v[144:147], v[104:107], v[220:223]\n"
    "ds_read_b64_tr_b16 v[144:145], v242 offset:8192\n"
    "ds_read_b64_tr_b16 v[146:147], v242 offset:12288\n"
    "v_add_f32 v224, v224, v227\n"
    "v_mfma_f32_16x16x32_bf16 v[192:195], v[148:151], v[116:119], v[192:195]\n"
    "ds_read_b64_tr_b16 v[148:149], v243 offset:8192\n"
    "ds_read_b64_tr_b16 v[150:151], v243 offset:12288\n"
    "v_exp_f32 v232, v188\n"
    "v_mfma_f32_16x16x32_bf16 v[192:195], v[152:155], v[128:131], v[192:195]\n"
    "ds_read_b64_tr_b16 v[152:153], v248 offset:8192\n"
    "ds_read_b64_tr_b16 v[154:155], v248 offset:12288\n"
    "v_exp_f32 v233, v189\n"
    "v_mfma_f32_16x16x32_bf16 v[192:195], v[156:159], v[140:143], v[192:195]\n"
    "ds_read_b64_tr_b16 v[156:157], v249 offset:8192\n"
    "ds_read_b64_tr_b16 v[158:159], v249 offset:12288\n"
    "v_exp_f32 v234, v190\n"
    "v_mfma_f32_16x16x32_bf16 v[196:199], v[160:163], v[140:143], v[220:223]\n"
    "ds_read_b64_tr_b16 v[160:161], v242 offset:9216\n"
    "ds_read_b64_tr_b16 v[162:163], v242 offset:13312\n"
    "v_add_f32 v228, v232, v233\n"
    "v_mfma_f32_16x16x32_bf16 v[196:199], v[164:167], v[128:131], v[196:199]\n"
    "ds_read_b64_tr_b16 v[164:165], v243 offset:9216\n"
    "ds_read_b64_tr_b16 v[166:167], v243 offset:13312\n"
    "v_exp_f32 v235, v191\n"
    "v_cvt_pk_bf16_f32 v206, v232, v233\n"
    "v_mfma_f32_16x16x32_bf16 v[196:199], v[168:171], v[116:119], v[196:199]\n"
    "ds_read_b64_tr_b16 v[168:169], v248 offset:9216\n"
    "ds_read_b64_tr_b16 v[170:171], v248 offset:13312\n"
    "v_add_f32 v228, v228, v234\n"
    "v_mfma_f32_16x16x32_bf16 v[196:199], v[172:175], v[104:107], v[196:199]\n"
    "ds_read_b64_tr_b16 v[172:173], v249 offset:9216\n"
    "ds_read_b64_tr_b16 v[174:175], v249 offset:13312\n"
    "v_exp_f32 v230, v184\n"
    "s_waitcnt vmcnt(4)\n"
    "s_mov_b64 exec, 1\n"
    "ds_add_u32 v253, v252 offset:0\n"
    "s_mov_b64 exec, -1\n"
    "s_add_u32 s55, s55, 4\n"
    "s_waitcnt lgkmcnt(9)\n"
    "v_mfma_f32_16x16x32_bf16 v[0:3], v[144:147], v[200:203], v[0:3]\n"
    "v_add_f32 v228, v228, v235\n"
    "v_exp_f32 v231, v185\n"
    "v_mfma_f32_16x16x32_bf16 v[12:15], v[148:151], v[200:203], v[12:15]\n"
    "v_cvt_pk_bf16_f32 v207, v234, v235\n"
    "v_add_f32 v228, v228, v230\n"
    "v_exp_f32 v232, v186\n"
    "v_mfma_f32_16x16x32_bf16 v[24:27], v[152:155], v[200:203], v[24:27]\n"
    "v_add_f32 v228, v228, v231\n"
    "v_exp_f32 v233, v187\n"
    "v_mfma_f32_16x16x32_bf16 v[36:39], v[156:159], v[200:203], v[36:39]\n"
    "v_cvt_pk_bf16_f32 v204, v230, v231\n"
    "v_add_f32 v228, v228, v232\n"
    "s_waitcnt lgkmcnt(1)\n"
    "v_mfma_f32_16x16x32_bf16 v[48:51], v[160:163], v[200:203], v[48:51]\n"
    "v_add_f32 v228, v228, v233\n"
    "v_cvt_pk_bf16_f32 v205, v232, v233\n"
    "v_cmp_nge_f32 vcc, s57, v228\n"
    "v_mfma_f32_16x16x32_bf16 v[60:63], v[164:167], v[200:203], v[60:63]\n"
    "v_exp_f32 v234, v192\n"
    "v_exp_f32 v235, v193\n"
    "v_mfma_f32_16x16x32_bf16 v[72:75], v[168:171], v[200:203], v[72:75]\n"
    "v_exp_f32 v230, v194\n"
    "v_mfma_f32_16x16x32_bf16 v[84:87], v[172:175], v[200:203], v[84:87]\n"
    "v_add_f32 v229, v234, v235\n"
    "v_exp_f32 v231, v195\n"
    "s_cbranch_vccnz .Lslow1_5\n"
    ".Lback1_5:\n"
    "v_mfma_f32_16x16x32_bf16 v[88:91], v[172:175], v[204:207], v[88:91]\n"
    "v_add_f32 v225, v225, v228\n"
    "v_cvt_pk_bf16_f32 v208, v234, v235\n"
    "v_add_f32 v229, v229, v230\n"
    "v_mfma_f32_16x16x32_bf16 v[76:79], v[168:171], v[204:207], v[76:79]\n"
    "v_exp_f32 v232, v196\n"
    "v_add_f32 v229, v229, v231\n"
    "v_mfma_f32_16x16x32_bf16 v[64:67], v[164:167], v[204:207], v[64:67]\n"
    "v_exp_f32 v233, v197\n"
    "v_mfma_f32_16x16x32_bf16 v[52:55], v[160:163], v[204:207], v[52:55]\n"
    "ds_read_b32 v251, v253 offset:0\n"
    "s_memrealtime s[70:71]\n"
    "v_cvt_pk_bf16_f32 v209, v230, v231\n"
    "v_mfma_f32_16x16x32_bf16 v[40:43], v[156:159], v[204:207], v[40:43]\n"
    "v_add_f32 v229, v229, v232\n"
    "v_exp_f32 v234, v198\n"
    "v_mfma_f32_16x16x32_bf16 v[28:31], v[152:155], v[204:207], v[28:31]\n"
    "v_add_f32 v229, v229, v233\n"
    "v_exp_f32 v235, v199\n"
    "v_mfma_f32_16x16x32_bf16 v[16:19], v[148:151], v[204:207], v[16:19]\n"
    "v_cvt_pk_bf16_f32 v210, v232, v233\n"
    "v_add_f32 v229, v229, v234\n"
    "v_mfma_f32_16x16x32_bf16 v[4:7], v[144:147], v[204:207], v[4:7]\n"
    "v_add_f32 v229, v229, v235\n"
    "v_cvt_pk_bf16_f32 v211, v234, v235\n"
    "v_cmp_nge_f32 vcc, s57, v229\n"
    "s_cbranch_vccnz .Lslow2_5\n"
    ".Lback2_5:\n"
    "s_waitcnt lgkmcnt(0)\n"
    "v_readfirstlane_b32 s64, v251\n"
    "s_sub_u32 s69, s70, s86\n"
    "s_cmp_ge_u32 s69, s63\n"
    "s_cselect_b32 s64, s64, 0\n"
    "s_cmp_ge_u32 s64, s55\n"
    "s_cbranch_scc0 .Lgw_76\n"
    ".Lgb_77:\n"
    "v_mfma_f32_16x16x32_bf16 v[8:11], v[144:147], v[208:211], v[8:11]\n"
    "ds_read_b128 v[144:147], v238 offset:16384\n"
    "v_add_f32 v226, v226, v229\n"
    "s_add_u32 m0, s51, 98304\n"
    "v_mfma_f32_16x16x32_bf16 v[20:23], v[148:151], v[208:211], v[20:23]\n"
    "ds_read_b128 v[148:151], v239 offset:16384\n"
    "buffer_load_dwordx4 v245, s[32:35], s4 offen offset:0 nt lds\n"
    "v_mfma_f32_16x16x32_bf16 v[32:35], v[152:155], v[208:211], v[32:35]\n"
    "ds_read_b128 v[152:155], v240 offset:16384\n"
    "v_mfma_f32_16x16x32_bf16 v[44:47], v[156:159], v[208:211], v[44:47]\n"
    "ds_read_b128 v[156:159], v241 offset:16384\n"
    "buffer_load_dwordx4 v245, s[32:35], s5 offen offset:1024 nt lds\n"
    "v_mfma_f32_16x16x32_bf16 v[56:59], v[160:163], v[208:211], v[56:59]\n"
    "ds_read_b128 v[160:163], v241 offset:20480\n"
    "s_add_u32 m0, s51, 106496\n"
    "v_mfma_f32_16x16x32_bf16 v[68:71], v[164:167], v[208:211], v[68:71]\n"
    "ds_read_b128 v[164:167], v240 offset:20480\n"
    "buffer_load_dwordx4 v254, s[36:39], s4 offen offset:0 nt lds\n"
    "v_mfma_f32_16x16x32_bf16 v[80:83], v[168:171], v[208:211], v[80:83]\n"
    "ds_read_b128 v[168:171], v239 offset:20480\n"
    "buffer_load_dwordx4 v254, s[36:39], s5 offen offset:1024 nt lds\n"
    "v_mfma_f32_16x16x32_bf16 v[92:95], v[172:175], v[208:211], v[92:95]\n"
    "ds_read_b128 v[172:175], v238 offset:20480\n"
    "s_add_u32 s49, s49, 1\n"
    "s_branch .Lloopg\n"
    ".Lexitg:\n"
    "v_mov_b32 v246, 256\n"
    "s_mov_b64 exec, 1\n"
    "ds_sub_u32 v253, v246 offset:8\n"
    "s_mov_b64 exec, -1\n"
    "s_sub_u32 s64, s70, s86\n"
    "s_add_u32 s64, s64, 90\n"
    "s_cmp_ge_u32 s64, s78\n"
    "s_cselect_b32 s78, 0, s78\n"
    "s_mul_i32 s65, s45, 12288\n"
    "s_add_u32 s65, s65, 98304\n"
    ".Llast:\n"
    "s_waitcnt lgkmcnt(7)\n"
    "v_mfma_f32_16x16x32_bf16 v[176:179], v[144:147], v[96:99], v[212:215]\n"
    "s_waitcnt lgkmcnt(6)\n"
    "v_mfma_f32_16x16x32_bf16 v[176:179], v[148:151], v[108:111], v[176:179]\n"
    "v_lshlrev_b32 v240, 4, v255\n"
    "s_waitcnt lgkmcnt(5)\n"
    "v_mfma_f32_16x16x32_bf16 v[176:179], v[152:155], v[120:123], v[176:179]\n"
    "s_waitcnt lgkmcnt(4)\n"
    "v_mfma_f32_16x16x32_bf16 v[176:179], v[156:159], v[132:135], v[176:179]\n"
    "s_waitcnt lgkmcnt(3)\n"
    "v_mfma_f32_16x16x32_bf16 v[180:183], v[160:163], v[132:135], v[212:215]\n"
    "s_waitcnt lgkmcnt(2)\n"
    "v_mfma_f32_16x16x32_bf16 v[180:183], v[164:167], v[120:123], v[180:183]\n"
    "v_add_u32 v237, s65, v240\n"
    "s_waitcnt lgkmcnt(1)\n"
    "v_mfma_f32_16x16x32_bf16 v[180:183], v[168:171], v[108:111], v[180:183]\n"
    "s_waitcnt lgkmcnt(0)\n"
    "v_mfma_f32_16x16x32_bf16 v[180:183], v[172:175], v[96:99], v[180:183]\n"
    "v_mfma_f32_16x16x32_bf16 v[188:191], v[172:175], v[100:103], v[216:219]\n"
    "v_mfma_f32_16x16x32_bf16 v[188:191], v[168:171], v[112:115], v[188:191]\n"
    "v_mfma_f32_16x16x32_bf16 v[188:191], v[164:167], v[124:127], v[188:191]\n"
    "v_max3_f32 v227, v176, v177, v178\n"
    "s_nop 3\n"
    "v_max3_f32 v227, v227, v179, v180\n"
    "v_mfma_f32_16x16x32_bf16 v[188:191], v[160:163], v[136:139], v[188:191]\n"
    "v_max3_f32 v227, v227, v181, v182\n"
    "v_mfma_f32_16x16x32_bf16 v[184:187], v[156:159], v[136:139], v[216:219]\n"
    "v_max_f32 v227, v227, v183\n"
    "v_cmp_nge_f32 vcc, 0x41000000, v227\n"
    "s_cbranch_vccnz .Lslow0_6\n"
    ".Lback0_6:\n"
    "v_mfma_f32_16x16x32_bf16 v[184:187], v[152:155], v[124:127], v[184:187]\n"
    "v_exp_f32 v176, v176\n"
    "v_mfma_f32_16x16x32_bf16 v[184:187], v[148:151], v[112:115], v[184:187]\n"
    "v_exp_f32 v177, v177\n"
    "v_exp_f32 v178, v178\n"
    "v_mfma_f32_16x16x32_bf16 v[184:187], v[144:147], v[100:103], v[184:187]\n"
    "v_exp_f32 v179, v179\n"
    "v_mfma_f32_16x16x32_bf16 v[192:195], v[144:147], v[104:107], v[220:223]\n"
    "v_exp_f32 v180, v180\n"
    "v_exp_f32 v181, v181\n"
    "v_mfma_f32_16x16x32_bf16 v[192:195], v[148:151], v[116:119], v[192:195]\n"
    "v_exp_f32 v182, v182\n"
    "v_mfma_f32_16x16x32_bf16 v[192:195], v[152:155], v[128:131], v[192:195]\n"
    "v_exp_f32 v183, v183\n"
    "v_max3_f32 v228, v188, v189, v190\n"
    "v_max3_f32 v228, v228, v191, v184\n"
    "v_mfma_f32_16x16x32_bf16 v[192:195], v[156:159], v[140:143], v[192:195]\n"
    "v_max3_f32 v228, v228, v185, v186\n"
    "v_max_f32 v228, v228, v187\n"
    "v_cmp_nge_f32 vcc, 0x41000000, v228\n"
    "s_cbranch_vccnz .Lslow1_6\n"
    ".Lback1_6:\n"
    "v_mfma_f32_16x16x32_bf16 v[196:199], v[160:163], v[140:143], v[220:223]\n"
    "v_add_f32 v224, v224, v176\n"
    "v_add_f32 v200, v177, v178\n"
    "v_add_f32 v224, v224, v179\n"
    "v_mfma_f32_16x16x32_bf16 v[196:199], v[164:167], v[128:131], v[196:199]\n"
    "v_add_f32 v200, v200, v180\n"
    "v_exp_f32 v184, v184\n"
    "v_mfma_f32_16x16x32_bf16 v[196:199], v[168:171], v[116:119], v[196:199]\n"
    "v_exp_f32 v185, v185\n"
    "v_exp_f32 v186, v186\n"
    "v_mfma_f32_16x16x32_bf16 v[196:199], v[172:175], v[104:107], v[196:199]\n"
    "v_exp_f32 v187, v187\n"
    "v_add_f32 v224, v224, v181\n"
    "v_add_f32 v200, v200, v182\n"
    "v_add_f32 v224, v224, v183\n"
    "v_add_f32 v200, v200, v224\n"
    "v_exp_f32 v188, v188\n"
    "v_exp_f32 v189, v189\n"
    "v_exp_f32 v190, v190\n"
    "v_exp_f32 v191, v191\n"
    "v_max3_f32 v229, v192, v193, v194\n"
    "v_max3_f32 v229, v229, v195, v196\n"
    "v_max3_f32 v229, v229, v197, v198\n"
    "v_max_f32 v229, v229, v199\n"
    "v_cmp_nge_f32 vcc, 0x41000000, v229\n"
    "s_cbranch_vccnz .Lslow2_6\n"
    ".Lback2_6:\n"
    "v_exp_f32 v192, v192\n"
    "v_mov_b32 v201, v200\n"
    "v_add_f32 v225, v225, v184\n"
    "v_exp_f32 v193, v193\n"
    "v_permlane16_swap_b32 v200, v201\n"
    "v_add_f32 v204, v185, v186\n"
    "v_exp_f32 v194, v194\n"
    "v_add_f32 v200, v200, v201\n"
    "v_add_f32 v225, v225, v187\n"
    "v_exp_f32 v195, v195\n"
    "v_mov_b32 v201, v200\n"
    "v_add_f32 v204, v204, v188\n"
    "v_exp_f32 v196, v196\n"
    "v_permlane32_swap_b32 v200, v201\n"
    "v_add_f32 v225, v225, v189\n"
    "v_exp_f32 v197, v197\n"
    "v_add_f32 v200, v200, v201\n"
    "v_add_f32 v204, v204, v190\n"
    "v_exp_f32 v198, v198\n"
    "v_rcp_f32 v212, v200\n"
    "v_add_f32 v225, v225, v191\n"
    "v_exp_f32 v199, v199\n"
    "v_mov_b32 v213, v212\n"
    "v_add_f32 v204, v204, v225\n"
    "v_add_f32 v226, v226, v192\n"
    "v_pk_mul_f32 v[176:177], v[176:177], v[212:213]\n"
    "v_mov_b32 v205, v204\n"
    "v_add_f32 v208, v193, v194\n"
    "v_pk_mul_f32 v[178:179], v[178:179], v[212:213]\n"
    "v_permlane16_swap_b32 v204, v205\n"
    "v_add_f32 v226, v226, v195\n"
    "v_pk_mul_f32 v[180:181], v[180:181], v[212:213]\n"
    "v_add_f32 v204, v204, v205\n"
    "v_add_f32 v208, v208, v196\n"
    "v_pk_mul_f32 v[182:183], v[182:183], v[212:213]\n"
    "v_mov_b32 v205, v204\n"
    "v_add_f32 v226, v226, v197\n"
    "v_cvt_pk_bf16_f32 v200, v176, v177\n"
    "v_permlane32_swap_b32 v204, v205\n"
    "v_add_f32 v208, v208, v198\n"
    "v_cvt_pk_bf16_f32 v201, v178, v179\n"
    "v_add_f32 v204, v204, v205\n"
    "v_add_f32 v226, v226, v199\n"
    "v_cvt_pk_bf16_f32 v202, v180, v181\n"
    "v_rcp_f32 v216, v204\n"
    "v_add_f32 v208, v208, v226\n"
    "v_cvt_pk_bf16_f32 v203, v182, v183\n"
    "v_mov_b32 v217, v216\n"
    "v_mov_b32 v209, v208\n"
    "v_pk_mul_f32 v[0:1], v[0:1], v[212:213]\n"
    "v_pk_mul_f32 v[184:185], v[184:185], v[216:217]\n"
    "v_permlane16_swap_b32 v208, v209\n"
    "v_pk_mul_f32 v[2:3], v[2:3], v[212:213]\n"
    "v_pk_mul_f32 v[186:187], v[186:187], v[216:217]\n"
    "v_add_f32 v208, v208, v209\n"
    "v_pk_mul_f32 v[12:13], v[12:13], v[212:213]\n"
    "v_pk_mul_f32 v[188:189], v[188:189], v[216:217]\n"
    "v_mov_b32 v209, v208\n"
    "v_pk_mul_f32 v[14:15], v[14:15], v[212:213]\n"
    "v_pk_mul_f32 v[190:191], v[190:191], v[216:217]\n"
    "v_permlane32_swap_b32 v208, v209\n"
    "v_pk_mul_f32 v[24:25], v[24:25], v[212:213]\n"
    "v_cvt_pk_bf16_f32 v204, v184, v185\n"
    "v_add_f32 v208, v208, v209\n"
    "v_pk_mul_f32 v[26:27], v[26:27], v[212:213]\n"
    "v_cvt_pk_bf16_f32 v205, v186, v187\n"
    "v_rcp_f32 v220, v208\n"
    "v_pk_mul_f32 v[36:37], v[36:37], v[212:213]\n"
    "v_cvt_pk_bf16_f32 v206, v188, v189\n"
    "v_mov_b32 v221, v220\n"
    "v_pk_mul_f32 v[38:39], v[38:39], v[212:213]\n"
    "v_cvt_pk_bf16_f32 v207, v190, v191\n"
    "v_pk_mul_f32 v[192:193], v[192:193], v[220:221]\n"
    "v_pk_mul_f32 v[48:49], v[48:49], v[212:213]\n"
    "v_pk_mul_f32 v[4:5], v[4:5], v[216:217]\n"
    "v_pk_mul_f32 v[194:195], v[194:195], v[220:221]\n"
    "v_pk_mul_f32 v[50:51], v[50:51], v[212:213]\n"
    "v_pk_mul_f32 v[6:7], v[6:7], v[216:217]\n"
    "v_pk_mul_f32 v[196:197], v[196:197], v[220:221]\n"
    "v_pk_mul_f32 v[60:61], v[60:61], v[212:213]\n"
    "v_pk_mul_f32 v[16:17], v[16:17], v[216:217]\n"
    "v_pk_mul_f32 v[198:199], v[198:199], v[220:221]\n"
    "v_pk_mul_f32 v[62:63], v[62:63], v[212:213]\n"
    "v_pk_mul_f32 v[18:19], v[18:19], v[216:217]\n"
    "v_cvt_pk_bf16_f32 v208, v192, v193\n"
    "v_pk_mul_f32 v[72:73], v[72:73], v[212:213]\n"
    "v_pk_mul_f32 v[28:29], v[28:29], v[216:217]\n"
    "v_cvt_pk_bf16_f32 v209, v194, v195\n"
    "v_pk_mul_f32 v[74:75], v[74:75], v[212:213]\n"
    "v_pk_mul_f32 v[30:31], v[30:31], v[216:217]\n"
    "v_cvt_pk_bf16_f32 v210, v196, v197\n"
    "v_pk_mul_f32 v[84:85], v[84:85], v[212:213]\n"
    "v_pk_mul_f32 v[40:41], v[40:41], v[216:217]\n"
    "v_cvt_pk_bf16_f32 v211, v198, v199\n"
    "v_pk_mul_f32 v[86:87], v[86:87], v[212:213]\n"
    "v_pk_mul_f32 v[42:43], v[42:43], v[216:217]\n"
    "v_pk_mul_f32 v[8:9], v[8:9], v[220:221]\n"
    "v_pk_mul_f32 v[52:53], v[52:53], v[216:217]\n"
    "v_pk_mul_f32 v[10:11], v[10:11], v[220:221]\n"
    "v_pk_mul_f32 v[54:55], v[54:55], v[216:217]\n"
    "v_pk_mul_f32 v[20:21], v[20:21], v[220:221]\n"
    "v_pk_mul_f32 v[64:65], v[64:65], v[216:217]\n"
    "v_pk_mul_f32 v[22:23], v[22:23], v[220:221]\n"
    "v_pk_mul_f32 v[66:67], v[66:67], v[216:217]\n"
    "v_pk_mul_f32 v[32:33], v[32:33], v[220:221]\n"
    "v_pk_mul_f32 v[76:77], v[76:77], v[216:217]\n"
    "v_pk_mul_f32 v[34:35], v[34:35], v[220:221]\n"
    "v_pk_mul_f32 v[78:79], v[78:79], v[216:217]\n"
    "v_pk_mul_f32 v[44:45], v[44:45], v[220:221]\n"
    "v_pk_mul_f32 v[88:89], v[88:89], v[216:217]\n"
    "v_pk_mul_f32 v[46:47], v[46:47], v[220:221]\n"
    "v_pk_mul_f32 v[90:91], v[90:91], v[216:217]\n"
    "v_pk_mul_f32 v[56:57], v[56:57], v[220:221]\n"
    "v_pk_mul_f32 v[58:59], v[58:59], v[220:221]\n"
    "v_pk_mul_f32 v[68:69], v[68:69], v[220:221]\n"
    "v_pk_mul_f32 v[70:71], v[70:71], v[220:221]\n"
    "v_pk_mul_f32 v[80:81], v[80:81], v[220:221]\n"
    "v_pk_mul_f32 v[82:83], v[82:83], v[220:221]\n"
    "v_pk_mul_f32 v[92:93], v[92:93], v[220:221]\n"
    "v_pk_mul_f32 v[94:95], v[94:95], v[220:221]\n"
    "s_waitcnt vmcnt(0)\n"
    "ds_read_b64_tr_b16 v[144:145], v242 offset:8192\n"
    "ds_read_b64_tr_b16 v[146:147], v242 offset:12288\n"
    "ds_read_b64_tr_b16 v[148:149], v243 offset:8192\n"
    "ds_read_b64_tr_b16 v[150:151], v243 offset:12288\n"
    "ds_read_b64_tr_b16 v[152:153], v248 offset:8192\n"
    "ds_read_b64_tr_b16 v[154:155], v248 offset:12288\n"
    "ds_read_b64_tr_b16 v[156:157], v249 offset:8192\n"
    "ds_read_b64_tr_b16 v[158:159], v249 offset:12288\n"
    "ds_read_b64_tr_b16 v[160:161], v242 offset:9216\n"
    "ds_read_b64_tr_b16 v[162:163], v242 offset:13312\n"
    "ds_read_b64_tr_b16 v[164:165], v243 offset:9216\n"
    "ds_read_b64_tr_b16 v[166:167], v243 offset:13312\n"
    "ds_read_b64_tr_b16 v[168:169], v248 offset:9216\n"
    "ds_read_b64_tr_b16 v[170:171], v248 offset:13312\n"
    "ds_read_b64_tr_b16 v[172:173], v249 offset:9216\n"
    "ds_read_b64_tr_b16 v[174:175], v249 offset:13312\n"
    "s_cmp_eq_u32 s61, 0\n"
    "s_cbranch_scc1 .Lla_78\n"
    "s_mov_b64 exec, 1\n"
    "ds_add_u32 v253, v252 offset:0\n"
    "s_mov_b64 exec, -1\n"
    "s_add_u32 s55, s55, 4\n"
    "s_cmp_eq_u32 s78, 0\n"
    "s_cbranch_scc0 .Lla_78\n"
    "s_mov_b64 exec, 1\n"
    "ds_add_u32 v253, v252 offset:8\n"
    "s_mov_b64 exec, -1\n"
    ".Lla_78:\n"
    "s_waitcnt lgkmcnt(14)\n"
    "v_mfma_f32_16x16x32_bf16 v[0:3], v[144:147], v[200:203], v[0:3]\n"
    "s_waitcnt lgkmcnt(12)\n"
    "v_mfma_f32_16x16x32_bf16 v[12:15], v[148:151], v[200:203], v[12:15]\n"
    "s_waitcnt lgkmcnt(10)\n"
    "v_mfma_f32_16x16x32_bf16 v[24:27], v[152:155], v[200:203], v[24:27]\n"
    "s_waitcnt lgkmcnt(8)\n"
    "v_mfma_f32_16x16x32_bf16 v[36:39], v[156:159], v[200:203], v[36:39]\n"
    "s_waitcnt lgkmcnt(6)\n"
    "v_mfma_f32_16x16x32_bf16 v[48:51], v[160:163], v[200:203], v[48:51]\n"
    "s_waitcnt lgkmcnt(4)\n"
    "v_mfma_f32_16x16x32_bf16 v[60:63], v[164:167], v[200:203], v[60:63]\n"
    "s_waitcnt lgkmcnt(2)\n"
    "v_mfma_f32_16x16x32_bf16 v[72:75], v[168:171], v[200:203], v[72:75]\n"
    "s_waitcnt lgkmcnt(0)\n"
    "v_mfma_f32_16x16x32_bf16 v[84:87], v[172:175], v[200:203], v[84:87]\n"
    "s_cmp_eq_u32 s61, 0\n"
    "s_cbranch_scc1 .Llp_79\n"
    "s_mov_b64 exec, 1\n"
    ".Lsp_80:\n"
    "ds_read_b32 v251, v253 offset:0\n"
    "s_waitcnt lgkmcnt(0)\n"
    "v_readfirstlane_b32 s64, v251\n"
    "s_nop 0\n"
    "s_cmp_ge_u32 s64, s55\n"
    "s_cbranch_scc1 .Lok_81\n"
    "s_sleep 1\n"
    "s_branch .Lsp_80\n"
    ".Lok_81:\n"
    "s_mov_b64 exec, -1\n"
    ".Llp_79:\n"
    "v_mfma_f32_16x16x32_bf16 v[88:91], v[172:175], v[204:207], v[88:91]\n"
    "v_cvt_pk_bf16_f32 v96, v0, v1\n"
    "v_cvt_pk_bf16_f32 v97, v2, v3\n"
    "v_cvt_pk_bf16_f32 v98, v12, v13\n"
    "v_mfma_f32_16x16x32_bf16 v[76:79], v[168:171], v[204:207], v[76:79]\n"
    "v_cvt_pk_bf16_f32 v99, v14, v15\n"
    "ds_write_b128 v237, v[96:99] offset:0\n"
    "v_mfma_f32_16x16x32_bf16 v[64:67], v[164:167], v[204:207], v[64:67]\n"
    "v_cvt_pk_bf16_f32 v100, v24, v25\n"
    "v_cvt_pk_bf16_f32 v101, v26, v27\n"
    "v_cvt_pk_bf16_f32 v102, v36, v37\n"
    "v_mfma_f32_16x16x32_bf16 v[52:55], v[160:163], v[204:207], v[52:55]\n"
    "v_cvt_pk_bf16_f32 v103, v38, v39\n"
    "ds_write_b128 v237, v[100:103] offset:1024\n"
    "v_mfma_f32_16x16x32_bf16 v[40:43], v[156:159], v[204:207], v[40:43]\n"
    "v_cvt_pk_bf16_f32 v104, v48, v49\n"
    "v_cvt_pk_bf16_f32 v105, v50, v51\n"
    "v_cvt_pk_bf16_f32 v106, v60, v61\n"
    "v_mfma_f32_16x16x32_bf16 v[28:31], v[152:155], v[204:207], v[28:31]\n"
    "v_cvt_pk_bf16_f32 v107, v62, v63\n"
    "ds_write_b128 v237, v[104:107] offset:6144\n"
    "v_mfma_f32_16x16x32_bf16 v[16:19], v[148:151], v[204:207], v[16:19]\n"
    "v_cvt_pk_bf16_f32 v108, v72, v73\n"
    "v_cvt_pk_bf16_f32 v109, v74, v75\n"
    "v_cvt_pk_bf16_f32 v110, v84, v85\n"
    "v_mfma_f32_16x16x32_bf16 v[4:7], v[144:147], v[204:207], v[4:7]\n"
    "v_cvt_pk_bf16_f32 v111, v86, v87\n"
    "ds_write_b128 v237, v[108:111] offset:7168\n"
    "v_mfma_f32_16x16x32_bf16 v[8:11], v[144:147], v[208:211], v[8:11]\n"
    "v_cvt_pk_bf16_f32 v124, v76, v77\n"
    "v_cvt_pk_bf16_f32 v125, v78, v79\n"
    "v_cvt_pk_bf16_f32 v126, v88, v89\n"
    "v_mfma_f32_16x16x32_bf16 v[20:23], v[148:151], v[208:211], v[20:23]\n"
    "v_cvt_pk_bf16_f32 v127, v90, v91\n"
    "ds_write_b128 v237, v[124:127] offset:9216\n"
    "v_mfma_f32_16x16x32_bf16 v[32:35], v[152:155], v[208:211], v[32:35]\n"
    "v_cvt_pk_bf16_f32 v120, v52, v53\n"
    "v_cvt_pk_bf16_f32 v121, v54, v55\n"
    "v_cvt_pk_bf16_f32 v122, v64, v65\n"
    "v_mfma_f32_16x16x32_bf16 v[44:47], v[156:159], v[208:211], v[44:47]\n"
    "v_cvt_pk_bf16_f32 v123, v66, v67\n"
    "ds_write_b128 v237, v[120:123] offset:8192\n"
    "v_mfma_f32_16x16x32_bf16 v[56:59], v[160:163], v[208:211], v[56:59]\n"
    "v_cvt_pk_bf16_f32 v116, v28, v29\n"
    "v_cvt_pk_bf16_f32 v117, v30, v31\n"
    "v_cvt_pk_bf16_f32 v118, v40, v41\n"
    "v_mfma_f32_16x16x32_bf16 v[68:71], v[164:167], v[208:211], v[68:71]\n"
    "v_cvt_pk_bf16_f32 v119, v42, v43\n"
    "ds_write_b128 v237, v[116:119] offset:3072\n"
    "v_mfma_f32_16x16x32_bf16 v[80:83], v[168:171], v[208:211], v[80:83]\n"
    "v_cvt_pk_bf16_f32 v112, v4, v5\n"
    "v_cvt_pk_bf16_f32 v113, v6, v7\n"
    "v_cvt_pk_bf16_f32 v114, v16, v17\n"
    "v_mfma_f32_16x16x32_bf16 v[92:95], v[172:175], v[208:211], v[92:95]\n"
    "v_cvt_pk_bf16_f32 v115, v18, v19\n"
    "ds_write_b128 v237, v[112:115] offset:2048\n"
    "v_cvt_pk_bf16_f32 v128, v8, v9\n"
    "v_cvt_pk_bf16_f32 v129, v10, v11\n"
    "v_cvt_pk_bf16_f32 v130, v20, v21\n"
    "v_cvt_pk_bf16_f32 v131, v22, v23\n"
    "ds_write_b128 v237, v[128:131] offset:4096\n"
    "v_cvt_pk_bf16_f32 v132, v32, v33\n"
    "v_cvt_pk_bf16_f32 v133, v34, v35\n"
    "v_cvt_pk_bf16_f32 v134, v44, v45\n"
    "v_cvt_pk_bf16_f32 v135, v46, v47\n"
    "ds_write_b128 v237, v[132:135] offset:5120\n"
    "v_cvt_pk_bf16_f32 v136, v56, v57\n"
    "v_cvt_pk_bf16_f32 v137, v58, v59\n"
    "v_cvt_pk_bf16_f32 v138, v68, v69\n"
    "v_cvt_pk_bf16_f32 v139, v70, v71\n"
    "ds_write_b128 v237, v[136:139] offset:10240\n"
    "v_cvt_pk_bf16_f32 v140, v80, v81\n"
    "v_cvt_pk_bf16_f32 v141, v82, v83\n"
    "v_cvt_pk_bf16_f32 v142, v92, v93\n"
    "v_cvt_pk_bf16_f32 v143, v94, v95\n"
    "ds_write_b128 v237, v[140:143] offset:11264\n"
    "s_waitcnt lgkmcnt(0)\n"
    "s_cmp_eq_u32 s61, 0\n"
    "s_cbranch_scc0 .Losetup\n"
    "s_cmp_eq_u32 s45, 0\n"
    "s_cbranch_scc0 .Lm0_82\n"
    "s_mov_b64 exec, 1\n"
    "ds_add_u32 v253, v252 offset:4\n"
    "s_mov_b64 exec, -1\n"
    ".Lm0_82:\n"
    "s_barrier\n"
    "ds_read_b64 v[246:247], v253 offset:24\n"
    "s_waitcnt lgkmcnt(0)\n"
    "v_readfirstlane_b32 s82, v246\n"
    "v_readfirstlane_b32 s83, v247\n"
    "s_add_u32 s82, s82, 0x7f4a7c15\n"
    "s_addc_u32 s83, s83, 0x9e3779b9\n"
    "s_branch .Lreduce\n"
    ".Lprojo:\n"
    "ds_read_b128 v[192:195], v240 offset:0\n"
    "ds_read_b128 v[196:199], v240 offset:2048\n"
    "ds_read_b128 v[200:203], v240 offset:4096\n"
    "s_waitcnt vmcnt(16)\n"
    "v_perm_b32 v219, v124, v120, s59\n"
    "v_perm_b32 v218, v116, v112, s59\n"
    "v_perm_b32 v217, v108, v104, s59\n"
    "v_perm_b32 v216, v100, v96, s59\n"
    "v_perm_b32 v220, v100, v96, s60\n"
    "v_perm_b32 v221, v108, v104, s60\n"
    "v_perm_b32 v222, v116, v112, s60\n"
    "v_perm_b32 v223, v124, v120, s60\n"
    "s_waitcnt lgkmcnt(2)\n"
    "v_mfma_f32_16x16x32_bf16 v[0:3], v[192:195], v[216:219], 0\n"
    "v_perm_b32 v224, v101, v97, s59\n"
    "v_perm_b32 v225, v109, v105, s59\n"
    "v_mfma_f32_16x16x32_bf16 v[12:15], v[192:195], v[220:223], 0\n"
    "v_perm_b32 v226, v117, v113, s59\n"
    "v_perm_b32 v227, v125, v121, s59\n"
    "s_waitcnt lgkmcnt(1)\n"
    "v_mfma_f32_16x16x32_bf16 v[16:19], v[196:199], v[220:223], 0\n"
    "v_mfma_f32_16x16x32_bf16 v[4:7], v[196:199], v[216:219], 0\n"
    "v_perm_b32 v228, v101, v97, s60\n"
    "v_perm_b32 v229, v109, v105, s60\n"
    "s_waitcnt lgkmcnt(0)\n"
    "v_mfma_f32_16x16x32_bf16 v[8:11], v[200:203], v[216:219], 0\n"
    "v_perm_b32 v230, v117, v113, s60\n"
    "v_perm_b32 v231, v125, v121, s60\n"
    "v_mfma_f32_16x16x32_bf16 v[20:23], v[200:203], v[220:223], 0\n"
    "v_mfma_f32_16x16x32_bf16 v[32:35], v[200:203], v[224:227], 0\n"
    "v_perm_b32 v216, v102, v98, s59\n"
    "v_perm_b32 v217, v110, v106, s59\n"
    "v_mfma_f32_16x16x32_bf16 v[44:47], v[200:203], v[228:231], 0\n"
    "v_perm_b32 v218, v118, v114, s59\n"
    "v_perm_b32 v219, v126, v122, s59\n"
    "v_mfma_f32_16x16x32_bf16 v[40:43], v[196:199], v[228:231], 0\n"
    "v_mfma_f32_16x16x32_bf16 v[28:31], v[196:199], v[224:227], 0\n"
    "v_perm_b32 v220, v102, v98, s60\n"
    "v_perm_b32 v221, v110, v106, s60\n"
    "v_mfma_f32_16x16x32_bf16 v[24:27], v[192:195], v[224:227], 0\n"
    "v_perm_b32 v222, v118, v114, s60\n"
    "v_perm_b32 v223, v126, v122, s60\n"
    "v_mfma_f32_16x16x32_bf16 v[36:39], v[192:195], v[228:231], 0\n"
    "ds_read_b128 v[204:207], v241 offset:0\n"
    "v_mfma_f32_16x16x32_bf16 v[48:51], v[192:195], v[216:219], 0\n"
    "v_perm_b32 v224, v103, v99, s59\n"
    "v_perm_b32 v225, v111, v107, s59\n"
    "v_mfma_f32_16x16x32_bf16 v[60:63], v[192:195], v[220:223], 0\n"
    "v_perm_b32 v226, v119, v115, s59\n"
    "v_perm_b32 v227, v127, v123, s59\n"
    "v_mfma_f32_16x16x32_bf16 v[64:67], v[196:199], v[220:223], 0\n"
    "ds_read_b128 v[208:211], v241 offset:2048\n"
    "v_mfma_f32_16x16x32_bf16 v[52:55], v[196:199], v[216:219], 0\n"
    "v_perm_b32 v228, v103, v99, s60\n"
    "v_perm_b32 v229, v111, v107, s60\n"
    "v_mfma_f32_16x16x32_bf16 v[56:59], v[200:203], v[216:219], 0\n"
    "v_perm_b32 v230, v119, v115, s60\n"
    "v_perm_b32 v231, v127, v123, s60\n"
    "v_mfma_f32_16x16x32_bf16 v[68:71], v[200:203], v[220:223], 0\n"
    "ds_read_b128 v[212:215], v241 offset:4096\n"
    "v_mfma_f32_16x16x32_bf16 v[80:83], v[200:203], v[224:227], 0\n"
    "s_waitcnt vmcnt(8)\n"
    "v_perm_b32 v219, v156, v152, s59\n"
    "v_perm_b32 v218, v148, v144, s59\n"
    "v_mfma_f32_16x16x32_bf16 v[92:95], v[200:203], v[228:231], 0\n"
    "v_perm_b32 v217, v140, v136, s59\n"
    "v_perm_b32 v216, v132, v128, s59\n"
    "s_add_u32 s52, s52, s53\n"
    "buffer_load_dwordx4 v[96:99], v232, s[28:31], s52 offen\n"
    "v_mfma_f32_16x16x32_bf16 v[88:91], v[196:199], v[228:231], 0\n"
    "buffer_load_dwordx4 v[100:103], v233, s[28:31], s52 offen\n"
    "v_mfma_f32_16x16x32_bf16 v[76:79], v[196:199], v[224:227], 0\n"
    "v_perm_b32 v220, v132, v128, s60\n"
    "v_perm_b32 v221, v140, v136, s60\n"
    "v_mfma_f32_16x16x32_bf16 v[72:75], v[192:195], v[224:227], 0\n"
    "v_perm_b32 v222, v148, v144, s60\n"
    "v_perm_b32 v223, v156, v152, s60\n"
    "buffer_load_dwordx4 v[104:107], v234, s[28:31], s52 offen\n"
    "v_mfma_f32_16x16x32_bf16 v[84:87], v[192:195], v[228:231], 0\n"
    "buffer_load_dwordx4 v[108:111], v235, s[28:31], s52 offen\n"
    "s_waitcnt lgkmcnt(0)\n"
    "v_mfma_f32_16x16x32_bf16 v[0:3], v[204:207], v[216:219], v[0:3]\n"
    "v_perm_b32 v224, v133, v129, s59\n"
    "v_perm_b32 v225, v141, v137, s59\n"
    "v_mfma_f32_16x16x32_bf16 v[12:15], v[204:207], v[220:223], v[12:15]\n"
    "v_perm_b32 v226, v149, v145, s59\n"
    "v_perm_b32 v227, v157, v153, s59\n"
    "buffer_load_dwordx4 v[112:115], v236, s[28:31], s52 offen\n"
    "v_mfma_f32_16x16x32_bf16 v[16:19], v[208:211], v[220:223], v[16:19]\n"
    "buffer_load_dwordx4 v[116:119], v237, s[28:31], s52 offen\n"
    "v_mfma_f32_16x16x32_bf16 v[4:7], v[208:211], v[216:219], v[4:7]\n"
    "v_perm_b32 v228, v133, v129, s60\n"
    "v_perm_b32 v229, v141, v137, s60\n"
    "v_mfma_f32_16x16x32_bf16 v[8:11], v[212:215], v[216:219], v[8:11]\n"
    "v_perm_b32 v230, v149, v145, s60\n"
    "v_perm_b32 v231, v157, v153, s60\n"
    "buffer_load_dwordx4 v[120:123], v238, s[28:31], s52 offen\n"
    "v_mfma_f32_16x16x32_bf16 v[20:23], v[212:215], v[220:223], v[20:23]\n"
    "buffer_load_dwordx4 v[124:127], v239, s[28:31], s52 offen\n"
    "v_mfma_f32_16x16x32_bf16 v[32:35], v[212:215], v[224:227], v[32:35]\n"
    "v_perm_b32 v216, v134, v130, s59\n"
    "v_perm_b32 v217, v142, v138, s59\n"
    "v_mfma_f32_16x16x32_bf16 v[44:47], v[212:215], v[228:231], v[44:47]\n"
    "v_perm_b32 v218, v150, v146, s59\n"
    "v_perm_b32 v219, v158, v154, s59\n"
    "v_mfma_f32_16x16x32_bf16 v[40:43], v[208:211], v[228:231], v[40:43]\n"
    "v_mfma_f32_16x16x32_bf16 v[28:31], v[208:211], v[224:227], v[28:31]\n"
    "v_perm_b32 v220, v134, v130, s60\n"
    "v_perm_b32 v221, v142, v138, s60\n"
    "v_mfma_f32_16x16x32_bf16 v[24:27], v[204:207], v[224:227], v[24:27]\n"
    "v_perm_b32 v222, v150, v146, s60\n"
    "v_perm_b32 v223, v158, v154, s60\n"
    "v_mfma_f32_16x16x32_bf16 v[36:39], v[204:207], v[228:231], v[36:39]\n"
    "ds_read_b128 v[192:195], v240 offset:6144\n"
    "v_mfma_f32_16x16x32_bf16 v[48:51], v[204:207], v[216:219], v[48:51]\n"
    "v_perm_b32 v224, v135, v131, s59\n"
    "v_perm_b32 v225, v143, v139, s59\n"
    "v_mfma_f32_16x16x32_bf16 v[60:63], v[204:207], v[220:223], v[60:63]\n"
    "v_perm_b32 v226, v151, v147, s59\n"
    "v_perm_b32 v227, v159, v155, s59\n"
    "v_mfma_f32_16x16x32_bf16 v[64:67], v[208:211], v[220:223], v[64:67]\n"
    "ds_read_b128 v[196:199], v240 offset:8192\n"
    "v_mfma_f32_16x16x32_bf16 v[52:55], v[208:211], v[216:219], v[52:55]\n"
    "v_perm_b32 v228, v135, v131, s60\n"
    "v_perm_b32 v229, v143, v139, s60\n"
    "v_mfma_f32_16x16x32_bf16 v[56:59], v[212:215], v[216:219], v[56:59]\n"
    "v_perm_b32 v230, v151, v147, s60\n"
    "v_perm_b32 v231, v159, v155, s60\n"
    "v_mfma_f32_16x16x32_bf16 v[68:71], v[212:215], v[220:223], v[68:71]\n"
    "ds_read_b128 v[200:203], v240 offset:10240\n"
    "v_mfma_f32_16x16x32_bf16 v[80:83], v[212:215], v[224:227], v[80:83]\n"
    "s_waitcnt vmcnt(8)\n"
    "v_perm_b32 v219, v188, v184, s59\n"
    "v_perm_b32 v218, v180, v176, s59\n"
    "v_mfma_f32_16x16x32_bf16 v[92:95], v[212:215], v[228:231], v[92:95]\n"
    "v_perm_b32 v217, v172, v168, s59\n"
    "v_perm_b32 v216, v164, v160, s59\n"
    "s_add_u32 s52, s52, s53\n"
    "buffer_load_dwordx4 v[128:131], v232, s[28:31], s52 offen\n"
    "v_mfma_f32_16x16x32_bf16 v[88:91], v[208:211], v[228:231], v[88:91]\n"
    "buffer_load_dwordx4 v[132:135], v233, s[28:31], s52 offen\n"
    "v_mfma_f32_16x16x32_bf16 v[76:79], v[208:211], v[224:227], v[76:79]\n"
    "v_perm_b32 v220, v164, v160, s60\n"
    "v_perm_b32 v221, v172, v168, s60\n"
    "v_mfma_f32_16x16x32_bf16 v[72:75], v[204:207], v[224:227], v[72:75]\n"
    "v_perm_b32 v222, v180, v176, s60\n"
    "v_perm_b32 v223, v188, v184, s60\n"
    "buffer_load_dwordx4 v[136:139], v234, s[28:31], s52 offen\n"
    "v_mfma_f32_16x16x32_bf16 v[84:87], v[204:207], v[228:231], v[84:87]\n"
    "buffer_load_dwordx4 v[140:143], v235, s[28:31], s52 offen\n"
    "s_waitcnt lgkmcnt(0)\n"
    "v_mfma_f32_16x16x32_bf16 v[0:3], v[192:195], v[216:219], v[0:3]\n"
    "v_perm_b32 v224, v165, v161, s59\n"
    "v_perm_b32 v225, v173, v169, s59\n"
    "v_mfma_f32_16x16x32_bf16 v[12:15], v[192:195], v[220:223], v[12:15]\n"
    "v_perm_b32 v226, v181, v177, s59\n"
    "v_perm_b32 v227, v189, v185, s59\n"
    "buffer_load_dwordx4 v[144:147], v236, s[28:31], s52 offen\n"
    "v_mfma_f32_16x16x32_bf16 v[16:19], v[196:199], v[220:223], v[16:19]\n"
    "buffer_load_dwordx4 v[148:151], v237, s[28:31], s52 offen\n"
    "v_mfma_f32_16x16x32_bf16 v[4:7], v[196:199], v[216:219], v[4:7]\n"
    "v_perm_b32 v228, v165, v161, s60\n"
    "v_perm_b32 v229, v173, v169, s60\n"
    "v_mfma_f32_16x16x32_bf16 v[8:11], v[200:203], v[216:219], v[8:11]\n"
    "v_perm_b32 v230, v181, v177, s60\n"
    "v_perm_b32 v231, v189, v185, s60\n"
    "buffer_load_dwordx4 v[152:155], v238, s[28:31], s52 offen\n"
    "v_mfma_f32_16x16x32_bf16 v[20:23], v[200:203], v[220:223], v[20:23]\n"
    "buffer_load_dwordx4 v[156:159], v239, s[28:31], s52 offen\n"
    "v_mfma_f32_16x16x32_bf16 v[32:35], v[200:203], v[224:227], v[32:35]\n"
    "v_perm_b32 v216, v166, v162, s59\n"
    "v_perm_b32 v217, v174, v170, s59\n"
    "v_mfma_f32_16x16x32_bf16 v[44:47], v[200:203], v[228:231], v[44:47]\n"
    "v_perm_b32 v218, v182, v178, s59\n"
    "v_perm_b32 v219, v190, v186, s59\n"
    "v_mfma_f32_16x16x32_bf16 v[40:43], v[196:199], v[228:231], v[40:43]\n"
    "v_mfma_f32_16x16x32_bf16 v[28:31], v[196:199], v[224:227], v[28:31]\n"
    "v_perm_b32 v220, v166, v162, s60\n"
    "v_perm_b32 v221, v174, v170, s60\n"
    "v_mfma_f32_16x16x32_bf16 v[24:27], v[192:195], v[224:227], v[24:27]\n"
    "v_perm_b32 v222, v182, v178, s60\n"
    "v_perm_b32 v223, v190, v186, s60\n"
    "v_mfma_f32_16x16x32_bf16 v[36:39], v[192:195], v[228:231], v[36:39]\n"
    "ds_read_b128 v[204:207], v241 offset:6144\n"
    "v_mfma_f32_16x16x32_bf16 v[48:51], v[192:195], v[216:219], v[48:51]\n"
    "v_perm_b32 v224, v167, v163, s59\n"
    "v_perm_b32 v225, v175, v171, s59\n"
    "v_mfma_f32_16x16x32_bf16 v[60:63], v[192:195], v[220:223], v[60:63]\n"
    "v_perm_b32 v226, v183, v179, s59\n"
    "v_perm_b32 v227, v191, v187, s59\n"
    "v_mfma_f32_16x16x32_bf16 v[64:67], v[196:199], v[220:223], v[64:67]\n"
    "ds_read_b128 v[208:211], v241 offset:8192\n"
    "v_mfma_f32_16x16x32_bf16 v[52:55], v[196:199], v[216:219], v[52:55]\n"
    "v_perm_b32 v228, v167, v163, s60\n"
    "v_perm_b32 v229, v175, v171, s60\n"
    "v_mfma_f32_16x16x32_bf16 v[56:59], v[200:203], v[216:219], v[56:59]\n"
    "v_perm_b32 v230, v183, v179, s60\n"
    "v_perm_b32 v231, v191, v187, s60\n"
    "v_mfma_f32_16x16x32_bf16 v[68:71], v[200:203], v[220:223], v[68:71]\n"
    "ds_read_b128 v[212:215], v241 offset:10240\n"
    "v_mfma_f32_16x16x32_bf16 v[80:83], v[200:203], v[224:227], v[80:83]\n"
    "s_waitcnt vmcnt(8)\n"
    "v_perm_b32 v219, v124, v120, s59\n"
    "v_perm_b32 v218, v116, v112, s59\n"
    "v_mfma_f32_16x16x32_bf16 v[92:95], v[200:203], v[228:231], v[92:95]\n"
    "v_perm_b32 v217, v108, v104, s59\n"
    "v_perm_b32 v216, v100, v96, s59\n"
    "s_add_u32 s52, s52, s53\n"
    "buffer_load_dwordx4 v[160:163], v232, s[28:31], s52 offen\n"
    "v_mfma_f32_16x16x32_bf16 v[88:91], v[196:199], v[228:231], v[88:91]\n"
    "buffer_load_dwordx4 v[164:167], v233, s[28:31], s52 offen\n"
    "v_mfma_f32_16x16x32_bf16 v[76:79], v[196:199], v[224:227], v[76:79]\n"
    "v_perm_b32 v220, v100, v96, s60\n"
    "v_perm_b32 v221, v108, v104, s60\n"
    "v_mfma_f32_16x16x32_bf16 v[72:75], v[192:195], v[224:227], v[72:75]\n"
    "v_perm_b32 v222, v116, v112, s60\n"
    "v_perm_b32 v223, v124, v120, s60\n"
    "buffer_load_dwordx4 v[168:171], v234, s[28:31], s52 offen\n"
    "v_mfma_f32_16x16x32_bf16 v[84:87], v[192:195], v[228:231], v[84:87]\n"
    "buffer_load_dwordx4 v[172:175], v235, s[28:31], s52 offen\n"
    "s_waitcnt lgkmcnt(0)\n"
    "v_mfma_f32_16x16x32_bf16 v[0:3], v[204:207], v[216:219], v[0:3]\n"
    "v_perm_b32 v224, v101, v97, s59\n"
    "v_perm_b32 v225, v109, v105, s59\n"
    "v_mfma_f32_16x16x32_bf16 v[12:15], v[204:207], v[220:223], v[12:15]\n"
    "v_perm_b32 v226, v117, v113, s59\n"
    "v_perm_b32 v227, v125, v121, s59\n"
    "buffer_load_dwordx4 v[176:179], v236, s[28:31], s52 offen\n"
    "v_mfma_f32_16x16x32_bf16 v[16:19], v[208:211], v[220:223], v[16:19]\n"
    "buffer_load_dwordx4 v[180:183], v237, s[28:31], s52 offen\n"
    "v_mfma_f32_16x16x32_bf16 v[4:7], v[208:211], v[216:219], v[4:7]\n"
    "v_perm_b32 v228, v101, v97, s60\n"
    "v_perm_b32 v229, v109, v105, s60\n"
    "v_mfma_f32_16x16x32_bf16 v[8:11], v[212:215], v[216:219], v[8:11]\n"
    "v_perm_b32 v230, v117, v113, s60\n"
    "v_perm_b32 v231, v125, v121, s60\n"
    "buffer_load_dwordx4 v[184:187], v238, s[28:31], s52 offen\n"
    "v_mfma_f32_16x16x32_bf16 v[20:23], v[212:215], v[220:223], v[20:23]\n"
    "buffer_load_dwordx4 v[188:191], v239, s[28:31], s52 offen\n"
    "s_cmp_eq_u32 s74, 0\n"
    "s_cbranch_scc0 .Leb_83\n"
    "s_barrier\n"
    ".Leb_83:\n"
    "v_mfma_f32_16x16x32_bf16 v[32:35], v[212:215], v[224:227], v[32:35]\n"
    "v_perm_b32 v216, v102, v98, s59\n"
    "v_perm_b32 v217, v110, v106, s59\n"
    "v_mfma_f32_16x16x32_bf16 v[44:47], v[212:215], v[228:231], v[44:47]\n"
    "v_perm_b32 v218, v118, v114, s59\n"
    "v_perm_b32 v219, v126, v122, s59\n"
    "v_mfma_f32_16x16x32_bf16 v[40:43], v[208:211], v[228:231], v[40:43]\n"
    "v_add_u32 v240, s54, v240\n"
    "v_mfma_f32_16x16x32_bf16 v[28:31], v[208:211], v[224:227], v[28:31]\n"
    "v_perm_b32 v220, v102, v98, s60\n"
    "v_perm_b32 v221, v110, v106, s60\n"
    "v_mfma_f32_16x16x32_bf16 v[24:27], v[204:207], v[224:227], v[24:27]\n"
    "v_perm_b32 v222, v118, v114, s60\n"
    "v_perm_b32 v223, v126, v122, s60\n"
    "v_mfma_f32_16x16x32_bf16 v[36:39], v[204:207], v[228:231], v[36:39]\n"
    "ds_read_b128 v[192:195], v240 offset:0\n"
    "v_mfma_f32_16x16x32_bf16 v[48:51], v[204:207], v[216:219], v[48:51]\n"
    "v_perm_b32 v224, v103, v99, s59\n"
    "v_perm_b32 v225, v111, v107, s59\n"
    "v_mfma_f32_16x16x32_bf16 v[60:63], v[204:207], v[220:223], v[60:63]\n"
    "v_perm_b32 v226, v119, v115, s59\n"
    "v_perm_b32 v227, v127, v123, s59\n"
    "v_mfma_f32_16x16x32_bf16 v[64:67], v[208:211], v[220:223], v[64:67]\n"
    "ds_read_b128 v[196:199], v240 offset:2048\n"
    "v_mfma_f32_16x16x32_bf16 v[52:55], v[208:211], v[216:219], v[52:55]\n"
    "v_perm_b32 v228, v103, v99, s60\n"
    "v_perm_b32 v229, v111, v107, s60\n"
    "v_mfma_f32_16x16x32_bf16 v[56:59], v[212:215], v[216:219], v[56:59]\n"
    "v_perm_b32 v230, v119, v115, s60\n"
    "v_perm_b32 v231, v127, v123, s60\n"
    "v_mfma_f32_16x16x32_bf16 v[68:71], v[212:215], v[220:223], v[68:71]\n"
    "ds_read_b128 v[200:203], v240 offset:4096\n"
    "s_cmp_eq_u32 s74, 0\n"
    "s_cbranch_scc0 .Llf_84\n"
    "s_waitcnt vmcnt(0)\n"
    "s_mov_b64 exec, 1\n"
    "global_atomic_swap_x2 v246, v[248:249], s[84:85] sc1\n"
    "s_mov_b64 exec, -1\n"
    ".Llf_84:\n"
    "v_mfma_f32_16x16x32_bf16 v[80:83], v[212:215], v[224:227], v[80:83]\n"
    "s_waitcnt vmcnt(8)\n"
    "v_perm_b32 v219, v156, v152, s59\n"
    "v_perm_b32 v218, v148, v144, s59\n"
    "v_mfma_f32_16x16x32_bf16 v[92:95], v[212:215], v[228:231], v[92:95]\n"
    "v_perm_b32 v217, v140, v136, s59\n"
    "v_perm_b32 v216, v132, v128, s59\n"
    "s_add_u32 s52, s52, s53\n"
    "buffer_load_dwordx4 v[96:99], v232, s[28:31], s52 offen\n"
    "v_mfma_f32_16x16x32_bf16 v[88:91], v[208:211], v[228:231], v[88:91]\n"
    "buffer_load_dwordx4 v[100:103], v233, s[28:31], s52 offen\n"
    "v_mfma_f32_16x16x32_bf16 v[76:79], v[208:211], v[224:227], v[76:79]\n"
    "v_perm_b32 v220, v132, v128, s60\n"
    "v_perm_b32 v221, v140, v136, s60\n"
    "v_mfma_f32_16x16x32_bf16 v[72:75], v[204:207], v[224:227], v[72:75]\n"
    "v_perm_b32 v222, v148, v144, s60\n"
    "v_perm_b32 v223, v156, v152, s60\n"
    "buffer_load_dwordx4 v[104:107], v234, s[28:31], s52 offen\n"
    "v_mfma_f32_16x16x32_bf16 v[84:87], v[204:207], v[228:231], v[84:87]\n"
    "buffer_load_dwordx4 v[108:111], v235, s[28:31], s52 offen\n"
    "s_waitcnt lgkmcnt(0)\n"
    "v_mfma_f32_16x16x32_bf16 v[0:3], v[192:195], v[216:219], v[0:3]\n"
    "v_perm_b32 v224, v133, v129, s59\n"
    "v_perm_b32 v225, v141, v137, s59\n"
    "v_mfma_f32_16x16x32_bf16 v[12:15], v[192:195], v[220:223], v[12:15]\n"
    "v_perm_b32 v226, v149, v145, s59\n"
    "v_perm_b32 v227, v157, v153, s59\n"
    "buffer_load_dwordx4 v[112:115], v236, s[28:31], s52 offen\n"
    "v_mfma_f32_16x16x32_bf16 v[16:19], v[196:199], v[220:223], v[16:19]\n"
    "buffer_load_dwordx4 v[116:119], v237, s[28:31], s52 offen\n"
    "v_mfma_f32_16x16x32_bf16 v[4:7], v[196:199], v[216:219], v[4:7]\n"
    "v_perm_b32 v228, v133, v129, s60\n"
    "v_perm_b32 v229, v141, v137, s60\n"
    "v_mfma_f32_16x16x32_bf16 v[8:11], v[200:203], v[216:219], v[8:11]\n"
    "v_perm_b32 v230, v149, v145, s60\n"
    "v_perm_b32 v231, v157, v153, s60\n"
    "buffer_load_dwordx4 v[120:123], v238, s[28:31], s52 offen\n"
    "v_mfma_f32_16x16x32_bf16 v[20:23], v[200:203], v[220:223], v[20:23]\n"
    "buffer_load_dwordx4 v[124:127], v239, s[28:31], s52 offen\n"
    "v_mfma_f32_16x16x32_bf16 v[32:35], v[200:203], v[224:227], v[32:35]\n"
    "v_perm_b32 v216, v134, v130, s59\n"
    "v_perm_b32 v217, v142, v138, s59\n"
    "v_mfma_f32_16x16x32_bf16 v[44:47], v[200:203], v[228:231], v[44:47]\n"
    "v_perm_b32 v218, v150, v146, s59\n"
    "v_perm_b32 v219, v158, v154, s59\n"
    "v_mfma_f32_16x16x32_bf16 v[40:43], v[196:199], v[228:231], v[40:43]\n"
    "v_add_u32 v241, s54, v241\n"
    "v_mfma_f32_16x16x32_bf16 v[28:31], v[196:199], v[224:227], v[28:31]\n"
    "v_perm_b32 v220, v134, v130, s60\n"
    "v_perm_b32 v221, v142, v138, s60\n"
    "v_mfma_f32_16x16x32_bf16 v[24:27], v[192:195], v[224:227], v[24:27]\n"
    "v_perm_b32 v222, v150, v146, s60\n"
    "v_perm_b32 v223, v158, v154, s60\n"
    "v_mfma_f32_16x16x32_bf16 v[36:39], v[192:195], v[228:231], v[36:39]\n"
    "ds_read_b128 v[204:207], v241 offset:0\n"
    "v_mfma_f32_16x16x32_bf16 v[48:51], v[192:195], v[216:219], v[48:51]\n"
    "v_perm_b32 v224, v135, v131, s59\n"
    "v_perm_b32 v225, v143, v139, s59\n"
    "v_mfma_f32_16x16x32_bf16 v[60:63], v[192:195], v[220:223], v[60:63]\n"
    "v_perm_b32 v226, v151, v147, s59\n"
    "v_perm_b32 v227, v159, v155, s59\n"
    "v_mfma_f32_16x16x32_bf16 v[64:67], v[196:199], v[220:223], v[64:67]\n"
    "ds_read_b128 v[208:211], v241 offset:2048\n"
    "v_mfma_f32_16x16x32_bf16 v[52:55], v[196:199], v[216:219], v[52:55]\n"
    "v_perm_b32 v228, v135, v131, s60\n"
    "v_perm_b32 v229, v143, v139, s60\n"
    "v_mfma_f32_16x16x32_bf16 v[56:59], v[200:203], v[216:219], v[56:59]\n"
    "v_perm_b32 v230, v151, v147, s60\n"
    "v_perm_b32 v231, v159, v155, s60\n"
    "v_mfma_f32_16x16x32_bf16 v[68:71], v[200:203], v[220:223], v[68:71]\n"
    "ds_read_b128 v[212:215], v241 offset:4096\n"
    "v_mfma_f32_16x16x32_bf16 v[80:83], v[200:203], v[224:227], v[80:83]\n"
    "s_waitcnt vmcnt(8)\n"
    "v_perm_b32 v219, v188, v184, s59\n"
    "v_perm_b32 v218, v180, v176, s59\n"
    "v_mfma_f32_16x16x32_bf16 v[92:95], v[200:203], v[228:231], v[92:95]\n"
    "v_perm_b32 v217, v172, v168, s59\n"
    "v_perm_b32 v216, v164, v160, s59\n"
    "s_add_u32 s52, s52, s53\n"
    "buffer_load_dwordx4 v[128:131], v232, s[28:31], s52 offen\n"
    "v_mfma_f32_16x16x32_bf16 v[88:91], v[196:199], v[228:231], v[88:91]\n"
    "buffer_load_dwordx4 v[132:135], v233, s[28:31], s52 offen\n"
    "v_mfma_f32_16x16x32_bf16 v[76:79], v[196:199], v[224:227], v[76:79]\n"
    "v_perm_b32 v220, v164, v160, s60\n"
    "v_perm_b32 v221, v172, v168, s60\n"
    "v_mfma_f32_16x16x32_bf16 v[72:75], v[192:195], v[224:227], v[72:75]\n"
    "v_perm_b32 v222, v180, v176, s60\n"
    "v_perm_b32 v223, v188, v184, s60\n"
    "buffer_load_dwordx4 v[136:139], v234, s[28:31], s52 offen\n"
    "v_mfma_f32_16x16x32_bf16 v[84:87], v[192:195], v[228:231], v[84:87]\n"
    "buffer_load_dwordx4 v[140:143], v235, s[28:31], s52 offen\n"
    "s_waitcnt lgkmcnt(0)\n"
    "v_mfma_f32_16x16x32_bf16 v[0:3], v[204:207], v[216:219], v[0:3]\n"
    "v_perm_b32 v224, v165, v161, s59\n"
    "v_perm_b32 v225, v173, v169, s59\n"
    "v_mfma_f32_16x16x32_bf16 v[12:15], v[204:207], v[220:223], v[12:15]\n"
    "v_perm_b32 v226, v181, v177, s59\n"
    "v_perm_b32 v227, v189, v185, s59\n"
    "buffer_load_dwordx4 v[144:147], v236, s[28:31], s52 offen\n"
    "v_mfma_f32_16x16x32_bf16 v[16:19], v[208:211], v[220:223], v[16:19]\n"
    "buffer_load_dwordx4 v[148:151], v237, s[28:31], s52 offen\n"
    "v_mfma_f32_16x16x32_bf16 v[4:7], v[208:211], v[216:219], v[4:7]\n"
    "v_perm_b32 v228, v165, v161, s60\n"
    "v_perm_b32 v229, v173, v169, s60\n"
    "v_mfma_f32_16x16x32_bf16 v[8:11], v[212:215], v[216:219], v[8:11]\n"
    "v_perm_b32 v230, v181, v177, s60\n"
    "v_perm_b32 v231, v189, v185, s60\n"
    "buffer_load_dwordx4 v[152:155], v238, s[28:31], s52 offen\n"
    "v_mfma_f32_16x16x32_bf16 v[20:23], v[212:215], v[220:223], v[20:23]\n"
    "buffer_load_dwordx4 v[156:159], v239, s[28:31], s52 offen\n"
    "v_mfma_f32_16x16x32_bf16 v[32:35], v[212:215], v[224:227], v[32:35]\n"
    "v_perm_b32 v216, v166, v162, s59\n"
    "v_perm_b32 v217, v174, v170, s59\n"
    "v_mfma_f32_16x16x32_bf16 v[44:47], v[212:215], v[228:231], v[44:47]\n"
    "v_perm_b32 v218, v182, v178, s59\n"
    "v_perm_b32 v219, v190, v186, s59\n"
    "v_mfma_f32_16x16x32_bf16 v[40:43], v[208:211], v[228:231], v[40:43]\n"
    "v_mfma_f32_16x16x32_bf16 v[28:31], v[208:211], v[224:227], v[28:31]\n"
    "v_perm_b32 v220, v166, v162, s60\n"
    "v_perm_b32 v221, v174, v170, s60\n"
    "v_mfma_f32_16x16x32_bf16 v[24:27], v[204:207], v[224:227], v[24:27]\n"
    "v_perm_b32 v222, v182, v178, s60\n"
    "v_perm_b32 v223, v190, v186, s60\n"
    "v_mfma_f32_16x16x32_bf16 v[36:39], v[204:207], v[228:231], v[36:39]\n"
    "ds_read_b128 v[192:195], v240 offset:6144\n"
    "v_mfma_f32_16x16x32_bf16 v[48:51], v[204:207], v[216:219], v[48:51]\n"
    "v_perm_b32 v224, v167, v163, s59\n"
    "v_perm_b32 v225, v175, v171, s59\n"
    "v_mfma_f32_16x16x32_bf16 v[60:63], v[204:207], v[220:223], v[60:63]\n"
    "v_perm_b32 v226, v183, v179, s59\n"
    "v_perm_b32 v227, v191, v187, s59\n"
    "v_mfma_f32_16x16x32_bf16 v[64:67], v[208:211], v[220:223], v[64:67]\n"
    "ds_read_b128 v[196:199], v240 offset:8192\n"
    "v_mfma_f32_16x16x32_bf16 v[52:55], v[208:211], v[216:219], v[52:55]\n"
    "v_perm_b32 v228, v167, v163, s60\n"
    "v_perm_b32 v229, v175, v171, s60\n"
    "v_mfma_f32_16x16x32_bf16 v[56:59], v[212:215], v[216:219], v[56:59]\n"
    "v_perm_b32 v230, v183, v179, s60\n"
    "v_perm_b32 v231, v191, v187, s60\n"
    "v_mfma_f32_16x16x32_bf16 v[68:71], v[212:215], v[220:223], v[68:71]\n"
    "ds_read_b128 v[200:203], v240 offset:10240\n"
    "v_mfma_f32_16x16x32_bf16 v[80:83], v[212:215], v[224:227], v[80:83]\n"
    "s_waitcnt vmcnt(8)\n"
    "v_perm_b32 v219, v124, v120, s59\n"
    "v_perm_b32 v218, v116, v112, s59\n"
    "v_mfma_f32_16x16x32_bf16 v[92:95], v[212:215], v[228:231], v[92:95]\n"
    "v_perm_b32 v217, v108, v104, s59\n"
    "v_perm_b32 v216, v100, v96, s59\n"
    "s_add_u32 s52, s52, s53\n"
    "buffer_load_dwordx4 v[160:163], v232, s[28:31], s52 offen\n"
    "v_mfma_f32_16x16x32_bf16 v[88:91], v[208:211], v[228:231], v[88:91]\n"
    "buffer_load_dwordx4 v[164:167], v233, s[28:31], s52 offen\n"
    "v_mfma_f32_16x16x32_bf16 v[76:79], v[208:211], v[224:227], v[76:79]\n"
    "v_perm_b32 v220, v100, v96, s60\n"
    "v_perm_b32 v221, v108, v104, s60\n"
    "v_mfma_f32_16x16x32_bf16 v[72:75], v[204:207], v[224:227], v[72:75]\n"
    "v_perm_b32 v222, v116, v112, s60\n"
    "v_perm_b32 v223, v124, v120, s60\n"
    "buffer_load_dwordx4 v[168:171], v234, s[28:31], s52 offen\n"
    "v_mfma_f32_16x16x32_bf16 v[84:87], v[204:207], v[228:231], v[84:87]\n"
    "buffer_load_dwordx4 v[172:175], v235, s[28:31], s52 offen\n"
    "s_waitcnt lgkmcnt(0)\n"
    "v_mfma_f32_16x16x32_bf16 v[0:3], v[192:195], v[216:219], v[0:3]\n"
    "v_perm_b32 v224, v101, v97, s59\n"
    "v_perm_b32 v225, v109, v105, s59\n"
    "v_mfma_f32_16x16x32_bf16 v[12:15], v[192:195], v[220:223], v[12:15]\n"
    "v_perm_b32 v226, v117, v113, s59\n"
    "v_perm_b32 v227, v125, v121, s59\n"
    "buffer_load_dwordx4 v[176:179], v236, s[28:31], s52 offen\n"
    "v_mfma_f32_16x16x32_bf16 v[16:19], v[196:199], v[220:223], v[16:19]\n"
    "buffer_load_dwordx4 v[180:183], v237, s[28:31], s52 offen\n"
    "v_mfma_f32_16x16x32_bf16 v[4:7], v[196:199], v[216:219], v[4:7]\n"
    "v_perm_b32 v228, v101, v97, s60\n"
    "v_perm_b32 v229, v109, v105, s60\n"
    "v_mfma_f32_16x16x32_bf16 v[8:11], v[200:203], v[216:219], v[8:11]\n"
    "v_perm_b32 v230, v117, v113, s60\n"
    "v_perm_b32 v231, v125, v121, s60\n"
    "buffer_load_dwordx4 v[184:187], v238, s[28:31], s52 offen\n"
    "v_mfma_f32_16x16x32_bf16 v[20:23], v[200:203], v[220:223], v[20:23]\n"
    "buffer_load_dwordx4 v[188:191], v239, s[28:31], s52 offen\n"
    "v_mfma_f32_16x16x32_bf16 v[32:35], v[200:203], v[224:227], v[32:35]\n"
    "v_perm_b32 v216, v102, v98, s59\n"
    "v_perm_b32 v217, v110, v106, s59\n"
    "v_mfma_f32_16x16x32_bf16 v[44:47], v[200:203], v[228:231], v[44:47]\n"
    "v_perm_b32 v218, v118, v114, s59\n"
    "v_perm_b32 v219, v126, v122, s59\n"
    "v_mfma_f32_16x16x32_bf16 v[40:43], v[196:199], v[228:231], v[40:43]\n"
    "v_mfma_f32_16x16x32_bf16 v[28:31], v[196:199], v[224:227], v[28:31]\n"
    "v_perm_b32 v220, v102, v98, s60\n"
    "v_perm_b32 v221, v110, v106, s60\n"
    "v_mfma_f32_16x16x32_bf16 v[24:27], v[192:195], v[224:227], v[24:27]\n"
    "v_perm_b32 v222, v118, v114, s60\n"
    "v_perm_b32 v223, v126, v122, s60\n"
    "v_mfma_f32_16x16x32_bf16 v[36:39], v[192:195], v[228:231], v[36:39]\n"
    "ds_read_b128 v[204:207], v241 offset:6144\n"
    "v_mfma_f32_16x16x32_bf16 v[48:51], v[192:195], v[216:219], v[48:51]\n"
    "v_perm_b32 v224, v103, v99, s59\n"
    "v_perm_b32 v225, v111, v107, s59\n"
    "v_mfma_f32_16x16x32_bf16 v[60:63], v[192:195], v[220:223], v[60:63]\n"
    "v_perm_b32 v226, v119, v115, s59\n"
    "v_perm_b32 v227, v127, v123, s59\n"
    "v_mfma_f32_16x16x32_bf16 v[64:67], v[196:199], v[220:223], v[64:67]\n"
    "ds_read_b128 v[208:211], v241 offset:8192\n"
    "v_mfma_f32_16x16x32_bf16 v[52:55], v[196:199], v[216:219], v[52:55]\n"
    "v_perm_b32 v228, v103, v99, s60\n"
    "v_perm_b32 v229, v111, v107, s60\n"
    "v_mfma_f32_16x16x32_bf16 v[56:59], v[200:203], v[216:219], v[56:59]\n"
    "v_perm_b32 v230, v119, v115, s60\n"
    "v_perm_b32 v231, v127, v123, s60\n"
    "v_mfma_f32_16x16x32_bf16 v[68:71], v[200:203], v[220:223], v[68:71]\n"
    "ds_read_b128 v[212:215], v241 offset:10240\n"
    "v_mfma_f32_16x16x32_bf16 v[80:83], v[200:203], v[224:227], v[80:83]\n"
    "s_waitcnt vmcnt(8)\n"
    "v_perm_b32 v219, v156, v152, s59\n"
    "v_perm_b32 v218, v148, v144, s59\n"
    "v_mfma_f32_16x16x32_bf16 v[92:95], v[200:203], v[228:231], v[92:95]\n"
    "v_perm_b32 v217, v140, v136, s59\n"
    "v_perm_b32 v216, v132, v128, s59\n"
    "s_add_u32 s52, s52, s53\n"
    "buffer_load_dwordx4 v[96:99], v232, s[28:31], s52 offen\n"
    "v_mfma_f32_16x16x32_bf16 v[88:91], v[196:199], v[228:231], v[88:91]\n"
    "buffer_load_dwordx4 v[100:103], v233, s[28:31], s52 offen\n"
    "v_mfma_f32_16x16x32_bf16 v[76:79], v[196:199], v[224:227], v[76:79]\n"
    "v_perm_b32 v220, v132, v128, s60\n"
    "v_perm_b32 v221, v140, v136, s60\n"
    "v_mfma_f32_16x16x32_bf16 v[72:75], v[192:195], v[224:227], v[72:75]\n"
    "v_perm_b32 v222, v148, v144, s60\n"
    "v_perm_b32 v223, v156, v152, s60\n"
    "buffer_load_dwordx4 v[104:107], v234, s[28:31], s52 offen\n"
    "v_mfma_f32_16x16x32_bf16 v[84:87], v[192:195], v[228:231], v[84:87]\n"
    "buffer_load_dwordx4 v[108:111], v235, s[28:31], s52 offen\n"
    "s_waitcnt lgkmcnt(0)\n"
    "v_mfma_f32_16x16x32_bf16 v[0:3], v[204:207], v[216:219], v[0:3]\n"
    "v_perm_b32 v224, v133, v129, s59\n"
    "v_perm_b32 v225, v141, v137, s59\n"
    "v_mfma_f32_16x16x32_bf16 v[12:15], v[204:207], v[220:223], v[12:15]\n"
    "v_perm_b32 v226, v149, v145, s59\n"
    "v_perm_b32 v227, v157, v153, s59\n"
    "buffer_load_dwordx4 v[112:115], v236, s[28:31], s52 offen\n"
    "v_mfma_f32_16x16x32_bf16 v[16:19], v[208:211], v[220:223], v[16:19]\n"
    "buffer_load_dwordx4 v[116:119], v237, s[28:31], s52 offen\n"
    "v_mfma_f32_16x16x32_bf16 v[4:7], v[208:211], v[216:219], v[4:7]\n"
    "v_perm_b32 v228, v133, v129, s60\n"
    "v_perm_b32 v229, v141, v137, s60\n"
    "v_mfma_f32_16x16x32_bf16 v[8:11], v[212:215], v[216:219], v[8:11]\n"
    "v_perm_b32 v230, v149, v145, s60\n"
    "v_perm_b32 v231, v157, v153, s60\n"
    "buffer_load_dwordx4 v[120:123], v238, s[28:31], s52 offen\n"
    "v_mfma_f32_16x16x32_bf16 v[20:23], v[212:215], v[220:223], v[20:23]\n"
    "buffer_load_dwordx4 v[124:127], v239, s[28:31], s52 offen\n"
    "v_mfma_f32_16x16x32_bf16 v[32:35], v[212:215], v[224:227], v[32:35]\n"
    "v_perm_b32 v216, v134, v130, s59\n"
    "v_perm_b32 v217, v142, v138, s59\n"
    "v_mfma_f32_16x16x32_bf16 v[44:47], v[212:215], v[228:231], v[44:47]\n"
    "v_perm_b32 v218, v150, v146, s59\n"
    "v_perm_b32 v219, v158, v154, s59\n"
    "v_mfma_f32_16x16x32_bf16 v[40:43], v[208:211], v[228:231], v[40:43]\n"
    "v_add_u32 v240, s54, v240\n"
    "v_mfma_f32_16x16x32_bf16 v[28:31], v[208:211], v[224:227], v[28:31]\n"
    "v_perm_b32 v220, v134, v130, s60\n"
    "v_perm_b32 v221, v142, v138, s60\n"
    "v_mfma_f32_16x16x32_bf16 v[24:27], v[204:207], v[224:227], v[24:27]\n"
    "v_perm_b32 v222, v150, v146, s60\n"
    "v_perm_b32 v223, v158, v154, s60\n"
    "v_mfma_f32_16x16x32_bf16 v[36:39], v[204:207], v[228:231], v[36:39]\n"
    "ds_read_b128 v[192:195], v240 offset:0\n"
    "v_mfma_f32_16x16x32_bf16 v[48:51], v[204:207], v[216:219], v[48:51]\n"
    "v_perm_b32 v224, v135, v131, s59\n"
    "v_perm_b32 v225, v143, v139, s59\n"
    "v_mfma_f32_16x16x32_bf16 v[60:63], v[204:207], v[220:223], v[60:63]\n"
    "v_perm_b32 v226, v151, v147, s59\n"
    "v_perm_b32 v227, v159, v155, s59\n"
    "v_mfma_f32_16x16x32_bf16 v[64:67], v[208:211], v[220:223], v[64:67]\n"
    "ds_read_b128 v[196:199], v240 offset:2048\n"
    "v_mfma_f32_16x16x32_bf16 v[52:55], v[208:211], v[216:219], v[52:55]\n"
    "v_perm_b32 v228, v135, v131, s60\n"
    "v_perm_b32 v229, v143, v139, s60\n"
    "v_mfma_f32_16x16x32_bf16 v[56:59], v[212:215], v[216:219], v[56:59]\n"
    "v_perm_b32 v230, v151, v147, s60\n"
    "v_perm_b32 v231, v159, v155, s60\n"
    "v_mfma_f32_16x16x32_bf16 v[68:71], v[212:215], v[220:223], v[68:71]\n"
    "ds_read_b128 v[200:203], v240 offset:4096\n"
    "v_mfma_f32_16x16x32_bf16 v[80:83], v[212:215], v[224:227], v[80:83]\n"
    "s_waitcnt vmcnt(8)\n"
    "v_perm_b32 v219, v188, v184, s59\n"
    "v_perm_b32 v218, v180, v176, s59\n"
    "v_mfma_f32_16x16x32_bf16 v[92:95], v[212:215], v[228:231], v[92:95]\n"
    "v_perm_b32 v217, v172, v168, s59\n"
    "v_perm_b32 v216, v164, v160, s59\n"
    "s_add_u32 s52, s52, s53\n"
    "buffer_load_dwordx4 v[128:131], v232, s[28:31], s52 offen\n"
    "v_mfma_f32_16x16x32_bf16 v[88:91], v[208:211], v[228:231], v[88:91]\n"
    "buffer_load_dwordx4 v[132:135], v233, s[28:31], s52 offen\n"
    "v_mfma_f32_16x16x32_bf16 v[76:79], v[208:211], v[224:227], v[76:79]\n"
    "v_perm_b32 v220, v164, v160, s60\n"
    "v_perm_b32 v221, v172, v168, s60\n"
    "v_mfma_f32_16x16x32_bf16 v[72:75], v[204:207], v[224:227], v[72:75]\n"
    "v_perm_b32 v222, v180, v176, s60\n"
    "v_perm_b32 v223, v188, v184, s60\n"
    "buffer_load_dwordx4 v[136:139], v234, s[28:31], s52 offen\n"
    "v_mfma_f32_16x16x32_bf16 v[84:87], v[204:207], v[228:231], v[84:87]\n"
    "buffer_load_dwordx4 v[140:143], v235, s[28:31], s52 offen\n"
    "s_waitcnt lgkmcnt(0)\n"
    "v_mfma_f32_16x16x32_bf16 v[0:3], v[192:195], v[216:219], v[0:3]\n"
    "v_perm_b32 v224, v165, v161, s59\n"
    "v_perm_b32 v225, v173, v169, s59\n"
    "v_mfma_f32_16x16x32_bf16 v[12:15], v[192:195], v[220:223], v[12:15]\n"
    "v_perm_b32 v226, v181, v177, s59\n"
    "v_perm_b32 v227, v189, v185, s59\n"
    "buffer_load_dwordx4 v[144:147], v236, s[28:31], s52 offen\n"
    "v_mfma_f32_16x16x32_bf16 v[16:19], v[196:199], v[220:223], v[16:19]\n"
    "buffer_load_dwordx4 v[148:151], v237, s[28:31], s52 offen\n"
    "v_mfma_f32_16x16x32_bf16 v[4:7], v[196:199], v[216:219], v[4:7]\n"
    "v_perm_b32 v228, v165, v161, s60\n"
    "v_perm_b32 v229, v173, v169, s60\n"
    "v_mfma_f32_16x16x32_bf16 v[8:11], v[200:203], v[216:219], v[8:11]\n"
    "v_perm_b32 v230, v181, v177, s60\n"
    "v_perm_b32 v231, v189, v185, s60\n"
    "buffer_load_dwordx4 v[152:155], v238, s[28:31], s52 offen\n"
    "v_mfma_f32_16x16x32_bf16 v[20:23], v[200:203], v[220:223], v[20:23]\n"
    "buffer_load_dwordx4 v[156:159], v239, s[28:31], s52 offen\n"
    "v_mfma_f32_16x16x32_bf16 v[32:35], v[200:203], v[224:227], v[32:35]\n"
    "v_perm_b32 v216, v166, v162, s59\n"
    "v_perm_b32 v217, v174, v170, s59\n"
    "v_mfma_f32_16x16x32_bf16 v[44:47], v[200:203], v[228:231], v[44:47]\n"
    "v_perm_b32 v218, v182, v178, s59\n"
    "v_perm_b32 v219, v190, v186, s59\n"
    "v_mfma_f32_16x16x32_bf16 v[40:43], v[196:199], v[228:231], v[40:43]\n"
    "v_add_u32 v241, s54, v241\n"
    "v_mfma_f32_16x16x32_bf16 v[28:31], v[196:199], v[224:227], v[28:31]\n"
    "v_perm_b32 v220, v166, v162, s60\n"
    "v_perm_b32 v221, v174, v170, s60\n"
    "v_mfma_f32_16x16x32_bf16 v[24:27], v[192:195], v[224:227], v[24:27]\n"
    "v_perm_b32 v222, v182, v178, s60\n"
    "v_perm_b32 v223, v190, v186, s60\n"
    "v_mfma_f32_16x16x32_bf16 v[36:39], v[192:195], v[228:231], v[36:39]\n"
    "ds_read_b128 v[204:207], v241 offset:0\n"
    "v_mfma_f32_16x16x32_bf16 v[48:51], v[192:195], v[216:219], v[48:51]\n"
    "v_perm_b32 v224, v167, v163, s59\n"
    "v_perm_b32 v225, v175, v171, s59\n"
    "v_mfma_f32_16x16x32_bf16 v[60:63], v[192:195], v[220:223], v[60:63]\n"
    "v_perm_b32 v226, v183, v179, s59\n"
    "v_perm_b32 v227, v191, v187, s59\n"
    "v_mfma_f32_16x16x32_bf16 v[64:67], v[196:199], v[220:223], v[64:67]\n"
    "ds_read_b128 v[208:211], v241 offset:2048\n"
    "v_mfma_f32_16x16x32_bf16 v[52:55], v[196:199], v[216:219], v[52:55]\n"
    "v_perm_b32 v228, v167, v163, s60\n"
    "v_perm_b32 v229, v175, v171, s60\n"
    "v_mfma_f32_16x16x32_bf16 v[56:59], v[200:203], v[216:219], v[56:59]\n"
    "v_perm_b32 v230, v183, v179, s60\n"
    "v_perm_b32 v231, v191, v187, s60\n"
    "v_mfma_f32_16x16x32_bf16 v[68:71], v[200:203], v[220:223], v[68:71]\n"
    "ds_read_b128 v[212:215], v241 offset:4096\n"
    "v_mfma_f32_16x16x32_bf16 v[80:83], v[200:203], v[224:227], v[80:83]\n"
    "s_waitcnt vmcnt(8)\n"
    "v_perm_b32 v219, v124, v120, s59\n"
    "v_perm_b32 v218, v116, v112, s59\n"
    "v_mfma_f32_16x16x32_bf16 v[92:95], v[200:203], v[228:231], v[92:95]\n"
    "v_perm_b32 v217, v108, v104, s59\n"
    "v_perm_b32 v216, v100, v96, s59\n"
    "s_add_u32 s52, s52, s53\n"
    "buffer_load_dwordx4 v[160:163], v232, s[28:31], s52 offen\n"
    "v_mfma_f32_16x16x32_bf16 v[88:91], v[196:199], v[228:231], v[88:91]\n"
    "buffer_load_dwordx4 v[164:167], v233, s[28:31], s52 offen\n"
    "v_mfma_f32_16x16x32_bf16 v[76:79], v[196:199], v[224:227], v[76:79]\n"
    "v_perm_b32 v220, v100, v96, s60\n"
    "v_perm_b32 v221, v108, v104, s60\n"
    "v_mfma_f32_16x16x32_bf16 v[72:75], v[192:195], v[224:227], v[72:75]\n"
    "v_perm_b32 v222, v116, v112, s60\n"
    "v_perm_b32 v223, v124, v120, s60\n"
    "buffer_load_dwordx4 v[168:171], v234, s[28:31], s52 offen\n"
    "v_mfma_f32_16x16x32_bf16 v[84:87], v[192:195], v[228:231], v[84:87]\n"
    "buffer_load_dwordx4 v[172:175], v235, s[28:31], s52 offen\n"
    "s_waitcnt lgkmcnt(0)\n"
    "v_mfma_f32_16x16x32_bf16 v[0:3], v[204:207], v[216:219], v[0:3]\n"
    "v_perm_b32 v224, v101, v97, s59\n"
    "v_perm_b32 v225, v109, v105, s59\n"
    "v_mfma_f32_16x16x32_bf16 v[12:15], v[204:207], v[220:223], v[12:15]\n"
    "v_perm_b32 v226, v117, v113, s59\n"
    "v_perm_b32 v227, v125, v121, s59\n"
    "buffer_load_dwordx4 v[176:179], v236, s[28:31], s52 offen\n"
    "v_mfma_f32_16x16x32_bf16 v[16:19], v[208:211], v[220:223], v[16:19]\n"
    "buffer_load_dwordx4 v[180:183], v237, s[28:31], s52 offen\n"
    "v_mfma_f32_16x16x32_bf16 v[4:7], v[208:211], v[216:219], v[4:7]\n"
    "v_perm_b32 v228, v101, v97, s60\n"
    "v_perm_b32 v229, v109, v105, s60\n"
    "v_mfma_f32_16x16x32_bf16 v[8:11], v[212:215], v[216:219], v[8:11]\n"
    "v_perm_b32 v230, v117, v113, s60\n"
    "v_perm_b32 v231, v125, v121, s60\n"
    "buffer_load_dwordx4 v[184:187], v238, s[28:31], s52 offen\n"
    "v_mfma_f32_16x16x32_bf16 v[20:23], v[212:215], v[220:223], v[20:23]\n"
    "buffer_load_dwordx4 v[188:191], v239, s[28:31], s52 offen\n"
    "v_mfma_f32_16x16x32_bf16 v[32:35], v[212:215], v[224:227], v[32:35]\n"
    "v_perm_b32 v216, v102, v98, s59\n"
    "v_perm_b32 v217, v110, v106, s59\n"
    "v_mfma_f32_16x16x32_bf16 v[44:47], v[212:215], v[228:231], v[44:47]\n"
    "v_perm_b32 v218, v118, v114, s59\n"
    "v_perm_b32 v219, v126, v122, s59\n"
    "v_mfma_f32_16x16x32_bf16 v[40:43], v[208:211], v[228:231], v[40:43]\n"
    "v_mfma_f32_16x16x32_bf16 v[28:31], v[208:211], v[224:227], v[28:31]\n"
    "v_perm_b32 v220, v102, v98, s60\n"
    "v_perm_b32 v221, v110, v106, s60\n"
    "v_mfma_f32_16x16x32_bf16 v[24:27], v[204:207], v[224:227], v[24:27]\n"
    "v_perm_b32 v222, v118, v114, s60\n"
    "v_perm_b32 v223, v126, v122, s60\n"
    "v_mfma_f32_16x16x32_bf16 v[36:39], v[204:207], v[228:231], v[36:39]\n"
    "ds_read_b128 v[192:195], v240 offset:6144\n"
    "v_mfma_f32_16x16x32_bf16 v[48:51], v[204:207], v[216:219], v[48:51]\n"
    "v_perm_b32 v224, v103, v99, s59\n"
    "v_perm_b32 v225, v111, v107, s59\n"
    "v_mfma_f32_16x16x32_bf16 v[60:63], v[204:207], v[220:223], v[60:63]\n"
    "v_perm_b32 v226, v119, v115, s59\n"
    "v_perm_b32 v227, v127, v123, s59\n"
    "v_mfma_f32_16x16x32_bf16 v[64:67], v[208:211], v[220:223], v[64:67]\n"
    "ds_read_b128 v[196:199], v240 offset:8192\n"
    "v_mfma_f32_16x16x32_bf16 v[52:55], v[208:211], v[216:219], v[52:55]\n"
    "v_perm_b32 v228, v103, v99, s60\n"
    "v_perm_b32 v229, v111, v107, s60\n"
    "v_mfma_f32_16x16x32_bf16 v[56:59], v[212:215], v[216:219], v[56:59]\n"
    "v_perm_b32 v230, v119, v115, s60\n"
    "v_perm_b32 v231, v127, v123, s60\n"
    "v_mfma_f32_16x16x32_bf16 v[68:71], v[212:215], v[220:223], v[68:71]\n"
    "ds_read_b128 v[200:203], v240 offset:10240\n"
    "v_mfma_f32_16x16x32_bf16 v[80:83], v[212:215], v[224:227], v[80:83]\n"
    "s_waitcnt vmcnt(8)\n"
    "v_perm_b32 v219, v156, v152, s59\n"
    "v_perm_b32 v218, v148, v144, s59\n"
    "v_mfma_f32_16x16x32_bf16 v[92:95], v[212:215], v[228:231], v[92:95]\n"
    "v_perm_b32 v217, v140, v136, s59\n"
    "v_perm_b32 v216, v132, v128, s59\n"
    "s_add_u32 s52, s52, s53\n"
    "buffer_load_dwordx4 v[96:99], v232, s[28:31], s52 offen\n"
    "v_mfma_f32_16x16x32_bf16 v[88:91], v[208:211], v[228:231], v[88:91]\n"
    "buffer_load_dwordx4 v[100:103], v233, s[28:31], s52 offen\n"
    "v_mfma_f32_16x16x32_bf16 v[76:79], v[208:211], v[224:227], v[76:79]\n"
    "v_perm_b32 v220, v132, v128, s60\n"
    "v_perm_b32 v221, v140, v136, s60\n"
    "v_mfma_f32_16x16x32_bf16 v[72:75], v[204:207], v[224:227], v[72:75]\n"
    "v_perm_b32 v222, v148, v144, s60\n"
    "v_perm_b32 v223, v156, v152, s60\n"
    "buffer_load_dwordx4 v[104:107], v234, s[28:31], s52 offen\n"
    "v_mfma_f32_16x16x32_bf16 v[84:87], v[204:207], v[228:231], v[84:87]\n"
    "buffer_load_dwordx4 v[108:111], v235, s[28:31], s52 offen\n"
    "s_waitcnt lgkmcnt(0)\n"
    "v_mfma_f32_16x16x32_bf16 v[0:3], v[192:195], v[216:219], v[0:3]\n"
    "v_perm_b32 v224, v133, v129, s59\n"
    "v_perm_b32 v225, v141, v137, s59\n"
    "v_mfma_f32_16x16x32_bf16 v[12:15], v[192:195], v[220:223], v[12:15]\n"
    "v_perm_b32 v226, v149, v145, s59\n"
    "v_perm_b32 v227, v157, v153, s59\n"
    "buffer_load_dwordx4 v[112:115], v236, s[28:31], s52 offen\n"
    "v_mfma_f32_16x16x32_bf16 v[16:19], v[196:199], v[220:223], v[16:19]\n"
    "buffer_load_dwordx4 v[116:119], v237, s[28:31], s52 offen\n"
    "v_mfma_f32_16x16x32_bf16 v[4:7], v[196:199], v[216:219], v[4:7]\n"
    "v_perm_b32 v228, v133, v129, s60\n"
    "v_perm_b32 v229, v141, v137, s60\n"
    "v_mfma_f32_16x16x32_bf16 v[8:11], v[200:203], v[216:219], v[8:11]\n"
    "v_perm_b32 v230, v149, v145, s60\n"
    "v_perm_b32 v231, v157, v153, s60\n"
    "buffer_load_dwordx4 v[120:123], v238, s[28:31], s52 offen\n"
    "v_mfma_f32_16x16x32_bf16 v[20:23], v[200:203], v[220:223], v[20:23]\n"
    "buffer_load_dwordx4 v[124:127], v239, s[28:31], s52 offen\n"
    "v_mfma_f32_16x16x32_bf16 v[32:35], v[200:203], v[224:227], v[32:35]\n"
    "v_perm_b32 v216, v134, v130, s59\n"
    "v_perm_b32 v217, v142, v138, s59\n"
    "v_mfma_f32_16x16x32_bf16 v[44:47], v[200:203], v[228:231], v[44:47]\n"
    "v_perm_b32 v218, v150, v146, s59\n"
    "v_perm_b32 v219, v158, v154, s59\n"
    "v_mfma_f32_16x16x32_bf16 v[40:43], v[196:199], v[228:231], v[40:43]\n"
    "v_mfma_f32_16x16x32_bf16 v[28:31], v[196:199], v[224:227], v[28:31]\n"
    "v_perm_b32 v220, v134, v130, s60\n"
    "v_perm_b32 v221, v142, v138, s60\n"
    "v_mfma_f32_16x16x32_bf16 v[24:27], v[192:195], v[224:227], v[24:27]\n"
    "v_perm_b32 v222, v150, v146, s60\n"
    "v_perm_b32 v223, v158, v154, s60\n"
    "v_mfma_f32_16x16x32_bf16 v[36:39], v[192:195], v[228:231], v[36:39]\n"
    "ds_read_b128 v[204:207], v241 offset:6144\n"
    "v_mfma_f32_16x16x32_bf16 v[48:51], v[192:195], v[216:219], v[48:51]\n"
    "v_perm_b32 v224, v135, v131, s59\n"
    "v_perm_b32 v225, v143, v139, s59\n"
    "v_mfma_f32_16x16x32_bf16 v[60:63], v[192:195], v[220:223], v[60:63]\n"
    "v_perm_b32 v226, v151, v147, s59\n"
    "v_perm_b32 v227, v159, v155, s59\n"
    "v_mfma_f32_16x16x32_bf16 v[64:67], v[196:199], v[220:223], v[64:67]\n"
    "ds_read_b128 v[208:211], v241 offset:8192\n"
    "v_mfma_f32_16x16x32_bf16 v[52:55], v[196:199], v[216:219], v[52:55]\n"
    "v_perm_b32 v228, v135, v131, s60\n"
    "v_perm_b32 v229, v143, v139, s60\n"
    "v_mfma_f32_16x16x32_bf16 v[56:59], v[200:203], v[216:219], v[56:59]\n"
    "v_perm_b32 v230, v151, v147, s60\n"
    "v_perm_b32 v231, v159, v155, s60\n"
    "v_mfma_f32_16x16x32_bf16 v[68:71], v[200:203], v[220:223], v[68:71]\n"
    "ds_read_b128 v[212:215], v241 offset:10240\n"
    "v_mfma_f32_16x16x32_bf16 v[80:83], v[200:203], v[224:227], v[80:83]\n"
    "s_waitcnt vmcnt(8)\n"
    "v_perm_b32 v219, v188, v184, s59\n"
    "v_perm_b32 v218, v180, v176, s59\n"
    "v_mfma_f32_16x16x32_bf16 v[92:95], v[200:203], v[228:231], v[92:95]\n"
    "v_perm_b32 v217, v172, v168, s59\n"
    "v_perm_b32 v216, v164, v160, s59\n"
    "s_add_u32 s52, s52, s53\n"
    "buffer_load_dwordx4 v[128:131], v232, s[28:31], s52 offen\n"
    "v_mfma_f32_16x16x32_bf16 v[88:91], v[196:199], v[228:231], v[88:91]\n"
    "buffer_load_dwordx4 v[132:135], v233, s[28:31], s52 offen\n"
    "v_mfma_f32_16x16x32_bf16 v[76:79], v[196:199], v[224:227], v[76:79]\n"
    "v_perm_b32 v220, v164, v160, s60\n"
    "v_perm_b32 v221, v172, v168, s60\n"
    "v_mfma_f32_16x16x32_bf16 v[72:75], v[192:195], v[224:227], v[72:75]\n"
    "v_perm_b32 v222, v180, v176, s60\n"
    "v_perm_b32 v223, v188, v184, s60\n"
    "buffer_load_dwordx4 v[136:139], v234, s[28:31], s52 offen\n"
    "v_mfma_f32_16x16x32_bf16 v[84:87], v[192:195], v[228:231], v[84:87]\n"
    "buffer_load_dwordx4 v[140:143], v235, s[28:31], s52 offen\n"
    "s_waitcnt lgkmcnt(0)\n"
    "v_mfma_f32_16x16x32_bf16 v[0:3], v[204:207], v[216:219], v[0:3]\n"
    "v_perm_b32 v224, v165, v161, s59\n"
    "v_perm_b32 v225, v173, v169, s59\n"
    "v_mfma_f32_16x16x32_bf16 v[12:15], v[204:207], v[220:223], v[12:15]\n"
    "v_perm_b32 v226, v181, v177, s59\n"
    "v_perm_b32 v227, v189, v185, s59\n"
    "buffer_load_dwordx4 v[144:147], v236, s[28:31], s52 offen\n"
    "v_mfma_f32_16x16x32_bf16 v[16:19], v[208:211], v[220:223], v[16:19]\n"
    "buffer_load_dwordx4 v[148:151], v237, s[28:31], s52 offen\n"
    "s_mov_b64 exec, 1\n"
    "ds_add_u32 v253, v252 offset:20\n"
    "s_mov_b64 exec, -1\n"
    "v_mfma_f32_16x16x32_bf16 v[4:7], v[208:211], v[216:219], v[4:7]\n"
    "v_perm_b32 v228, v165, v161, s60\n"
    "v_perm_b32 v229, v173, v169, s60\n"
    "v_mfma_f32_16x16x32_bf16 v[8:11], v[212:215], v[216:219], v[8:11]\n"
    "v_perm_b32 v230, v181, v177, s60\n"
    "v_perm_b32 v231, v189, v185, s60\n"
    "buffer_load_dwordx4 v[152:155], v238, s[28:31], s52 offen\n"
    "v_mfma_f32_16x16x32_bf16 v[20:23], v[212:215], v[220:223], v[20:23]\n"
    "buffer_load_dwordx4 v[156:159], v239, s[28:31], s52 offen\n"
    "v_mfma_f32_16x16x32_bf16 v[32:35], v[212:215], v[224:227], v[32:35]\n"
    "v_perm_b32 v216, v166, v162, s59\n"
    "v_perm_b32 v217, v174, v170, s59\n"
    "v_mfma_f32_16x16x32_bf16 v[44:47], v[212:215], v[228:231], v[44:47]\n"
    "v_perm_b32 v218, v182, v178, s59\n"
    "v_perm_b32 v219, v190, v186, s59\n"
    "v_mfma_f32_16x16x32_bf16 v[40:43], v[208:211], v[228:231], v[40:43]\n"
    "v_add_u32 v240, s54, v240\n"
    "v_mfma_f32_16x16x32_bf16 v[28:31], v[208:211], v[224:227], v[28:31]\n"
    "v_perm_b32 v220, v166, v162, s60\n"
    "v_perm_b32 v221, v174, v170, s60\n"
    "v_mfma_f32_16x16x32_bf16 v[24:27], v[204:207], v[224:227], v[24:27]\n"
    "v_perm_b32 v222, v182, v178, s60\n"
    "v_perm_b32 v223, v190, v186, s60\n"
    "v_mfma_f32_16x16x32_bf16 v[36:39], v[204:207], v[228:231], v[36:39]\n"
    "ds_read_b128 v[192:195], v240 offset:0\n"
    "v_mfma_f32_16x16x32_bf16 v[48:51], v[204:207], v[216:219], v[48:51]\n"
    "v_perm_b32 v224, v167, v163, s59\n"
    "v_perm_b32 v225, v175, v171, s59\n"
    "v_mfma_f32_16x16x32_bf16 v[60:63], v[204:207], v[220:223], v[60:63]\n"
    "v_perm_b32 v226, v183, v179, s59\n"
    "v_perm_b32 v227, v191, v187, s59\n"
    "v_mfma_f32_16x16x32_bf16 v[64:67], v[208:211], v[220:223], v[64:67]\n"
    "ds_read_b128 v[196:199], v240 offset:2048\n"
    "v_mfma_f32_16x16x32_bf16 v[52:55], v[208:211], v[216:219], v[52:55]\n"
    "v_perm_b32 v228, v167, v163, s60\n"
    "v_perm_b32 v229, v175, v171, s60\n"
    "v_mfma_f32_16x16x32_bf16 v[56:59], v[212:215], v[216:219], v[56:59]\n"
    "v_perm_b32 v230, v183, v179, s60\n"
    "v_perm_b32 v231, v191, v187, s60\n"
    "v_mfma_f32_16x16x32_bf16 v[68:71], v[212:215], v[220:223], v[68:71]\n"
    "ds_read_b128 v[200:203], v240 offset:4096\n"
    "v_mfma_f32_16x16x32_bf16 v[80:83], v[212:215], v[224:227], v[80:83]\n"
    "s_waitcnt vmcnt(8)\n"
    "v_perm_b32 v219, v124, v120, s59\n"
    "v_perm_b32 v218, v116, v112, s59\n"
    "v_mfma_f32_16x16x32_bf16 v[92:95], v[212:215], v[228:231], v[92:95]\n"
    "v_perm_b32 v217, v108, v104, s59\n"
    "v_perm_b32 v216, v100, v96, s59\n"
    "s_add_u32 s52, s52, s53\n"
    "buffer_load_dwordx4 v[160:163], v232, s[28:31], s52 offen\n"
    "v_mfma_f32_16x16x32_bf16 v[88:91], v[208:211], v[228:231], v[88:91]\n"
    "buffer_load_dwordx4 v[164:167], v233, s[28:31], s52 offen\n"
    "v_mfma_f32_16x16x32_bf16 v[76:79], v[208:211], v[224:227], v[76:79]\n"
    "v_perm_b32 v220, v100, v96, s60\n"
    "v_perm_b32 v221, v108, v104, s60\n"
    "v_mfma_f32_16x16x32_bf16 v[72:75], v[204:207], v[224:227], v[72:75]\n"
    "v_perm_b32 v222, v116, v112, s60\n"
    "v_perm_b32 v223, v124, v120, s60\n"
    "buffer_load_dwordx4 v[168:171], v234, s[28:31], s52 offen\n"
    "v_mfma_f32_16x16x32_bf16 v[84:87], v[204:207], v[228:231], v[84:87]\n"
    "buffer_load_dwordx4 v[172:175], v235, s[28:31], s52 offen\n"
    "s_waitcnt lgkmcnt(0)\n"
    "v_mfma_f32_16x16x32_bf16 v[0:3], v[192:195], v[216:219], v[0:3]\n"
    "v_perm_b32 v224, v101, v97, s59\n"
    "v_perm_b32 v225, v109, v105, s59\n"
    "v_mfma_f32_16x16x32_bf16 v[12:15], v[192:195], v[220:223], v[12:15]\n"
    "v_perm_b32 v226, v117, v113, s59\n"
    "v_perm_b32 v227, v125, v121, s59\n"
    "buffer_load_dwordx4 v[176:179], v236, s[28:31], s52 offen\n"
    "v_mfma_f32_16x16x32_bf16 v[16:19], v[196:199], v[220:223], v[16:19]\n"
    "buffer_load_dwordx4 v[180:183], v237, s[28:31], s52 offen\n"
    "v_mfma_f32_16x16x32_bf16 v[4:7], v[196:199], v[216:219], v[4:7]\n"
    "v_perm_b32 v228, v101, v97, s60\n"
    "v_perm_b32 v229, v109, v105, s60\n"
    "v_mfma_f32_16x16x32_bf16 v[8:11], v[200:203], v[216:219], v[8:11]\n"
    "v_perm_b32 v230, v117, v113, s60\n"
    "v_perm_b32 v231, v125, v121, s60\n"
    "buffer_load_dwordx4 v[184:187], v238, s[28:31], s52 offen\n"
    "v_mfma_f32_16x16x32_bf16 v[20:23], v[200:203], v[220:223], v[20:23]\n"
    "buffer_load_dwordx4 v[188:191], v239, s[28:31], s52 offen\n"
    "v_mfma_f32_16x16x32_bf16 v[32:35], v[200:203], v[224:227], v[32:35]\n"
    "v_perm_b32 v216, v102, v98, s59\n"
    "v_perm_b32 v217, v110, v106, s59\n"
    "v_mfma_f32_16x16x32_bf16 v[44:47], v[200:203], v[228:231], v[44:47]\n"
    "v_perm_b32 v218, v118, v114, s59\n"
    "v_perm_b32 v219, v126, v122, s59\n"
    "v_mfma_f32_16x16x32_bf16 v[40:43], v[196:199], v[228:231], v[40:43]\n"
    "v_add_u32 v241, s54, v241\n"
    "v_mfma_f32_16x16x32_bf16 v[28:31], v[196:199], v[224:227], v[28:31]\n"
    "v_perm_b32 v220, v102, v98, s60\n"
    "v_perm_b32 v221, v110, v106, s60\n"
    "v_mfma_f32_16x16x32_bf16 v[24:27], v[192:195], v[224:227], v[24:27]\n"
    "v_perm_b32 v222, v118, v114, s60\n"
    "v_perm_b32 v223, v126, v122, s60\n"
    "v_mfma_f32_16x16x32_bf16 v[36:39], v[192:195], v[228:231], v[36:39]\n"
    "ds_read_b128 v[204:207], v241 offset:0\n"
    "v_mfma_f32_16x16x32_bf16 v[48:51], v[192:195], v[216:219], v[48:51]\n"
    "v_perm_b32 v224, v103, v99, s59\n"
    "v_perm_b32 v225, v111, v107, s59\n"
    "v_mfma_f32_16x16x32_bf16 v[60:63], v[192:195], v[220:223], v[60:63]\n"
    "v_perm_b32 v226, v119, v115, s59\n"
    "v_perm_b32 v227, v127, v123, s59\n"
    "v_mfma_f32_16x16x32_bf16 v[64:67], v[196:199], v[220:223], v[64:67]\n"
    "ds_read_b128 v[208:211], v241 offset:2048\n"
    "v_mfma_f32_16x16x32_bf16 v[52:55], v[196:199], v[216:219], v[52:55]\n"
    "v_perm_b32 v228, v103, v99, s60\n"
    "v_perm_b32 v229, v111, v107, s60\n"
    "v_mfma_f32_16x16x32_bf16 v[56:59], v[200:203], v[216:219], v[56:59]\n"
    "v_perm_b32 v230, v119, v115, s60\n"
    "v_perm_b32 v231, v127, v123, s60\n"
    "v_mfma_f32_16x16x32_bf16 v[68:71], v[200:203], v[220:223], v[68:71]\n"
    "ds_read_b128 v[212:215], v241 offset:4096\n"
    "v_mfma_f32_16x16x32_bf16 v[80:83], v[200:203], v[224:227], v[80:83]\n"
    "s_waitcnt vmcnt(8)\n"
    "v_perm_b32 v219, v156, v152, s59\n"
    "v_perm_b32 v218, v148, v144, s59\n"
    "v_mfma_f32_16x16x32_bf16 v[92:95], v[200:203], v[228:231], v[92:95]\n"
    "v_perm_b32 v217, v140, v136, s59\n"
    "v_perm_b32 v216, v132, v128, s59\n"
    "s_add_u32 s52, s52, s53\n"
    "buffer_load_dwordx4 v[96:99], v232, s[28:31], s52 offen\n"
    "v_mfma_f32_16x16x32_bf16 v[88:91], v[196:199], v[228:231], v[88:91]\n"
    "buffer_load_dwordx4 v[100:103], v233, s[28:31], s52 offen\n"
    "v_mfma_f32_16x16x32_bf16 v[76:79], v[196:199], v[224:227], v[76:79]\n"
    "v_perm_b32 v220, v132, v128, s60\n"
    "v_perm_b32 v221, v140, v136, s60\n"
    "v_mfma_f32_16x16x32_bf16 v[72:75], v[192:195], v[224:227], v[72:75]\n"
    "v_perm_b32 v222, v148, v144, s60\n"
    "v_perm_b32 v223, v156, v152, s60\n"
    "buffer_load_dwordx4 v[104:107], v234, s[28:31], s52 offen\n"
    "v_mfma_f32_16x16x32_bf16 v[84:87], v[192:195], v[228:231], v[84:87]\n"
    "buffer_load_dwordx4 v[108:111], v235, s[28:31], s52 offen\n"
    "s_waitcnt lgkmcnt(0)\n"
    "v_mfma_f32_16x16x32_bf16 v[0:3], v[204:207], v[216:219], v[0:3]\n"
    "v_perm_b32 v224, v133, v129, s59\n"
    "v_perm_b32 v225, v141, v137, s59\n"
    "v_mfma_f32_16x16x32_bf16 v[12:15], v[204:207], v[220:223], v[12:15]\n"
    "v_perm_b32 v226, v149, v145, s59\n"
    "v_perm_b32 v227, v157, v153, s59\n"
    "buffer_load_dwordx4 v[112:115], v236, s[28:31], s52 offen\n"
    "v_mfma_f32_16x16x32_bf16 v[16:19], v[208:211], v[220:223], v[16:19]\n"
    "buffer_load_dwordx4 v[116:119], v237, s[28:31], s52 offen\n"
    "v_mfma_f32_16x16x32_bf16 v[4:7], v[208:211], v[216:219], v[4:7]\n"
    "v_perm_b32 v228, v133, v129, s60\n"
    "v_perm_b32 v229, v141, v137, s60\n"
    "v_mfma_f32_16x16x32_bf16 v[8:11], v[212:215], v[216:219], v[8:11]\n"
    "v_perm_b32 v230, v149, v145, s60\n"
    "v_perm_b32 v231, v157, v153, s60\n"
    "buffer_load_dwordx4 v[120:123], v238, s[28:31], s52 offen\n"
    "v_mfma_f32_16x16x32_bf16 v[20:23], v[212:215], v[220:223], v[20:23]\n"
    "buffer_load_dwordx4 v[124:127], v239, s[28:31], s52 offen\n"
    "v_mfma_f32_16x16x32_bf16 v[32:35], v[212:215], v[224:227], v[32:35]\n"
    "v_perm_b32 v216, v134, v130, s59\n"
    "v_perm_b32 v217, v142, v138, s59\n"
    "v_mfma_f32_16x16x32_bf16 v[44:47], v[212:215], v[228:231], v[44:47]\n"
    "v_perm_b32 v218, v150, v146, s59\n"
    "v_perm_b32 v219, v158, v154, s59\n"
    "v_mfma_f32_16x16x32_bf16 v[40:43], v[208:211], v[228:231], v[40:43]\n"
    "v_mfma_f32_16x16x32_bf16 v[28:31], v[208:211], v[224:227], v[28:31]\n"
    "v_perm_b32 v220, v134, v130, s60\n"
    "v_perm_b32 v221, v142, v138, s60\n"
    "v_mfma_f32_16x16x32_bf16 v[24:27], v[204:207], v[224:227], v[24:27]\n"
    "v_perm_b32 v222, v150, v146, s60\n"
    "v_perm_b32 v223, v158, v154, s60\n"
    "v_mfma_f32_16x16x32_bf16 v[36:39], v[204:207], v[228:231], v[36:39]\n"
    "ds_read_b128 v[192:195], v240 offset:6144\n"
    "v_mfma_f32_16x16x32_bf16 v[48:51], v[204:207], v[216:219], v[48:51]\n"
    "v_perm_b32 v224, v135, v131, s59\n"
    "v_perm_b32 v225, v143, v139, s59\n"
    "v_mfma_f32_16x16x32_bf16 v[60:63], v[204:207], v[220:223], v[60:63]\n"
    "v_perm_b32 v226, v151, v147, s59\n"
    "v_perm_b32 v227, v159, v155, s59\n"
    "v_mfma_f32_16x16x32_bf16 v[64:67], v[208:211], v[220:223], v[64:67]\n"
    "ds_read_b128 v[196:199], v240 offset:8192\n"
    "v_mfma_f32_16x16x32_bf16 v[52:55], v[208:211], v[216:219], v[52:55]\n"
    "v_perm_b32 v228, v135, v131, s60\n"
    "v_perm_b32 v229, v143, v139, s60\n"
    "v_mfma_f32_16x16x32_bf16 v[56:59], v[212:215], v[216:219], v[56:59]\n"
    "v_perm_b32 v230, v151, v147, s60\n"
    "v_perm_b32 v231, v159, v155, s60\n"
    "v_mfma_f32_16x16x32_bf16 v[68:71], v[212:215], v[220:223], v[68:71]\n"
    "ds_read_b128 v[200:203], v240 offset:10240\n"
    "v_mfma_f32_16x16x32_bf16 v[80:83], v[212:215], v[224:227], v[80:83]\n"
    "s_waitcnt vmcnt(8)\n"
    "v_perm_b32 v219, v188, v184, s59\n"
    "v_perm_b32 v218, v180, v176, s59\n"
    "v_mfma_f32_16x16x32_bf16 v[92:95], v[212:215], v[228:231], v[92:95]\n"
    "v_perm_b32 v217, v172, v168, s59\n"
    "v_perm_b32 v216, v164, v160, s59\n"
    "v_mfma_f32_16x16x32_bf16 v[88:91], v[208:211], v[228:231], v[88:91]\n"
    "s_cmp_eq_u32 s74, 0\n"
    "s_cbranch_scc1 .Lch_85\n"
    "s_mov_b32 s65, 32768\n"
    "buffer_load_dwordx4 v[128:131], v232, s[32:35], s65 offen\n"
    "buffer_load_dwordx4 v[132:135], v233, s[32:35], s65 offen\n"
    "buffer_load_dwordx4 v[136:139], v234, s[32:35], s65 offen\n"
    "buffer_load_dwordx4 v[140:143], v235, s[32:35], s65 offen\n"
    "buffer_load_dwordx4 v[144:147], v236, s[32:35], s65 offen\n"
    "buffer_load_dwordx4 v[148:151], v237, s[32:35], s65 offen\n"
    "buffer_load_dwordx4 v[152:155], v238, s[32:35], s65 offen\n"
    "buffer_load_dwordx4 v[156:159], v239, s[32:35], s65 offen\n"
    ".Lch_85:\n"
    "v_mfma_f32_16x16x32_bf16 v[76:79], v[208:211], v[224:227], v[76:79]\n"
    "v_perm_b32 v220, v164, v160, s60\n"
    "v_perm_b32 v221, v172, v168, s60\n"
    "v_mfma_f32_16x16x32_bf16 v[72:75], v[204:207], v[224:227], v[72:75]\n"
    "v_perm_b32 v222, v180, v176, s60\n"
    "v_perm_b32 v223, v188, v184, s60\n"
    "v_mfma_f32_16x16x32_bf16 v[84:87], v[204:207], v[228:231], v[84:87]\n"
    "s_waitcnt lgkmcnt(0)\n"
    "v_mfma_f32_16x16x32_bf16 v[0:3], v[192:195], v[216:219], v[0:3]\n"
    "v_perm_b32 v224, v165, v161, s59\n"
    "v_perm_b32 v225, v173, v169, s59\n"
    "v_mfma_f32_16x16x32_bf16 v[12:15], v[192:195], v[220:223], v[12:15]\n"
    "v_perm_b32 v226, v181, v177, s59\n"
    "v_perm_b32 v227, v189, v185, s59\n"
    "v_mfma_f32_16x16x32_bf16 v[16:19], v[196:199], v[220:223], v[16:19]\n"
    "v_mfma_f32_16x16x32_bf16 v[4:7], v[196:199], v[216:219], v[4:7]\n"
    "v_perm_b32 v228, v165, v161, s60\n"
    "v_perm_b32 v229, v173, v169, s60\n"
    "v_mfma_f32_16x16x32_bf16 v[8:11], v[200:203], v[216:219], v[8:11]\n"
    "v_perm_b32 v230, v181, v177, s60\n"
    "v_perm_b32 v231, v189, v185, s60\n"
    "v_mfma_f32_16x16x32_bf16 v[20:23], v[200:203], v[220:223], v[20:23]\n"
    "v_mfma_f32_16x16x32_bf16 v[32:35], v[200:203], v[224:227], v[32:35]\n"
    "v_perm_b32 v216, v166, v162, s59\n"
    "v_perm_b32 v217, v174, v170, s59\n"
    "v_mfma_f32_16x16x32_bf16 v[44:47], v[200:203], v[228:231], v[44:47]\n"
    "v_perm_b32 v218, v182, v178, s59\n"
    "v_perm_b32 v219, v190, v186, s59\n"
    "v_mfma_f32_16x16x32_bf16 v[40:43], v[196:199], v[228:231], v[40:43]\n"
    "v_mfma_f32_16x16x32_bf16 v[28:31], v[196:199], v[224:227], v[28:31]\n"
    "v_perm_b32 v220, v166, v162, s60\n"
    "v_perm_b32 v221, v174, v170, s60\n"
    "v_mfma_f32_16x16x32_bf16 v[24:27], v[192:195], v[224:227], v[24:27]\n"
    "v_perm_b32 v222, v182, v178, s60\n"
    "v_perm_b32 v223, v190, v186, s60\n"
    "v_mfma_f32_16x16x32_bf16 v[36:39], v[192:195], v[228:231], v[36:39]\n"
    "ds_read_b128 v[204:207], v241 offset:6144\n"
    "v_mfma_f32_16x16x32_bf16 v[48:51], v[192:195], v[216:219], v[48:51]\n"
    "v_perm_b32 v224, v167, v163, s59\n"
    "v_perm_b32 v225, v175, v171, s59\n"
    "v_mfma_f32_16x16x32_bf16 v[60:63], v[192:195], v[220:223], v[60:63]\n"
    "v_perm_b32 v226, v183, v179, s59\n"
    "v_perm_b32 v227, v191, v187, s59\n"
    "v_mfma_f32_16x16x32_bf16 v[64:67], v[196:199], v[220:223], v[64:67]\n"
    "ds_read_b128 v[208:211], v241 offset:8192\n"
    "v_mfma_f32_16x16x32_bf16 v[52:55], v[196:199], v[216:219], v[52:55]\n"
    "v_perm_b32 v228, v167, v163, s60\n"
    "v_perm_b32 v229, v175, v171, s60\n"
    "v_mfma_f32_16x16x32_bf16 v[56:59], v[200:203], v[216:219], v[56:59]\n"
    "v_perm_b32 v230, v183, v179, s60\n"
    "v_perm_b32 v231, v191, v187, s60\n"
    "v_mfma_f32_16x16x32_bf16 v[68:71], v[200:203], v[220:223], v[68:71]\n"
    "ds_read_b128 v[212:215], v241 offset:10240\n"
    "v_mfma_f32_16x16x32_bf16 v[80:83], v[200:203], v[224:227], v[80:83]\n"
    "s_waitcnt vmcnt(0)\n"
    "v_perm_b32 v219, v124, v120, s59\n"
    "v_perm_b32 v218, v116, v112, s59\n"
    "v_mfma_f32_16x16x32_bf16 v[92:95], v[200:203], v[228:231], v[92:95]\n"
    "v_perm_b32 v217, v108, v104, s59\n"
    "v_perm_b32 v216, v100, v96, s59\n"
    "v_mfma_f32_16x16x32_bf16 v[88:91], v[196:199], v[228:231], v[88:91]\n"
    "s_cmp_eq_u32 s74, 0\n"
    "s_cbranch_scc1 .Lch_86\n"
    "s_mov_b32 s65, 65536\n"
    "buffer_load_dwordx4 v[160:163], v232, s[32:35], s65 offen\n"
    "buffer_load_dwordx4 v[164:167], v233, s[32:35], s65 offen\n"
    "buffer_load_dwordx4 v[168:171], v234, s[32:35], s65 offen\n"
    "buffer_load_dwordx4 v[172:175], v235, s[32:35], s65 offen\n"
    "buffer_load_dwordx4 v[176:179], v236, s[32:35], s65 offen\n"
    "buffer_load_dwordx4 v[180:183], v237, s[32:35], s65 offen\n"
    "buffer_load_dwordx4 v[184:187], v238, s[32:35], s65 offen\n"
    "buffer_load_dwordx4 v[188:191], v239, s[32:35], s65 offen\n"
    ".Lch_86:\n"
    "v_mfma_f32_16x16x32_bf16 v[76:79], v[196:199], v[224:227], v[76:79]\n"
    "v_perm_b32 v220, v100, v96, s60\n"
    "v_perm_b32 v221, v108, v104, s60\n"
    "v_mfma_f32_16x16x32_bf16 v[72:75], v[192:195], v[224:227], v[72:75]\n"
    "v_perm_b32 v222, v116, v112, s60\n"
    "v_perm_b32 v223, v124, v120, s60\n"
    "v_mfma_f32_16x16x32_bf16 v[84:87], v[192:195], v[228:231], v[84:87]\n"
    "s_waitcnt lgkmcnt(0)\n"
    "v_mfma_f32_16x16x32_bf16 v[0:3], v[204:207], v[216:219], v[0:3]\n"
    "v_perm_b32 v224, v101, v97, s59\n"
    "v_perm_b32 v225, v109, v105, s59\n"
    "v_mfma_f32_16x16x32_bf16 v[12:15], v[204:207], v[220:223], v[12:15]\n"
    "v_perm_b32 v226, v117, v113, s59\n"
    "v_perm_b32 v227, v125, v121, s59\n"
    "v_mfma_f32_16x16x32_bf16 v[16:19], v[208:211], v[220:223], v[16:19]\n"
    "v_mfma_f32_16x16x32_bf16 v[4:7], v[208:211], v[216:219], v[4:7]\n"
    "v_perm_b32 v228, v101, v97, s60\n"
    "v_perm_b32 v229, v109, v105, s60\n"
    "v_mfma_f32_16x16x32_bf16 v[8:11], v[212:215], v[216:219], v[8:11]\n"
    "v_perm_b32 v230, v117, v113, s60\n"
    "v_perm_b32 v231, v125, v121, s60\n"
    "v_mfma_f32_16x16x32_bf16 v[20:23], v[212:215], v[220:223], v[20:23]\n"
    "v_mfma_f32_16x16x32_bf16 v[32:35], v[212:215], v[224:227], v[32:35]\n"
    "v_perm_b32 v216, v102, v98, s59\n"
    "v_perm_b32 v217, v110, v106, s59\n"
    "v_mfma_f32_16x16x32_bf16 v[44:47], v[212:215], v[228:231], v[44:47]\n"
    "v_perm_b32 v218, v118, v114, s59\n"
    "v_perm_b32 v219, v126, v122, s59\n"
    "v_mfma_f32_16x16x32_bf16 v[40:43], v[208:211], v[228:231], v[40:43]\n"
    "v_mfma_f32_16x16x32_bf16 v[28:31], v[208:211], v[224:227], v[28:31]\n"
    "v_perm_b32 v220, v102, v98, s60\n"
    "v_perm_b32 v221, v110, v106, s60\n"
    "v_mfma_f32_16x16x32_bf16 v[24:27], v[204:207], v[224:227], v[24:27]\n"
    "v_perm_b32 v222, v118, v114, s60\n"
    "v_perm_b32 v223, v126, v122, s60\n"
    "v_mfma_f32_16x16x32_bf16 v[36:39], v[204:207], v[228:231], v[36:39]\n"
    "v_mfma_f32_16x16x32_bf16 v[48:51], v[204:207], v[216:219], v[48:51]\n"
    "v_perm_b32 v224, v103, v99, s59\n"
    "v_perm_b32 v225, v111, v107, s59\n"
    "v_mfma_f32_16x16x32_bf16 v[60:63], v[204:207], v[220:223], v[60:63]\n"
    "v_perm_b32 v226, v119, v115, s59\n"
    "v_perm_b32 v227, v127, v123, s59\n"
    "v_mfma_f32_16x16x32_bf16 v[64:67], v[208:211], v[220:223], v[64:67]\n"
    "v_mfma_f32_16x16x32_bf16 v[52:55], v[208:211], v[216:219], v[52:55]\n"
    "v_perm_b32 v228, v103, v99, s60\n"
    "v_perm_b32 v229, v111, v107, s60\n"
    "v_mfma_f32_16x16x32_bf16 v[56:59], v[212:215], v[216:219], v[56:59]\n"
    "v_perm_b32 v230, v119, v115, s60\n"
    "v_perm_b32 v231, v127, v123, s60\n"
    "v_mfma_f32_16x16x32_bf16 v[68:71], v[212:215], v[220:223], v[68:71]\n"
    "v_mfma_f32_16x16x32_bf16 v[80:83], v[212:215], v[224:227], v[80:83]\n"
    "v_mfma_f32_16x16x32_bf16 v[92:95], v[212:215], v[228:231], v[92:95]\n"
    "v_mfma_f32_16x16x32_bf16 v[88:91], v[208:211], v[228:231], v[88:91]\n"
    "s_cmp_eq_u32 s74, 0\n"
    "s_cbranch_scc1 .Lch_87\n"
    "s_mov_b32 s65, 0\n"
    "s_mov_b64 exec, 1\n"
    "ds_sub_u32 v253, v252 offset:8\n"
    "s_mov_b64 exec, -1\n"
    "buffer_load_dwordx4 v[96:99], v232, s[32:35], s65 offen\n"
    "buffer_load_dwordx4 v[100:103], v233, s[32:35], s65 offen\n"
    "buffer_load_dwordx4 v[104:107], v234, s[32:35], s65 offen\n"
    "buffer_load_dwordx4 v[108:111], v235, s[32:35], s65 offen\n"
    "buffer_load_dwordx4 v[112:115], v236, s[32:35], s65 offen\n"
    "buffer_load_dwordx4 v[116:119], v237, s[32:35], s65 offen\n"
    "buffer_load_dwordx4 v[120:123], v238, s[32:35], s65 offen\n"
    "buffer_load_dwordx4 v[124:127], v239, s[32:35], s65 offen\n"
    ".Lch_87:\n"
    "v_mfma_f32_16x16x32_bf16 v[76:79], v[208:211], v[224:227], v[76:79]\n"
    "v_mfma_f32_16x16x32_bf16 v[72:75], v[204:207], v[224:227], v[72:75]\n"
    "v_mfma_f32_16x16x32_bf16 v[84:87], v[204:207], v[228:231], v[84:87]\n"
    "v_lshlrev_b32 v240, 4, v255\n"
    "s_cmp_eq_u32 s56, 1\n"
    "s_cbranch_scc0 .Lstore\n"
    "v_cvt_pk_bf16_f32 v192, v0, v12\n"
    "v_cvt_pk_bf16_f32 v193, v24, v36\n"
    "v_cvt_pk_bf16_f32 v194, v48, v60\n"
    "s_nop 1\n"
    "v_cvt_pk_bf16_f32 v195, v72, v84\n"
    "v_cvt_pk_bf16_f32 v196, v1, v13\n"
    "v_cvt_pk_bf16_f32 v197, v25, v37\n"
    "v_cvt_pk_bf16_f32 v198, v49, v61\n"
    "v_cvt_pk_bf16_f32 v199, v73, v85\n"
    "v_cvt_pk_bf16_f32 v200, v2, v14\n"
    "v_cvt_pk_bf16_f32 v201, v26, v38\n"
    "v_cvt_pk_bf16_f32 v202, v50, v62\n"
    "v_cvt_pk_bf16_f32 v203, v74, v86\n"
    "v_cvt_pk_bf16_f32 v204, v3, v15\n"
    "v_cvt_pk_bf16_f32 v205, v27, v39\n"
    "v_cvt_pk_bf16_f32 v206, v51, v63\n"
    "v_cvt_pk_bf16_f32 v207, v75, v87\n"
    "v_cvt_pk_bf16_f32 v208, v4, v16\n"
    "v_cvt_pk_bf16_f32 v209, v28, v40\n"
    "v_cvt_pk_bf16_f32 v210, v52, v64\n"
    "v_cvt_pk_bf16_f32 v211, v76, v88\n"
    "v_cvt_pk_bf16_f32 v212, v5, v17\n"
    "v_cvt_pk_bf16_f32 v213, v29, v41\n"
    "v_cvt_pk_bf16_f32 v214, v53, v65\n"
    "v_cvt_pk_bf16_f32 v215, v77, v89\n"
    "v_cvt_pk_bf16_f32 v216, v6, v18\n"
    "v_cvt_pk_bf16_f32 v217, v30, v42\n"
    "v_cvt_pk_bf16_f32 v218, v54, v66\n"
    "v_cvt_pk_bf16_f32 v219, v78, v90\n"
    "v_cvt_pk_bf16_f32 v220, v7, v19\n"
    "v_cvt_pk_bf16_f32 v221, v31, v43\n"
    "v_cvt_pk_bf16_f32 v222, v55, v67\n"
    "v_cvt_pk_bf16_f32 v223, v79, v91\n"
    "v_cvt_pk_bf16_f32 v224, v8, v20\n"
    "v_cvt_pk_bf16_f32 v225, v32, v44\n"
    "v_cvt_pk_bf16_f32 v226, v56, v68\n"
    "v_cvt_pk_bf16_f32 v227, v80, v92\n"
    "v_cvt_pk_bf16_f32 v228, v9, v21\n"
    "v_cvt_pk_bf16_f32 v229, v33, v45\n"
    "v_cvt_pk_bf16_f32 v230, v57, v69\n"
    "v_cvt_pk_bf16_f32 v231, v81, v93\n"
    "v_cvt_pk_bf16_f32 v0, v10, v22\n"
    "v_cvt_pk_bf16_f32 v1, v34, v46\n"
    "v_cvt_pk_bf16_f32 v2, v58, v70\n"
    "v_cvt_pk_bf16_f32 v3, v82, v94\n"
    "v_cvt_pk_bf16_f32 v4, v11, v23\n"
    "v_cvt_pk_bf16_f32 v5, v35, v47\n"
    "v_cvt_pk_bf16_f32 v6, v59, v71\n"
    "v_cvt_pk_bf16_f32 v7, v83, v95\n"
    "s_mov_b32 s56, 2\n"
    "s_mov_b32 s74, 0\n"
    "s_mov_b32 s54, 24576\n"
    "s_mov_b64 s[28:29], s[32:33]\n"
    "s_mov_b32 s52, 65536\n"
    "v_add_u32 v241, 1024, v240\n"
    "s_mov_b64 exec, 1\n"
    ".Lsp_88:\n"
    "ds_read_b32 v251, v253 offset:12\n"
    "s_waitcnt lgkmcnt(0)\n"
    "v_readfirstlane_b32 s64, v251\n"
    "s_nop 0\n"
    "s_cmp_ge_u32 s64, 116\n"
    "s_cbranch_scc1 .Lok_89\n"
    "s_sleep 4\n"
    "s_branch .Lsp_88\n"
    ".Lok_89:\n"
    "s_mov_b64 exec, -1\n"
    "s_waitcnt vmcnt(0)\n"
    "s_mul_i32 s65, s79, 49152\n"
    "s_add_u32 s65, s65, 0\n"
    "s_add_u32 s66, s65, 4096\n"
    "s_add_u32 s67, s65, 8192\n"
    "s_lshl_b32 s64, s45, 1\n"
    "s_sub_u32 s64, s46, s64\n"
    "v_add_u32 v247, 147520, v240\n"
    "s_cmp_eq_u32 s64, 0\n"
    "s_cbranch_scc0 .Lst_90\n"
    "ds_write_b128 v247, v[192:195] offset:0\n"
    "ds_write_b128 v247, v[196:199] offset:1024\n"
    "ds_write_b128 v247, v[200:203] offset:2048\n"
    "ds_write_b128 v247, v[204:207] offset:3072\n"
    "ds_write_b128 v247, v[208:211] offset:4096\n"
    "ds_write_b128 v247, v[212:215] offset:5120\n"
    "s_branch .Lsu_91\n"
    ".Lst_90:\n"
    "buffer_store_dwordx4 v[192:195], v240, s[40:43], s65 offen offset:0 sc1\n"
    "buffer_store_dwordx4 v[196:199], v240, s[40:43], s65 offen offset:1024 sc1\n"
    "buffer_store_dwordx4 v[200:203], v240, s[40:43], s65 offen offset:2048 sc1\n"
    "buffer_store_dwordx4 v[204:207], v240, s[40:43], s65 offen offset:3072 sc1\n"
    "buffer_store_dwordx4 v[208:211], v240, s[40:43], s66 offen offset:0 sc1\n"
    "buffer_store_dwordx4 v[212:215], v240, s[40:43], s66 offen offset:1024 sc1\n"
    ".Lsu_91:\n"
    "s_cmp_eq_u32 s64, 1\n"
    "s_cbranch_scc0 .Lst_92\n"
    "ds_write_b128 v247, v[216:219] offset:0\n"
    "ds_write_b128 v247, v[220:223] offset:1024\n"
    "ds_write_b128 v247, v[224:227] offset:2048\n"
    "ds_write_b128 v247, v[228:231] offset:3072\n"
    "ds_write_b128 v247, v[0:3] offset:4096\n"
    "ds_write_b128 v247, v[4:7] offset:5120\n"
    "s_branch .Lsu_93\n"
    ".Lst_92:\n"
    "buffer_store_dwordx4 v[216:219], v240, s[40:43], s66 offen offset:2048 sc1\n"
    "buffer_store_dwordx4 v[220:223], v240, s[40:43], s66 offen offset:3072 sc1\n"
    "buffer_store_dwordx4 v[224:227], v240, s[40:43], s67 offen offset:0 sc1\n"
    "buffer_store_dwordx4 v[228:231], v240, s[40:43], s67 offen offset:1024 sc1\n"
    "buffer_store_dwordx4 v[0:3], v240, s[40:43], s67 offen offset:2048 sc1\n"
    "buffer_store_dwordx4 v[4:7], v240, s[40:43], s67 offen offset:3072 sc1\n"
    ".Lsu_93:\n"
    "s_waitcnt lgkmcnt(0)\n"
    "s_lshl_b32 s64, s45, 3\n"
    "s_add_u32 s64, s64, s46\n"
    "s_lshl_b32 s64, s64, 3\n"
    "v_mov_b32 v246, s64\n"
    "v_mov_b32 v248, s82\n"
    "v_mov_b32 v249, s83\n"
    "s_mov_b64 exec, 1\n"
    ".Lsp_94:\n"
    "ds_read_b32 v251, v253 offset:4\n"
    "s_waitcnt lgkmcnt(0)\n"
    "v_readfirstlane_b32 s64, v251\n"
    "s_nop 0\n"
    "s_cmp_ge_u32 s64, 1\n"
    "s_cbranch_scc1 .Lok_95\n"
    "s_sleep 1\n"
    "s_branch .Lsp_94\n"
    ".Lok_95:\n"
    "s_mov_b64 exec, -1\n"
    "s_branch .Lproj\n"
    ".Losetup:\n"
    "s_mov_b64 exec, 1\n"
    "ds_add_u32 v253, v252 offset:0\n"
    "s_mov_b64 exec, -1\n"
    "s_add_u32 s55, s55, 4\n"
    "s_mov_b32 s56, 1\n"
    "s_lshl_b32 s64, s79, 11\n"
    "s_add_u32 s64, s64, s45\n"
    "s_lshl_b32 s64, s64, 8\n"
    "s_add_u32 s28, s12, s64\n"
    "s_addc_u32 s29, s13, 0\n"
    "s_and_b32 s29, s29, 0xffff\n"
    "s_mov_b32 s30, 523520\n"
    "s_mov_b32 s31, 0x20000\n"
    "s_lshl_b32 s64, s46, 11\n"
    "s_add_u32 s64, s64, s45\n"
    "s_lshl_b32 s64, s64, 8\n"
    "s_add_u32 s32, s22, s64\n"
    "s_addc_u32 s33, s23, 0\n"
    "s_and_b32 s33, s33, 0xffff\n"
    "s_mov_b32 s34, 523520\n"
    "s_mov_b32 s35, 0x20000\n"
    "s_mov_b32 s53, 32768\n"
    "v_add_u32 v240, 98304, v240\n"
    "v_add_u32 v241, 1024, v240\n"
    "v_lshrrev_b32 v246, 4, v255\n"
    "v_and_b32 v247, 15, v255\n"
    "v_lshlrev_b32 v247, 4, v247\n"
    "v_lshl_add_u32 v232, v246, 12, v247\n"
    "v_add_u32 v233, 1024, v232\n"
    "v_add_u32 v234, 2048, v232\n"
    "v_add_u32 v235, 3072, v232\n"
    "v_add_u32 v236, 16384, v232\n"
    "v_add_u32 v237, 17408, v232\n"
    "v_add_u32 v238, 18432, v232\n"
    "v_add_u32 v239, 19456, v232\n"
    "s_lshl_b32 s64, s47, 5\n"
    "s_add_u32 s64, s64, s45\n"
    "s_mul_i32 s64, s64, 12288\n"
    "s_add_u32 s40, s24, s64\n"
    "s_addc_u32 s41, s25, 0\n"
    "s_and_b32 s41, s41, 0xffff\n"
    "s_mov_b32 s42, 12939264\n"
    "s_mov_b32 s43, 0x20000\n"
    "s_cmp_eq_u32 s78, 0\n"
    "s_cbranch_scc1 .Lprol\n"
    ".Ldl_96:\n"
    "s_memrealtime s[70:71]\n"
    "s_waitcnt lgkmcnt(0)\n"
    "s_sub_u32 s69, s70, s86\n"
    "s_cmp_ge_u32 s69, s78\n"
    "s_cbranch_scc1 .Ldn_97\n"
    "s_sleep 4\n"
    "s_branch .Ldl_96\n"
    ".Ldn_97:\n"
    "s_mov_b64 exec, 1\n"
    "ds_add_u32 v253, v252 offset:8\n"
    "s_mov_b64 exec, -1\n"
    "s_branch .Lprol\n"
    ".Lstore:\n"
    "s_mul_i32 s65, s46, 49152\n"
    "s_add_u32 s65, s65, 12582912\n"
    "s_add_u32 s66, s65, 4096\n"
    "s_add_u32 s67, s65, 8192\n"
    "s_lshl_b32 s64, s45, 1\n"
    "s_sub_u32 s64, s46, s64\n"
    "v_add_u32 v247, 153664, v240\n"
    "s_cmp_eq_u32 s64, 0\n"
    "s_cbranch_scc0 .Lst_98\n"
    "v_cvt_pk_bf16_f32 v96, v0, v12\n"
    "v_cvt_pk_bf16_f32 v97, v24, v36\n"
    "v_cvt_pk_bf16_f32 v98, v48, v60\n"
    "v_cvt_pk_bf16_f32 v99, v72, v84\n"
    "ds_write_b128 v247, v[96:99] offset:0\n"
    "v_cvt_pk_bf16_f32 v100, v1, v13\n"
    "v_cvt_pk_bf16_f32 v101, v25, v37\n"
    "v_cvt_pk_bf16_f32 v102, v49, v61\n"
    "v_cvt_pk_bf16_f32 v103, v73, v85\n"
    "ds_write_b128 v247, v[100:103] offset:1024\n"
    "v_cvt_pk_bf16_f32 v104, v2, v14\n"
    "v_cvt_pk_bf16_f32 v105, v26, v38\n"
    "v_cvt_pk_bf16_f32 v106, v50, v62\n"
    "v_cvt_pk_bf16_f32 v107, v74, v86\n"
    "ds_write_b128 v247, v[104:107] offset:2048\n"
    "v_cvt_pk_bf16_f32 v108, v3, v15\n"
    "v_cvt_pk_bf16_f32 v109, v27, v39\n"
    "v_cvt_pk_bf16_f32 v110, v51, v63\n"
    "v_cvt_pk_bf16_f32 v111, v75, v87\n"
    "ds_write_b128 v247, v[108:111] offset:3072\n"
    "v_cvt_pk_bf16_f32 v112, v4, v16\n"
    "v_cvt_pk_bf16_f32 v113, v28, v40\n"
    "v_cvt_pk_bf16_f32 v114, v52, v64\n"
    "v_cvt_pk_bf16_f32 v115, v76, v88\n"
    "ds_write_b128 v247, v[112:115] offset:4096\n"
    "v_cvt_pk_bf16_f32 v116, v5, v17\n"
    "v_cvt_pk_bf16_f32 v117, v29, v41\n"
    "v_cvt_pk_bf16_f32 v118, v53, v65\n"
    "v_cvt_pk_bf16_f32 v119, v77, v89\n"
    "ds_write_b128 v247, v[116:119] offset:5120\n"
    "s_branch .Lsu_99\n"
    ".Lst_98:\n"
    "v_cvt_pk_bf16_f32 v96, v0, v12\n"
    "v_cvt_pk_bf16_f32 v97, v24, v36\n"
    "v_cvt_pk_bf16_f32 v98, v48, v60\n"
    "v_cvt_pk_bf16_f32 v99, v72, v84\n"
    "buffer_store_dwordx4 v[96:99], v240, s[40:43], s65 offen offset:0 sc1\n"
    "v_cvt_pk_bf16_f32 v100, v1, v13\n"
    "v_cvt_pk_bf16_f32 v101, v25, v37\n"
    "v_cvt_pk_bf16_f32 v102, v49, v61\n"
    "v_cvt_pk_bf16_f32 v103, v73, v85\n"
    "buffer_store_dwordx4 v[100:103], v240, s[40:43], s65 offen offset:1024 sc1\n"
    "v_cvt_pk_bf16_f32 v104, v2, v14\n"
    "v_cvt_pk_bf16_f32 v105, v26, v38\n"
    "v_cvt_pk_bf16_f32 v106, v50, v62\n"
    "v_cvt_pk_bf16_f32 v107, v74, v86\n"
    "buffer_store_dwordx4 v[104:107], v240, s[40:43], s65 offen offset:2048 sc1\n"
    "v_cvt_pk_bf16_f32 v108, v3, v15\n"
    "v_cvt_pk_bf16_f32 v109, v27, v39\n"
    "v_cvt_pk_bf16_f32 v110, v51, v63\n"
    "v_cvt_pk_bf16_f32 v111, v75, v87\n"
    "buffer_store_dwordx4 v[108:111], v240, s[40:43], s65 offen offset:3072 sc1\n"
    "v_cvt_pk_bf16_f32 v112, v4, v16\n"
    "v_cvt_pk_bf16_f32 v113, v28, v40\n"
    "v_cvt_pk_bf16_f32 v114, v52, v64\n"
    "v_cvt_pk_bf16_f32 v115, v76, v88\n"
    "buffer_store_dwordx4 v[112:115], v240, s[40:43], s66 offen offset:0 sc1\n"
    "v_cvt_pk_bf16_f32 v116, v5, v17\n"
    "v_cvt_pk_bf16_f32 v117, v29, v41\n"
    "v_cvt_pk_bf16_f32 v118, v53, v65\n"
    "v_cvt_pk_bf16_f32 v119, v77, v89\n"
    "buffer_store_dwordx4 v[116:119], v240, s[40:43], s66 offen offset:1024 sc1\n"
    ".Lsu_99:\n"
    "s_cmp_eq_u32 s64, 1\n"
    "s_cbranch_scc0 .Lst_100\n"
    "v_cvt_pk_bf16_f32 v120, v6, v18\n"
    "v_cvt_pk_bf16_f32 v121, v30, v42\n"
    "v_cvt_pk_bf16_f32 v122, v54, v66\n"
    "v_cvt_pk_bf16_f32 v123, v78, v90\n"
    "ds_write_b128 v247, v[120:123] offset:0\n"
    "v_cvt_pk_bf16_f32 v124, v7, v19\n"
    "v_cvt_pk_bf16_f32 v125, v31, v43\n"
    "v_cvt_pk_bf16_f32 v126, v55, v67\n"
    "v_cvt_pk_bf16_f32 v127, v79, v91\n"
    "ds_write_b128 v247, v[124:127] offset:1024\n"
    "v_cvt_pk_bf16_f32 v128, v8, v20\n"
    "v_cvt_pk_bf16_f32 v129, v32, v44\n"
    "v_cvt_pk_bf16_f32 v130, v56, v68\n"
    "v_cvt_pk_bf16_f32 v131, v80, v92\n"
    "ds_write_b128 v247, v[128:131] offset:2048\n"
    "v_cvt_pk_bf16_f32 v132, v9, v21\n"
    "v_cvt_pk_bf16_f32 v133, v33, v45\n"
    "v_cvt_pk_bf16_f32 v134, v57, v69\n"
    "v_cvt_pk_bf16_f32 v135, v81, v93\n"
    "ds_write_b128 v247, v[132:135] offset:3072\n"
    "v_cvt_pk_bf16_f32 v136, v10, v22\n"
    "v_cvt_pk_bf16_f32 v137, v34, v46\n"
    "v_cvt_pk_bf16_f32 v138, v58, v70\n"
    "v_cvt_pk_bf16_f32 v139, v82, v94\n"
    "ds_write_b128 v247, v[136:139] offset:4096\n"
    "v_cvt_pk_bf16_f32 v140, v11, v23\n"
    "v_cvt_pk_bf16_f32 v141, v35, v47\n"
    "v_cvt_pk_bf16_f32 v142, v59, v71\n"
    "v_cvt_pk_bf16_f32 v143, v83, v95\n"
    "ds_write_b128 v247, v[140:143] offset:5120\n"
    "s_branch .Lsu_101\n"
    ".Lst_100:\n"
    "v_cvt_pk_bf16_f32 v120, v6, v18\n"
    "v_cvt_pk_bf16_f32 v121, v30, v42\n"
    "v_cvt_pk_bf16_f32 v122, v54, v66\n"
    "v_cvt_pk_bf16_f32 v123, v78, v90\n"
    "buffer_store_dwordx4 v[120:123], v240, s[40:43], s66 offen offset:2048 sc1\n"
    "v_cvt_pk_bf16_f32 v124, v7, v19\n"
    "v_cvt_pk_bf16_f32 v125, v31, v43\n"
    "v_cvt_pk_bf16_f32 v126, v55, v67\n"
    "v_cvt_pk_bf16_f32 v127, v79, v91\n"
    "buffer_store_dwordx4 v[124:127], v240, s[40:43], s66 offen offset:3072 sc1\n"
    "v_cvt_pk_bf16_f32 v128, v8, v20\n"
    "v_cvt_pk_bf16_f32 v129, v32, v44\n"
    "v_cvt_pk_bf16_f32 v130, v56, v68\n"
    "v_cvt_pk_bf16_f32 v131, v80, v92\n"
    "buffer_store_dwordx4 v[128:131], v240, s[40:43], s67 offen offset:0 sc1\n"
    "v_cvt_pk_bf16_f32 v132, v9, v21\n"
    "v_cvt_pk_bf16_f32 v133, v33, v45\n"
    "v_cvt_pk_bf16_f32 v134, v57, v69\n"
    "v_cvt_pk_bf16_f32 v135, v81, v93\n"
    "buffer_store_dwordx4 v[132:135], v240, s[40:43], s67 offen offset:1024 sc1\n"
    "v_cvt_pk_bf16_f32 v136, v10, v22\n"
    "v_cvt_pk_bf16_f32 v137, v34, v46\n"
    "v_cvt_pk_bf16_f32 v138, v58, v70\n"
    "v_cvt_pk_bf16_f32 v139, v82, v94\n"
    "buffer_store_dwordx4 v[136:139], v240, s[40:43], s67 offen offset:2048 sc1\n"
    "v_cvt_pk_bf16_f32 v140, v11, v23\n"
    "v_cvt_pk_bf16_f32 v141, v35, v47\n"
    "v_cvt_pk_bf16_f32 v142, v59, v71\n"
    "v_cvt_pk_bf16_f32 v143, v83, v95\n"
    "buffer_store_dwordx4 v[140:143], v240, s[40:43], s67 offen offset:3072 sc1\n"
    ".Lsu_101:\n"
    "s_waitcnt lgkmcnt(0)\n"
    ".Lreduce:\n"
    "v_lshrrev_b32 v246, 4, v255\n"
    "v_bfe_u32 v247, v255, 2, 2\n"
    "v_cmp_eq_u32 vcc, v246, v247\n"
    "v_and_b32 v247, 1, v255\n"
    "v_lshlrev_b32 v247, 4, v247\n"
    "v_mov_b32 v248, 0x3f80\n"
    "v_lshlrev_b32 v247, v247, v248\n"
    "v_cndmask_b32 v247, 0, v247, vcc\n"
    "v_and_b32 v248, 2, v255\n"
    "v_cmp_eq_u32 vcc, 0, v248\n"
    "v_mov_b64 v[146:147], 0\n"
    "v_mov_b64 v[148:149], 0\n"
    "v_cndmask_b32 v144, 0, v247, vcc\n"
    "v_sub_u32 v145, v247, v144\n"
    "v_mov_b32 v150, v144\n"
    "v_mov_b32 v151, v145\n"
    "s_lshl_b32 s66, s61, 5\n"
    "s_add_u32 s66, s66, s47\n"
    "s_mul_i32 s66, s66, 393216\n"
    "s_add_u32 s32, s24, s66\n"
    "s_addc_u32 s33, s25, 0\n"
    "s_and_b32 s33, s33, 0xffff\n"
    "s_mov_b32 s34, 393216\n"
    "s_mov_b32 s35, 0x20000\n"
    "s_and_b32 s66, s46, 1\n"
    "s_mul_i32 s66, s66, 3\n"
    "s_add_u32 s66, s66, s45\n"
    "s_lshr_b32 s67, s66, 1\n"
    "s_lshl_b32 s67, s67, 4\n"
    "s_and_b32 s66, s66, 1\n"
    "s_lshl_b32 s66, s66, 1\n"
    "s_add_u32 s67, s67, s66\n"
    "s_mul_i32 s66, s47, 48\n"
    "s_add_u32 s67, s67, s66\n"
    "s_lshl_b32 s67, s67, 1\n"
    "s_add_u32 s67, s67, s61\n"
    "s_lshl_b32 s67, s67, 2\n"
    "s_lshr_b32 s66, s46, 1\n"
    "s_add_u32 s67, s67, s66\n"
    "s_lshl_b32 s67, s67, 8\n"
    "s_add_u32 s36, s26, s67\n"
    "s_addc_u32 s37, s27, 0\n"
    "s_and_b32 s37, s37, 0xffff\n"
    "s_mov_b32 s38, 26880\n"
    "s_mov_b32 s39, 0x20000\n"
    "v_and_b32 v247, 15, v255\n"
    "v_lshlrev_b32 v247, 4, v247\n"
    "v_lshl_add_u32 v168, v246, 13, v247\n"
    "s_lshl_b32 s65, s61, 2\n"
    "s_add_u32 s65, s65, s66\n"
    "s_lshl_b32 s65, s65, 6\n"
    "v_and_b32 v246, 7, v255\n"
    "v_lshlrev_b32 v246, 3, v246\n"
    "v_add_u32 v169, s65, v246\n"
    "v_mov_b32 v2, s82\n"
    "v_mov_b32 v3, s83\n"
    "v_mov_b64 v[6:7], 0\n"
    "s_mul_i32 s52, s46, 3\n"
    "s_add_u32 s52, s52, s45\n"
    "s_lshl_b32 s52, s52, 11\n"
    "s_mul_i32 s64, s61, 6144\n"
    "s_add_u32 s64, s64, s51\n"
    "s_add_u32 s64, s64, 147520\n"
    "v_add_u32 v170, s64, v240\n"
    "s_cmp_eq_u32 s61, 0\n"
    "s_cselect_b32 s67, s79, s46\n"
    "s_cbranch_scc1 .Lrm\n"
    "s_lshl_b32 s64, s45, 3\n"
    "s_add_u32 s64, s64, s46\n"
    "s_lshl_b32 s64, s64, 3\n"
    "v_mov_b32 v246, s64\n"
    "v_mov_b32 v248, s82\n"
    "v_mov_b32 v249, s83\n"
    "s_waitcnt vmcnt(0)\n"
    "s_mov_b64 exec, 1\n"
    "global_atomic_swap_x2 v246, v[248:249], s[84:85] offset:256 sc1\n"
    "s_mov_b64 exec, -1\n"
    "s_branch .Lrg\n"
    ".Lrm:\n"
    "s_mov_b64 exec, 1\n"
    ".Lsp_102:\n"
    "ds_read_b32 v251, v253 offset:20\n"
    "s_waitcnt lgkmcnt(0)\n"
    "v_readfirstlane_b32 s64, v251\n"
    "s_nop 0\n"
    "s_cmp_ge_u32 s64, 8\n"
    "s_cbranch_scc1 .Lok_103\n"
    "s_sleep 1\n"
    "s_branch .Lsp_102\n"
    ".Lok_103:\n"
    "s_mov_b64 exec, -1\n"
    ".Lrg:\n"
    "s_cmp_eq_u32 s45, 3\n"
    "s_cbranch_scc1 .Lrdone\n"
    "s_mov_b32 s66, 0\n"
    "s_mov_b64 exec, 0xff\n"
    ".Lrpoll:\n"
    "global_atomic_add_x2 v[4:5], v169, v[6:7], s[84:85] sc0 sc1\n"
    "s_add_u32 s66, s66, 1\n"
    "s_waitcnt vmcnt(0)\n"
    "v_cmp_ne_u64 vcc, v[4:5], v[2:3]\n"
    "s_cbranch_vccz .Lrgo\n"
    "s_cmp_gt_u32 s66, 0x20000\n"
    "s_cbranch_scc1 .Lrgo\n"
    "s_sleep 1\n"
    "s_branch .Lrpoll\n"
    ".Lrgo:\n"
    "s_mov_b64 exec, -1\n"
    "s_cmp_eq_u32 s67, 0\n"
    "s_cbranch_scc0 .Lro_104\n"
    "ds_read_b128 v[8:11], v170 offset:0\n"
    "ds_read_b128 v[12:15], v170 offset:1024\n"
    "s_branch .Lrp_105\n"
    ".Lro_104:\n"
    "buffer_load_dwordx4 v[8:11], v240, s[32:35], s52 offen offset:0 sc1 nt\n"
    "buffer_load_dwordx4 v[12:15], v240, s[32:35], s52 offen offset:1024 sc1 nt\n"
    ".Lrp_105:\n"
    "s_add_u32 s52, s52, 49152\n"
    "s_cmp_eq_u32 s67, 1\n"
    "s_cbranch_scc0 .Lro_106\n"
    "ds_read_b128 v[16:19], v170 offset:0\n"
    "ds_read_b128 v[20:23], v170 offset:1024\n"
    "s_branch .Lrp_107\n"
    ".Lro_106:\n"
    "buffer_load_dwordx4 v[16:19], v240, s[32:35], s52 offen offset:0 sc1 nt\n"
    "buffer_load_dwordx4 v[20:23], v240, s[32:35], s52 offen offset:1024 sc1 nt\n"
    ".Lrp_107:\n"
    "s_add_u32 s52, s52, 49152\n"
    "s_cmp_eq_u32 s67, 2\n"
    "s_cbranch_scc0 .Lro_108\n"
    "ds_read_b128 v[24:27], v170 offset:0\n"
    "ds_read_b128 v[28:31], v170 offset:1024\n"
    "s_branch .Lrp_109\n"
    ".Lro_108:\n"
    "buffer_load_dwordx4 v[24:27], v240, s[32:35], s52 offen offset:0 sc1 nt\n"
    "buffer_load_dwordx4 v[28:31], v240, s[32:35], s52 offen offset:1024 sc1 nt\n"
    ".Lrp_109:\n"
    "s_add_u32 s52, s52, 49152\n"
    "s_cmp_eq_u32 s67, 3\n"
    "s_cbranch_scc0 .Lro_110\n"
    "ds_read_b128 v[32:35], v170 offset:0\n"
    "ds_read_b128 v[36:39], v170 offset:1024\n"
    "s_branch .Lrp_111\n"
    ".Lro_110:\n"
    "buffer_load_dwordx4 v[32:35], v240, s[32:35], s52 offen offset:0 sc1 nt\n"
    "buffer_load_dwordx4 v[36:39], v240, s[32:35], s52 offen offset:1024 sc1 nt\n"
    ".Lrp_111:\n"
    "s_add_u32 s52, s52, 49152\n"
    "s_cmp_eq_u32 s67, 4\n"
    "s_cbranch_scc0 .Lro_112\n"
    "ds_read_b128 v[40:43], v170 offset:0\n"
    "ds_read_b128 v[44:47], v170 offset:1024\n"
    "s_branch .Lrp_113\n"
    ".Lro_112:\n"
    "buffer_load_dwordx4 v[40:43], v240, s[32:35], s52 offen offset:0 sc1 nt\n"
    "buffer_load_dwordx4 v[44:47], v240, s[32:35], s52 offen offset:1024 sc1 nt\n"
    ".Lrp_113:\n"
    "s_add_u32 s52, s52, 49152\n"
    "s_cmp_eq_u32 s67, 5\n"
    "s_cbranch_scc0 .Lro_114\n"
    "ds_read_b128 v[48:51], v170 offset:0\n"
    "ds_read_b128 v[52:55], v170 offset:1024\n"
    "s_branch .Lrp_115\n"
    ".Lro_114:\n"
    "buffer_load_dwordx4 v[48:51], v240, s[32:35], s52 offen offset:0 sc1 nt\n"
    "buffer_load_dwordx4 v[52:55], v240, s[32:35], s52 offen offset:1024 sc1 nt\n"
    ".Lrp_115:\n"
    "s_add_u32 s52, s52, 49152\n"
    "s_cmp_eq_u32 s67, 6\n"
    "s_cbranch_scc0 .Lro_116\n"
    "ds_read_b128 v[56:59], v170 offset:0\n"
    "ds_read_b128 v[60:63], v170 offset:1024\n"
    "s_branch .Lrp_117\n"
    ".Lro_116:\n"
    "buffer_load_dwordx4 v[56:59], v240, s[32:35], s52 offen offset:0 sc1 nt\n"
    "buffer_load_dwordx4 v[60:63], v240, s[32:35], s52 offen offset:1024 sc1 nt\n"
    ".Lrp_117:\n"
    "s_add_u32 s52, s52, 49152\n"
    "s_cmp_eq_u32 s67, 7\n"
    "s_cbranch_scc0 .Lro_118\n"
    "ds_read_b128 v[64:67], v170 offset:0\n"
    "ds_read_b128 v[68:71], v170 offset:1024\n"
    "s_branch .Lrp_119\n"
    ".Lro_118:\n"
    "buffer_load_dwordx4 v[64:67], v240, s[32:35], s52 offen offset:0 sc1 nt\n"
    "buffer_load_dwordx4 v[68:71], v240, s[32:35], s52 offen offset:1024 sc1 nt\n"
    ".Lrp_119:\n"
    "s_or_b32 s64, s46, s44\n"
    "s_cbranch_scc1 .Lnoep3\n"
    "s_mov_b64 exec, 1\n"
    "global_atomic_swap_x2 v6, v[2:3], s[84:85] offset:512 sc1\n"
    "s_mov_b64 exec, -1\n"
    ".Lnoep3:\n"
    "s_waitcnt vmcnt(12) lgkmcnt(0)\n"
    "v_mfma_f32_16x16x32_bf16 v[152:155], v[144:147], v[8:11], 0\n"
    "v_mfma_f32_16x16x32_bf16 v[156:159], v[148:151], v[8:11], 0\n"
    "v_mfma_f32_16x16x32_bf16 v[160:163], v[144:147], v[12:15], 0\n"
    "v_mfma_f32_16x16x32_bf16 v[164:167], v[148:151], v[12:15], 0\n"
    "s_waitcnt vmcnt(10) lgkmcnt(0)\n"
    "v_mfma_f32_16x16x32_bf16 v[152:155], v[144:147], v[16:19], v[152:155]\n"
    "v_mfma_f32_16x16x32_bf16 v[156:159], v[148:151], v[16:19], v[156:159]\n"
    "v_mfma_f32_16x16x32_bf16 v[160:163], v[144:147], v[20:23], v[160:163]\n"
    "v_mfma_f32_16x16x32_bf16 v[164:167], v[148:151], v[20:23], v[164:167]\n"
    "s_waitcnt vmcnt(8) lgkmcnt(0)\n"
    "v_mfma_f32_16x16x32_bf16 v[152:155], v[144:147], v[24:27], v[152:155]\n"
    "v_mfma_f32_16x16x32_bf16 v[156:159], v[148:151], v[24:27], v[156:159]\n"
    "v_mfma_f32_16x16x32_bf16 v[160:163], v[144:147], v[28:31], v[160:163]\n"
    "v_mfma_f32_16x16x32_bf16 v[164:167], v[148:151], v[28:31], v[164:167]\n"
    "s_waitcnt vmcnt(6) lgkmcnt(0)\n"
    "v_mfma_f32_16x16x32_bf16 v[152:155], v[144:147], v[32:35], v[152:155]\n"
    "v_mfma_f32_16x16x32_bf16 v[156:159], v[148:151], v[32:35], v[156:159]\n"
    "v_mfma_f32_16x16x32_bf16 v[160:163], v[144:147], v[36:39], v[160:163]\n"
    "v_mfma_f32_16x16x32_bf16 v[164:167], v[148:151], v[36:39], v[164:167]\n"
    "s_waitcnt vmcnt(4) lgkmcnt(0)\n"
    "v_mfma_f32_16x16x32_bf16 v[152:155], v[144:147], v[40:43], v[152:155]\n"
    "v_mfma_f32_16x16x32_bf16 v[156:159], v[148:151], v[40:43], v[156:159]\n"
    "v_mfma_f32_16x16x32_bf16 v[160:163], v[144:147], v[44:47], v[160:163]\n"
    "v_mfma_f32_16x16x32_bf16 v[164:167], v[148:151], v[44:47], v[164:167]\n"
    "s_waitcnt vmcnt(2) lgkmcnt(0)\n"
    "v_mfma_f32_16x16x32_bf16 v[152:155], v[144:147], v[48:51], v[152:155]\n"
    "v_mfma_f32_16x16x32_bf16 v[156:159], v[148:151], v[48:51], v[156:159]\n"
    "v_mfma_f32_16x16x32_bf16 v[160:163], v[144:147], v[52:55], v[160:163]\n"
    "v_mfma_f32_16x16x32_bf16 v[164:167], v[148:151], v[52:55], v[164:167]\n"
    "s_waitcnt vmcnt(0) lgkmcnt(0)\n"
    "v_mfma_f32_16x16x32_bf16 v[152:155], v[144:147], v[56:59], v[152:155]\n"
    "v_mfma_f32_16x16x32_bf16 v[156:159], v[148:151], v[56:59], v[156:159]\n"
    "v_mfma_f32_16x16x32_bf16 v[160:163], v[144:147], v[60:63], v[160:163]\n"
    "v_mfma_f32_16x16x32_bf16 v[164:167], v[148:151], v[60:63], v[164:167]\n"
    "s_waitcnt vmcnt(0) lgkmcnt(0)\n"
    "v_mfma_f32_16x16x32_bf16 v[152:155], v[144:147], v[64:67], v[152:155]\n"
    "v_mfma_f32_16x16x32_bf16 v[156:159], v[148:151], v[64:67], v[156:159]\n"
    "v_mfma_f32_16x16x32_bf16 v[160:163], v[144:147], v[68:71], v[160:163]\n"
    "v_mfma_f32_16x16x32_bf16 v[164:167], v[148:151], v[68:71], v[164:167]\n"
    "s_nop 4\n"
    "v_cvt_pk_bf16_f32 v72, v152, v153\n"
    "v_cvt_pk_bf16_f32 v73, v154, v155\n"
    "v_cvt_pk_bf16_f32 v74, v156, v157\n"
    "v_cvt_pk_bf16_f32 v75, v158, v159\n"
    "buffer_store_dwordx4 v[72:75], v168, s[36:39], 0 offen offset:0\n"
    "v_cvt_pk_bf16_f32 v76, v160, v161\n"
    "v_cvt_pk_bf16_f32 v77, v162, v163\n"
    "v_cvt_pk_bf16_f32 v78, v164, v165\n"
    "v_cvt_pk_bf16_f32 v79, v166, v167\n"
    "buffer_store_dwordx4 v[76:79], v168, s[36:39], 0 offen offset:2048\n"
    ".Lrdone:\n"
    "s_endpgm\n"
    ".Lpd_28:\n"
    "s_and_b32 s64, s64, 255\n"
    "s_cbranch_scc1 .Lpc_27\n"
    "s_cmp_lt_u32 s65, 2556\n"
    "s_cselect_b32 s69, s91, s92\n"
    "s_cmp_ge_u32 s65, 4649\n"
    "s_cselect_b32 s69, s93, s69\n"
    "s_add_u32 s69, s69, s63\n"
    "s_cmp_ge_u32 s65, s69\n"
    "s_cbranch_scc1 .Lpb_26\n"
    ".Lpc_27:\n"
    "s_sleep 2\n"
    "s_branch .Lpa_25\n"
    ".Ltw_29:\n"
    "global_load_dword v251, v236, s[88:89] offset:128\n"
    "s_mov_b32 s90, -1\n"
    "global_load_dword v236, v236, s[88:89]\n"
    "s_branch .Ltb_30\n"
    ".Lpd_35:\n"
    "s_and_b32 s64, s64, 255\n"
    "s_cbranch_scc1 .Lpc_34\n"
    "s_cmp_lt_u32 s65, 2556\n"
    "s_cselect_b32 s69, s91, s92\n"
    "s_cmp_ge_u32 s65, 4649\n"
    "s_cselect_b32 s69, s93, s69\n"
    "s_add_u32 s69, s69, s63\n"
    "s_cmp_ge_u32 s65, s69\n"
    "s_cbranch_scc1 .Lpb_33\n"
    ".Lpc_34:\n"
    "s_sleep 2\n"
    "s_branch .Lpa_32\n"
    ".Lpd_40:\n"
    "s_and_b32 s64, s64, 255\n"
    "s_cbranch_scc1 .Lpc_39\n"
    "s_cmp_lt_u32 s65, 2556\n"
    "s_cselect_b32 s69, s91, s92\n"
    "s_cmp_ge_u32 s65, 4649\n"
    "s_cselect_b32 s69, s93, s69\n"
    "s_add_u32 s69, s69, s63\n"
    "s_cmp_ge_u32 s65, s69\n"
    "s_cbranch_scc1 .Lpb_38\n"
    ".Lpc_39:\n"
    "s_sleep 2\n"
    "s_branch .Lpa_37\n"
    ".Ltw_41:\n"
    "global_load_dword v251, v236, s[88:89] offset:128\n"
    "s_mov_b32 s90, -1\n"
    "global_load_dword v236, v236, s[88:89]\n"
    "s_branch .Ltb_42\n"
    ".Lpd_47:\n"
    "s_and_b32 s64, s64, 255\n"
    "s_cbranch_scc1 .Lpc_46\n"
    "s_cmp_lt_u32 s65, 2556\n"
    "s_cselect_b32 s69, s91, s92\n"
    "s_cmp_ge_u32 s65, 4649\n"
    "s_cselect_b32 s69, s93, s69\n"
    "s_add_u32 s69, s69, s63\n"
    "s_cmp_ge_u32 s65, s69\n"
    "s_cbranch_scc1 .Lpb_45\n"
    ".Lpc_46:\n"
    "s_sleep 2\n"
    "s_branch .Lpa_44\n"
    ".Lpd_52:\n"
    "s_and_b32 s64, s64, 255\n"
    "s_cbranch_scc1 .Lpc_51\n"
    "s_cmp_lt_u32 s65, 2556\n"
    "s_cselect_b32 s69, s91, s92\n"
    "s_cmp_ge_u32 s65, 4649\n"
    "s_cselect_b32 s69, s93, s69\n"
    "s_add_u32 s69, s69, s63\n"
    "s_cmp_ge_u32 s65, s69\n"
    "s_cbranch_scc1 .Lpb_50\n"
    ".Lpc_51:\n"
    "s_sleep 2\n"
    "s_branch .Lpa_49\n"
    ".Ltw_53:\n"
    "global_load_dword v251, v236, s[88:89] offset:128\n"
    "s_mov_b32 s90, -1\n"
    "global_load_dword v236, v236, s[88:89]\n"
    "s_branch .Ltb_54\n"
    ".Lpd_59:\n"
    "s_and_b32 s64, s64, 255\n"
    "s_cbranch_scc1 .Lpc_58\n"
    "s_cmp_lt_u32 s65, 2556\n"
    "s_cselect_b32 s69, s91, s92\n"
    "s_cmp_ge_u32 s65, 4649\n"
    "s_cselect_b32 s69, s93, s69\n"
    "s_add_u32 s69, s69, s63\n"
    "s_cmp_ge_u32 s65, s69\n"
    "s_cbranch_scc1 .Lpb_57\n"
    ".Lpc_58:\n"
    "s_sleep 2\n"
    "s_branch .Lpa_56\n"
    ".Lpd_64:\n"
    "s_and_b32 s64, s64, 255\n"
    "s_cbranch_scc1 .Lpc_63\n"
    "s_cmp_lt_u32 s65, 2556\n"
    "s_cselect_b32 s69, s91, s92\n"
    "s_cmp_ge_u32 s65, 4649\n"
    "s_cselect_b32 s69, s93, s69\n"
    "s_add_u32 s69, s69, s63\n"
    "s_cmp_ge_u32 s65, s69\n"
    "s_cbranch_scc1 .Lpb_62\n"
    ".Lpc_63:\n"
    "s_sleep 2\n"
    "s_branch .Lpa_61\n"
    ".Ltw_65:\n"
    "global_load_dword v251, v236, s[88:89] offset:128\n"
    "s_mov_b32 s90, -1\n"
    "global_load_dword v236, v236, s[88:89]\n"
    "s_branch .Ltb_66\n"
    ".Lgw_69:\n"
    "s_mov_b64 exec, 1\n"
    ".Lsp_120:\n"
    "ds_read_b32 v251, v253 offset:0\n"
    "s_waitcnt lgkmcnt(0)\n"
    "v_readfirstlane_b32 s64, v251\n"
    "s_nop 0\n"
    "s_cmp_ge_u32 s64, s55\n"
    "s_cbranch_scc1 .Lok_121\n"
    "s_sleep 1\n"
    "s_branch .Lsp_120\n"
    ".Lok_121:\n"
    "s_mov_b64 exec, -1\n"
    ".Ldl_122:\n"
    "s_memrealtime s[70:71]\n"
    "s_waitcnt lgkmcnt(0)\n"
    "s_sub_u32 s69, s70, s86\n"
    "s_cmp_ge_u32 s69, s63\n"
    "s_cbranch_scc1 .Ldn_123\n"
    "s_sleep 4\n"
    "s_branch .Ldl_122\n"
    ".Ldn_123:\n"
    "s_branch .Lgb_70\n"
    ".Lgw_71:\n"
    "s_mov_b64 exec, 1\n"
    ".Lsp_124:\n"
    "ds_read_b32 v251, v253 offset:0\n"
    "s_waitcnt lgkmcnt(0)\n"
    "v_readfirstlane_b32 s64, v251\n"
    "s_nop 0\n"
    "s_cmp_ge_u32 s64, s55\n"
    "s_cbranch_scc1 .Lok_125\n"
    "s_sleep 1\n"
    "s_branch .Lsp_124\n"
    ".Lok_125:\n"
    "s_mov_b64 exec, -1\n"
    ".Ldl_126:\n"
    "s_memrealtime s[70:71]\n"
    "s_waitcnt lgkmcnt(0)\n"
    "s_sub_u32 s69, s70, s86\n"
    "s_cmp_ge_u32 s69, s63\n"
    "s_cbranch_scc1 .Ldn_127\n"
    "s_sleep 4\n"
    "s_branch .Ldl_126\n"
    ".Ldn_127:\n"
    "s_branch .Lgb_72\n"
    ".Lgw_74:\n"
    "s_mov_b64 exec, 1\n"
    ".Lsp_128:\n"
    "ds_read_b32 v251, v253 offset:0\n"
    "s_waitcnt lgkmcnt(0)\n"
    "v_readfirstlane_b32 s64, v251\n"
    "s_nop 0\n"
    "s_cmp_ge_u32 s64, s55\n"
    "s_cbranch_scc1 .Lok_129\n"
    "s_sleep 1\n"
    "s_branch .Lsp_128\n"
    ".Lok_129:\n"
    "s_mov_b64 exec, -1\n"
    ".Ldl_130:\n"
    "s_memrealtime s[70:71]\n"
    "s_waitcnt lgkmcnt(0)\n"
    "s_sub_u32 s69, s70, s86\n"
    "s_cmp_ge_u32 s69, s63\n"
    "s_cbranch_scc1 .Ldn_131\n"
    "s_sleep 4\n"
    "s_branch .Ldl_130\n"
    ".Ldn_131:\n"
    "s_branch .Lgb_75\n"
    ".Lgw_76:\n"
    "s_mov_b64 exec, 1\n"
    ".Lsp_132:\n"
    "ds_read_b32 v251, v253 offset:0\n"
    "s_waitcnt lgkmcnt(0)\n"
    "v_readfirstlane_b32 s64, v251\n"
    "s_nop 0\n"
    "s_cmp_ge_u32 s64, s55\n"
    "s_cbranch_scc1 .Lok_133\n"
    "s_sleep 1\n"
    "s_branch .Lsp_132\n"
    ".Lok_133:\n"
    "s_mov_b64 exec, -1\n"
    ".Ldl_134:\n"
    "s_memrealtime s[70:71]\n"
    "s_waitcnt lgkmcnt(0)\n"
    "s_sub_u32 s69, s70, s86\n"
    "s_cmp_ge_u32 s69, s63\n"
    "s_cbranch_scc1 .Ldn_135\n"
    "s_sleep 4\n"
    "s_branch .Ldl_134\n"
    ".Ldn_135:\n"
    "s_branch .Lgb_77\n"
    ".Lslow0_0:\n"
    "s_mov_b32 s50, 0\n"
    "s_branch .Lslow0\n"
    ".Lslow0_1:\n"
    "s_mov_b32 s50, 1\n"
    "s_branch .Lslow0\n"
    ".Lslow0_2:\n"
    "s_mov_b32 s50, 2\n"
    "s_branch .Lslow0\n"
    ".Lslow0_3:\n"
    "s_mov_b32 s50, 3\n"
    "s_branch .Lslow0\n"
    ".Lslow0_4:\n"
    "s_mov_b32 s50, 4\n"
    "s_branch .Lslow0\n"
    ".Lslow0_5:\n"
    "s_mov_b32 s50, 5\n"
    "s_branch .Lslow0\n"
    ".Lslow0_6:\n"
    "s_mov_b32 s50, 6\n"
    "s_branch .Lslow0\n"
    ".Lslow0:\n"
    "s_nop 7\n"
    "v_max3_f32 v200, v176, v177, v178\n"
    "v_max3_f32 v200, v200, v179, v180\n"
    "v_max3_f32 v200, v200, v181, v182\n"
    "v_max_f32 v200, v200, v183\n"
    "v_mov_b32 v201, v200\n"
    "s_nop 1\n"
    "v_permlane16_swap_b32 v200, v201\n"
    "v_max_f32 v200, v200, v201\n"
    "v_mov_b32 v201, v200\n"
    "s_nop 1\n"
    "v_permlane32_swap_b32 v200, v201\n"
    "v_max3_f32 v200, v200, v201, 0\n"
    "v_exp_f32 v202, -v200\n"
    "v_sub_f32 v212, v212, v200\n"
    "v_sub_f32 v213, v213, v200\n"
    "v_sub_f32 v214, v214, v200\n"
    "v_sub_f32 v215, v215, v200\n"
    "v_sub_f32 v176, v176, v200\n"
    "v_sub_f32 v177, v177, v200\n"
    "v_sub_f32 v178, v178, v200\n"
    "v_sub_f32 v179, v179, v200\n"
    "v_sub_f32 v180, v180, v200\n"
    "v_sub_f32 v181, v181, v200\n"
    "v_sub_f32 v182, v182, v200\n"
    "v_sub_f32 v183, v183, v200\n"
    "v_mov_b32 v203, v202\n"
    "v_mul_f32 v224, v224, v202\n"
    "v_pk_mul_f32 v[0:1], v[0:1], v[202:203]\n"
    "v_pk_mul_f32 v[2:3], v[2:3], v[202:203]\n"
    "v_pk_mul_f32 v[12:13], v[12:13], v[202:203]\n"
    "v_pk_mul_f32 v[14:15], v[14:15], v[202:203]\n"
    "v_pk_mul_f32 v[24:25], v[24:25], v[202:203]\n"
    "v_pk_mul_f32 v[26:27], v[26:27], v[202:203]\n"
    "v_pk_mul_f32 v[36:37], v[36:37], v[202:203]\n"
    "v_pk_mul_f32 v[38:39], v[38:39], v[202:203]\n"
    "v_pk_mul_f32 v[48:49], v[48:49], v[202:203]\n"
    "v_pk_mul_f32 v[50:51], v[50:51], v[202:203]\n"
    "v_pk_mul_f32 v[60:61], v[60:61], v[202:203]\n"
    "v_pk_mul_f32 v[62:63], v[62:63], v[202:203]\n"
    "v_pk_mul_f32 v[72:73], v[72:73], v[202:203]\n"
    "v_pk_mul_f32 v[74:75], v[74:75], v[202:203]\n"
    "v_pk_mul_f32 v[84:85], v[84:85], v[202:203]\n"
    "v_pk_mul_f32 v[86:87], v[86:87], v[202:203]\n"
    "s_cmp_eq_u32 s50, 6\n"
    "s_cbranch_scc1 .Lback0_6\n"
    "v_exp_f32 v176, v176\n"
    "v_exp_f32 v177, v177\n"
    "v_exp_f32 v178, v178\n"
    "v_add_f32 v227, v176, v177\n"
    "v_exp_f32 v179, v179\n"
    "v_cvt_pk_bf16_f32 v200, v176, v177\n"
    "v_add_f32 v227, v227, v178\n"
    "v_exp_f32 v180, v180\n"
    "v_add_f32 v227, v227, v179\n"
    "v_exp_f32 v181, v181\n"
    "v_cvt_pk_bf16_f32 v201, v178, v179\n"
    "v_add_f32 v227, v227, v180\n"
    "v_exp_f32 v182, v182\n"
    "v_add_f32 v227, v227, v181\n"
    "v_exp_f32 v183, v183\n"
    "v_cvt_pk_bf16_f32 v202, v180, v181\n"
    "v_add_f32 v227, v227, v182\n"
    "v_add_f32 v227, v227, v183\n"
    "v_cvt_pk_bf16_f32 v203, v182, v183\n"
    "s_cmp_eq_u32 s50, 0\n"
    "s_cbranch_scc1 .Lback0_0\n"
    "s_cmp_eq_u32 s50, 1\n"
    "s_cbranch_scc1 .Lback0_1\n"
    "s_cmp_eq_u32 s50, 2\n"
    "s_cbranch_scc1 .Lback0_2\n"
    "s_cmp_eq_u32 s50, 3\n"
    "s_cbranch_scc1 .Lback0_3\n"
    "s_cmp_eq_u32 s50, 4\n"
    "s_cbranch_scc1 .Lback0_4\n"
    "s_branch .Lback0_5\n"
    ".Lslow1_0:\n"
    "s_mov_b32 s50, 0\n"
    "s_branch .Lslow1\n"
    ".Lslow1_1:\n"
    "s_mov_b32 s50, 1\n"
    "s_branch .Lslow1\n"
    ".Lslow1_2:\n"
    "s_mov_b32 s50, 2\n"
    "s_branch .Lslow1\n"
    ".Lslow1_3:\n"
    "s_mov_b32 s50, 3\n"
    "s_branch .Lslow1\n"
    ".Lslow1_4:\n"
    "s_mov_b32 s50, 4\n"
    "s_branch .Lslow1\n"
    ".Lslow1_5:\n"
    "s_mov_b32 s50, 5\n"
    "s_branch .Lslow1\n"
    ".Lslow1_6:\n"
    "s_mov_b32 s50, 6\n"
    "s_branch .Lslow1\n"
    ".Lslow1:\n"
    "s_nop 7\n"
    "v_max3_f32 v204, v184, v185, v186\n"
    "v_max3_f32 v204, v204, v187, v188\n"
    "v_max3_f32 v204, v204, v189, v190\n"
    "v_max_f32 v204, v204, v191\n"
    "v_mov_b32 v205, v204\n"
    "s_nop 1\n"
    "v_permlane16_swap_b32 v204, v205\n"
    "v_max_f32 v204, v204, v205\n"
    "v_mov_b32 v205, v204\n"
    "s_nop 1\n"
    "v_permlane32_swap_b32 v204, v205\n"
    "v_max3_f32 v204, v204, v205, 0\n"
    "v_exp_f32 v206, -v204\n"
    "v_sub_f32 v216, v216, v204\n"
    "v_sub_f32 v217, v217, v204\n"
    "v_sub_f32 v218, v218, v204\n"
    "v_sub_f32 v219, v219, v204\n"
    "v_sub_f32 v184, v184, v204\n"
    "v_sub_f32 v185, v185, v204\n"
    "v_sub_f32 v186, v186, v204\n"
    "v_sub_f32 v187, v187, v204\n"
    "v_sub_f32 v188, v188, v204\n"
    "v_sub_f32 v189, v189, v204\n"
    "v_sub_f32 v190, v190, v204\n"
    "v_sub_f32 v191, v191, v204\n"
    "v_mov_b32 v207, v206\n"
    "v_mul_f32 v225, v225, v206\n"
    "v_pk_mul_f32 v[4:5], v[4:5], v[206:207]\n"
    "v_pk_mul_f32 v[6:7], v[6:7], v[206:207]\n"
    "v_pk_mul_f32 v[16:17], v[16:17], v[206:207]\n"
    "v_pk_mul_f32 v[18:19], v[18:19], v[206:207]\n"
    "v_pk_mul_f32 v[28:29], v[28:29], v[206:207]\n"
    "v_pk_mul_f32 v[30:31], v[30:31], v[206:207]\n"
    "v_pk_mul_f32 v[40:41], v[40:41], v[206:207]\n"
    "v_pk_mul_f32 v[42:43], v[42:43], v[206:207]\n"
    "v_pk_mul_f32 v[52:53], v[52:53], v[206:207]\n"
    "v_pk_mul_f32 v[54:55], v[54:55], v[206:207]\n"
    "v_pk_mul_f32 v[64:65], v[64:65], v[206:207]\n"
    "v_pk_mul_f32 v[66:67], v[66:67], v[206:207]\n"
    "v_pk_mul_f32 v[76:77], v[76:77], v[206:207]\n"
    "v_pk_mul_f32 v[78:79], v[78:79], v[206:207]\n"
    "v_pk_mul_f32 v[88:89], v[88:89], v[206:207]\n"
    "v_pk_mul_f32 v[90:91], v[90:91], v[206:207]\n"
    "s_cmp_eq_u32 s50, 6\n"
    "s_cbranch_scc1 .Lback1_6\n"
    "v_exp_f32 v188, v188\n"
    "v_exp_f32 v189, v189\n"
    "v_exp_f32 v190, v190\n"
    "v_add_f32 v228, v188, v189\n"
    "v_exp_f32 v191, v191\n"
    "v_cvt_pk_bf16_f32 v206, v188, v189\n"
    "v_add_f32 v228, v228, v190\n"
    "v_exp_f32 v184, v184\n"
    "v_add_f32 v228, v228, v191\n"
    "v_exp_f32 v185, v185\n"
    "v_cvt_pk_bf16_f32 v207, v190, v191\n"
    "v_add_f32 v228, v228, v184\n"
    "v_exp_f32 v186, v186\n"
    "v_add_f32 v228, v228, v185\n"
    "v_exp_f32 v187, v187\n"
    "v_cvt_pk_bf16_f32 v204, v184, v185\n"
    "v_add_f32 v228, v228, v186\n"
    "v_add_f32 v228, v228, v187\n"
    "v_cvt_pk_bf16_f32 v205, v186, v187\n"
    "s_cmp_eq_u32 s50, 0\n"
    "s_cbranch_scc1 .Lback1_0\n"
    "s_cmp_eq_u32 s50, 1\n"
    "s_cbranch_scc1 .Lback1_1\n"
    "s_cmp_eq_u32 s50, 2\n"
    "s_cbranch_scc1 .Lback1_2\n"
    "s_cmp_eq_u32 s50, 3\n"
    "s_cbranch_scc1 .Lback1_3\n"
    "s_cmp_eq_u32 s50, 4\n"
    "s_cbranch_scc1 .Lback1_4\n"
    "s_branch .Lback1_5\n"
    ".Lslow2_0:\n"
    "s_mov_b32 s50, 0\n"
    "s_branch .Lslow2\n"
    ".Lslow2_1:\n"
    "s_mov_b32 s50, 1\n"
    "s_branch .Lslow2\n"
    ".Lslow2_2:\n"
    "s_mov_b32 s50, 2\n"
    "s_branch .Lslow2\n"
    ".Lslow2_3:\n"
    "s_mov_b32 s50, 3\n"
    "s_branch .Lslow2\n"
    ".Lslow2_4:\n"
    "s_mov_b32 s50, 4\n"
    "s_branch .Lslow2\n"
    ".Lslow2_5:\n"
    "s_mov_b32 s50, 5\n"
    "s_branch .Lslow2\n"
    ".Lslow2_6:\n"
    "s_mov_b32 s50, 6\n"
    "s_branch .Lslow2\n"
    ".Lslow2:\n"
    "s_nop 7\n"
    "v_max3_f32 v208, v192, v193, v194\n"
    "v_max3_f32 v208, v208, v195, v196\n"
    "v_max3_f32 v208, v208, v197, v198\n"
    "v_max_f32 v208, v208, v199\n"
    "v_mov_b32 v209, v208\n"
    "s_nop 1\n"
    "v_permlane16_swap_b32 v208, v209\n"
    "v_max_f32 v208, v208, v209\n"
    "v_mov_b32 v209, v208\n"
    "s_nop 1\n"
    "v_permlane32_swap_b32 v208, v209\n"
    "v_max3_f32 v208, v208, v209, 0\n"
    "v_exp_f32 v210, -v208\n"
    "v_sub_f32 v220, v220, v208\n"
    "v_sub_f32 v221, v221, v208\n"
    "v_sub_f32 v222, v222, v208\n"
    "v_sub_f32 v223, v223, v208\n"
    "v_sub_f32 v192, v192, v208\n"
    "v_sub_f32 v193, v193, v208\n"
    "v_sub_f32 v194, v194, v208\n"
    "v_sub_f32 v195, v195, v208\n"
    "v_sub_f32 v196, v196, v208\n"
    "v_sub_f32 v197, v197, v208\n"
    "v_sub_f32 v198, v198, v208\n"
    "v_sub_f32 v199, v199, v208\n"
    "v_mov_b32 v211, v210\n"
    "v_mul_f32 v226, v226, v210\n"
    "v_pk_mul_f32 v[8:9], v[8:9], v[210:211]\n"
    "v_pk_mul_f32 v[10:11], v[10:11], v[210:211]\n"
    "v_pk_mul_f32 v[20:21], v[20:21], v[210:211]\n"
    "v_pk_mul_f32 v[22:23], v[22:23], v[210:211]\n"
    "v_pk_mul_f32 v[32:33], v[32:33], v[210:211]\n"
    "v_pk_mul_f32 v[34:35], v[34:35], v[210:211]\n"
    "v_pk_mul_f32 v[44:45], v[44:45], v[210:211]\n"
    "v_pk_mul_f32 v[46:47], v[46:47], v[210:211]\n"
    "v_pk_mul_f32 v[56:57], v[56:57], v[210:211]\n"
    "v_pk_mul_f32 v[58:59], v[58:59], v[210:211]\n"
    "v_pk_mul_f32 v[68:69], v[68:69], v[210:211]\n"
    "v_pk_mul_f32 v[70:71], v[70:71], v[210:211]\n"
    "v_pk_mul_f32 v[80:81], v[80:81], v[210:211]\n"
    "v_pk_mul_f32 v[82:83], v[82:83], v[210:211]\n"
    "v_pk_mul_f32 v[92:93], v[92:93], v[210:211]\n"
    "v_pk_mul_f32 v[94:95], v[94:95], v[210:211]\n"
    "s_cmp_eq_u32 s50, 6\n"
    "s_cbranch_scc1 .Lback2_6\n"
    "v_exp_f32 v192, v192\n"
    "v_exp_f32 v193, v193\n"
    "v_exp_f32 v194, v194\n"
    "v_add_f32 v229, v192, v193\n"
    "v_exp_f32 v195, v195\n"
    "v_cvt_pk_bf16_f32 v208, v192, v193\n"
    "v_add_f32 v229, v229, v194\n"
    "v_exp_f32 v196, v196\n"
    "v_add_f32 v229, v229, v195\n"
    "v_exp_f32 v197, v197\n"
    "v_cvt_pk_bf16_f32 v209, v194, v195\n"
    "v_add_f32 v229, v229, v196\n"
    "v_exp_f32 v198, v198\n"
    "v_add_f32 v229, v229, v197\n"
    "v_exp_f32 v199, v199\n"
    "v_cvt_pk_bf16_f32 v210, v196, v197\n"
    "v_add_f32 v229, v229, v198\n"
    "v_add_f32 v229, v229, v199\n"
    "v_cvt_pk_bf16_f32 v211, v198, v199\n"
    "s_cmp_eq_u32 s50, 0\n"
    "s_cbranch_scc1 .Lback2_0\n"
    "s_cmp_eq_u32 s50, 1\n"
    "s_cbranch_scc1 .Lback2_1\n"
    "s_cmp_eq_u32 s50, 2\n"
    "s_cbranch_scc1 .Lback2_2\n"
    "s_cmp_eq_u32 s50, 3\n"
    "s_cbranch_scc1 .Lback2_3\n"
    "s_cmp_eq_u32 s50, 4\n"
    "s_cbranch_scc1 .Lback2_4\n"
    "s_branch .Lback2_5\n"
    ".Lend:\n"
    "s_endpgm\n"
    :: "s"(ka), "s"(wg), "v"(tid)
    : "memory", "vcc", "scc",
      "v1", "v2", "v3", "v4", "v5", "v6", "v7", "v8", "v9", "v10", "v11", "v12", "v13", "v14", "v15", "v16",
      "v17", "v18", "v19", "v20", "v21", "v22", "v23", "v24", "v25", "v26", "v27", "v28", "v29", "v30", "v31", "v32",
      "v33", "v34", "v35", "v36", "v37", "v38", "v39", "v40", "v41", "v42", "v43", "v44", "v45", "v46", "v47", "v48",
      "v49", "v50", "v51", "v52", "v53", "v54", "v55", "v56", "v57", "v58", "v59", "v60", "v61", "v62", "v63", "v64",
      "v65", "v66", "v67", "v68", "v69", "v70", "v71", "v72", "v73", "v74", "v75", "v76", "v77", "v78", "v79", "v80",
      "v81", "v82", "v83", "v84", "v85", "v86", "v87", "v88", "v89", "v90", "v91", "v92", "v93", "v94", "v95", "v96",
      "v97", "v98", "v99", "v100", "v101", "v102", "v103", "v104", "v105", "v106", "v107", "v108", "v109", "v110", "v111", "v112",
      "v113", "v114", "v115", "v116", "v117", "v118", "v119", "v120", "v121", "v122", "v123", "v124", "v125", "v126", "v127", "v128",
      "v129", "v130", "v131", "v132", "v133", "v134", "v135", "v136", "v137", "v138", "v139", "v140", "v141", "v142", "v143", "v144",
      "v145", "v146", "v147", "v148", "v149", "v150", "v151", "v152", "v153", "v154", "v155", "v156", "v157", "v158", "v159", "v160",
      "v161", "v162", "v163", "v164", "v165", "v166", "v167", "v168", "v169", "v170", "v171", "v172", "v173", "v174", "v175", "v176",
      "v177", "v178", "v179", "v180", "v181", "v182", "v183", "v184", "v185", "v186", "v187", "v188", "v189", "v190", "v191", "v192",
      "v193", "v194", "v195", "v196", "v197", "v198", "v199", "v200", "v201", "v202", "v203", "v204", "v205", "v206", "v207", "v208",
      "v209", "v210", "v211", "v212", "v213", "v214", "v215", "v216", "v217", "v218", "v219", "v220", "v221", "v222", "v223", "v224",
      "v225", "v226", "v227", "v228", "v229", "v230", "v231", "v232", "v233", "v234", "v235", "v236", "v237", "v238", "v239", "v240",
      "v241", "v242", "v243", "v244", "v245", "v246", "v247", "v248", "v249", "v250", "v251", "v252", "v253", "v254", "v255", "s4",
      "s5", "s6", "s7", "s8", "s9", "s10", "s11", "s12", "s13", "s14", "s15", "s16", "s17", "s18", "s19", "s20",
      "s21", "s22", "s23", "s24", "s25", "s26", "s27", "s28", "s29", "s30", "s31", "s33", "s34", "s35", "s36", "s37",
      "s38", "s39", "s40", "s41", "s42", "s43", "s44", "s45", "s46", "s47", "s48", "s49", "s50", "s51", "s52", "s53",
      "s54", "s55", "s56", "s57", "s58", "s59", "s60", "s61", "s62", "s63", "s64", "s65", "s66", "s67", "s68", "s69",
      "s70", "s71", "s72", "s73", "s74", "s75", "s76", "s77", "s78", "s79", "s80", "s81", "s82", "s83", "s84", "s85",
      "s86", "s87", "s88", "s89", "s90", "s91", "s92", "s93", "s94", "s95");
}

extern "C" void kernel_launch(void* const* d_in, const int* in_sizes, int n_in, void* d_out, int out_size, void* d_ws, size_t ws_size,
                              hipStream_t stream) {
  (void)in_sizes; (void)n_in; (void)out_size; (void)ws_size;
  fused_attn<<<256, 512, 0, stream>>>((const char*)d_in[0], (const char*)d_in[1], (const char*)d_in[2], (const char*)d_in[3],
      (const char*)d_in[4], (const char*)d_in[5], (const char*)d_in[6], (const char*)d_in[7], (const char*)d_in[8], (const char*)d_in[9],
      (char*)d_ws, (char*)d_out);
}
```
